```python
import torch

def _softmax(x, axis=-1):
    return torch.softmax(x, dim=axis)

def _gelu(x):
    return torch.nn.functional.gelu(x, approximate='tanh')

def _silu(x):
    return torch.nn.functional.silu(x)

def _sigmoid(x):
    return torch.sigmoid(x)

def _layernorm(x, axis=-1, eps=1e-05):
    m = x.mean(dim=axis, keepdim=True)
    v = x.var(dim=axis, keepdim=True, unbiased=False)
    return (x - m) / torch.sqrt(v + eps)

def _scatter_add(tgt, idx, src, axis=0):
    return tgt.clone().index_add_(axis, idx, src)

def _segment_sum(src, seg, n):
    out = torch.zeros((n,) + src.shape[1:], dtype=src.dtype)
    return out.index_add_(0, seg, src)

def _pad(x, pw):
    flat = tuple((v for pair in reversed(pw) for v in pair))
    return torch.nn.functional.pad(x, flat)
kernel_name = 'adaLN_cond__spectral_fnet__geglu_ffn__prompt'

def setup_inputs():

    _w = lambda a: (lambda t: t.to(torch.bfloat16) if t.is_floating_point() else t)(torch.as_tensor(a))

    def _A_setup():
        B, S, D = (1, 2048, 512)
        ns = torch.arange(S)[:, None] * torch.arange(S)[None, :]
        nd = torch.arange(D)[:, None] * torch.arange(D)[None, :]
        Ws_re = torch.cos(-2.0 * torch.pi * ns / S).to(float)
        Ws_im = torch.sin(-2.0 * torch.pi * ns / S).to(float)
        Wd_re = torch.cos(-2.0 * torch.pi * nd / D).to(float)
        Wd_im = torch.sin(-2.0 * torch.pi * nd / D).to(float)
        return {'x': _w((lambda *s: torch.randn(*s, dtype=torch.bfloat16) * 0.05)(B, S, D)), 'dft_seq_re': _w(Ws_re), 'dft_seq_im': _w(Ws_im), 'dft_hid_re': _w(Wd_re), 'dft_hid_im': _w(Wd_im)}
    _A_in = _A_setup()

    def _B_setup():
        B, S, D, F = (1, 2048, 512, 1792)
        return {'x': _w((lambda *s: torch.randn(*s, dtype=torch.bfloat16) * 0.05)(B, S, D)), 'w_gate': _w((lambda *s: torch.randn(*s, dtype=torch.bfloat16) * 0.4)(D, F)), 'w_up': _w((lambda *s: torch.randn(*s, dtype=torch.bfloat16) * 0.4)(D, F)), 'w_down': _w((lambda *s: torch.randn(*s, dtype=torch.bfloat16) * 0.4)(F, D))}
    _B_in = _B_setup()
    return {'A_x': _A_in['x'], 'A_dft_seq_re': _A_in['dft_seq_re'], 'A_dft_seq_im': _A_in['dft_seq_im'], 'A_dft_hid_re': _A_in['dft_hid_re'], 'A_dft_hid_im': _A_in['dft_hid_im'], 'B_x': _B_in['x'], 'B_w_gate': _B_in['w_gate'], 'B_w_up': _B_in['w_up'], 'B_w_down': _B_in['w_down'], 'c': _w((lambda *s: torch.randn(*s, dtype=torch.bfloat16) * 0.2)(1, 512) * 8.0), 'w_mod': _w((lambda *s: torch.randn(*s, dtype=torch.bfloat16) * 0.2)(512, 6 * 512) * 512 ** (-0.5) * 8.0)}

def reference(A_x, A_dft_seq_re, A_dft_seq_im, A_dft_hid_re, A_dft_hid_im, B_x, B_w_gate, B_w_up, B_w_down, c, w_mod):

    def _A_ref(x, dft_seq_re, dft_seq_im, dft_hid_re, dft_hid_im):
        a = torch.einsum('bsd,ed->bse', x, dft_hid_re)
        b = torch.einsum('bsd,ed->bse', x, dft_hid_im)
        y = torch.einsum('ts,bse->bte', dft_seq_re, a) - torch.einsum('ts,bse->bte', dft_seq_im, b)
        return y

    def _B_ref(x, w_gate, w_up, w_down):
        return _gelu(x @ w_gate) * (x @ w_up) @ w_down
    x = A_x
    mod = (_silu(c) @ w_mod).reshape(c.shape[0], 6, 1, -1)
    sa, ca, ga, sb, cb, gb = (mod[:, 0].clone(), mod[:, 1].clone(), mod[:, 2].clone(), mod[:, 3].clone(), mod[:, 4].clone(), mod[:, 5].clone())
    h = _layernorm(x, axis=-1) * (1 + ca) + sa
    x = x + ga * _A_ref(h, A_dft_seq_re, A_dft_seq_im, A_dft_hid_re, A_dft_hid_im)
    h = _layernorm(x, axis=-1) * (1 + cb) + sb
    x = x + gb * _B_ref(h, B_w_gate, B_w_up, B_w_down)
    return x
```

```cpp
#include <hip/hip_runtime.h>
#include <hip/hip_bf16.h>
typedef unsigned short u16;
__device__ __forceinline__ float b2f(u16 v){ return __uint_as_float(((unsigned)v)<<16); }
__device__ __forceinline__ u16 f2b(float f){ unsigned u=__float_as_uint(f); return (u16)((u + 0x7FFF + ((u>>16)&1))>>16); }
constexpr int S=2048, D=512, F=1792;

__global__ void k_mod(const u16* c, const u16* wmod, float* mod){
  int j = blockIdx.x*blockDim.x+threadIdx.x; if(j>=6*D) return;
  float acc=0;
  for(int k=0;k<D;k++){ float cv=b2f(c[k]); float s = cv/(1.f+expf(-cv)); acc += s*b2f(wmod[k*6*D+j]); }
  mod[j]=acc;
}
template<bool BF>
__global__ void k_ln(const void* xin, const float* shift, const float* scale, float* h){
  int row=blockIdx.x; int t=threadIdx.x;
  float v = BF ? b2f(((const u16*)xin)[row*D+t]) : ((const float*)xin)[row*D+t];
  __shared__ float sm[512];
  sm[t]=v; __syncthreads();
  float m=0; for(int i=0;i<D;i++) m+=sm[i]; m/=D;
  float var=0; for(int i=0;i<D;i++){ float d=sm[i]-m; var+=d*d;} var/=D;
  h[row*D+t]=(v-m)/sqrtf(var+1e-5f)*(1.f+scale[t])+shift[t];
}
__global__ void k_ab(const float* h, const u16* Wre, const u16* Wim, float* a, float* b){
  int s=blockIdx.x, e=threadIdx.x;
  __shared__ float hs[512]; hs[e]=h[s*D+e]; __syncthreads();
  float x=0,y=0;
  for(int d=0;d<D;d++){ x+=hs[d]*b2f(Wre[e*D+d]); y+=hs[d]*b2f(Wim[e*D+d]); }
  a[s*D+e]=x; b[s*D+e]=y;
}
__global__ void k_y(const u16* Sre, const u16* Sim, const float* a, const float* b, const u16* x, const float* ga, float* x1){
  int t=blockIdx.x, e=threadIdx.x;
  float p=0,q=0;
  for(int s=0;s<S;s++){ p+=b2f(Sre[t*S+s])*a[s*D+e]; q+=b2f(Sim[t*S+s])*b[s*D+e]; }
  x1[t*D+e]=b2f(x[t*D+e])+ga[e]*(p-q);
}
__global__ void k_gu(const float* h, const u16* wg, const u16* wu, float* act){
  int t=blockIdx.x; int f=blockIdx.y*256+threadIdx.x;
  float g=0,u=0;
  for(int d=0;d<D;d++){ float hv=h[t*D+d]; g+=hv*b2f(wg[d*F+f]); u+=hv*b2f(wu[d*F+f]); }
  float ge=0.5f*g*(1.f+tanhf(0.7978845608028654f*(g+0.044715f*g*g*g)));
  act[t*F+f]=ge*u;
}
__global__ void k_down(const float* act, const u16* wd, const float* x1, const float* gb, u16* out){
  int t=blockIdx.x, e=threadIdx.x;
  float p=0;
  for(int f=0;f<F;f++) p+=act[t*F+f]*b2f(wd[f*D+e]);
  out[t*D+e]=f2b(x1[t*D+e]+gb[e]*p);
}
extern "C" void kernel_launch(void* const* d_in, const int* in_sizes, int n_in,
                              void* d_out, int out_size, void* d_ws, size_t ws_size,
                              hipStream_t stream) {
  const u16* x=(const u16*)d_in[0]; const u16* Sre=(const u16*)d_in[1]; const u16* Sim=(const u16*)d_in[2];
  const u16* Dre=(const u16*)d_in[3]; const u16* Dim=(const u16*)d_in[4];
  const u16* wg=(const u16*)d_in[6]; const u16* wu=(const u16*)d_in[7]; const u16* wd=(const u16*)d_in[8];
  const u16* c=(const u16*)d_in[9]; const u16* wmod=(const u16*)d_in[10];
  float* ws=(float*)d_ws;
  float* mod=ws; float* h=ws+4096; float* a=h+S*D; float* b=a+S*D; float* x1=b+S*D; float* h2=x1+S*D; float* act=h2+S*D;
  k_mod<<<12,256,0,stream>>>(c,wmod,mod);
  k_ln<true><<<S,512,0,stream>>>(x,mod,mod+D,h);
  k_ab<<<S,512,0,stream>>>(h,Dre,Dim,a,b);
  k_y<<<S,512,0,stream>>>(Sre,Sim,a,b,x,mod+2*D,x1);
  k_ln<false><<<S,512,0,stream>>>(x1,mod+3*D,mod+4*D,h2);
  k_gu<<<dim3(S,7),256,0,stream>>>(h2,wg,wu,act);
  k_down<<<S,512,0,stream>>>(act,wd,x1,mod+5*D,(u16*)d_out);
}
```

```cpp
#include <hip/hip_runtime.h>
#include <hip/hip_bf16.h>
typedef unsigned short u16;
__device__ __forceinline__ float b2f(u16 v){ return __uint_as_float(((unsigned)v)<<16); }
__device__ __forceinline__ u16 f2b(float f){ unsigned u=__float_as_uint(f); return (u16)((u + 0x7FFF + ((u>>16)&1))>>16); }
constexpr int S=2048, D=512, F=1792;

__global__ void k_mod(const u16* c, const u16* wmod, float* mod){
  int j = blockIdx.x*blockDim.x+threadIdx.x; if(j>=6*D) return;
  float acc=0;
  for(int k=0;k<D;k++){ float cv=b2f(c[k]); float s = cv/(1.f+expf(-cv)); acc += s*b2f(wmod[k*6*D+j]); }
  mod[j]=acc;
}
template<bool BF>
__global__ void k_ln(const void* xin, const float* shift, const float* scale, float* h){
  int row=blockIdx.x; int t=threadIdx.x;
  float v = BF ? b2f(((const u16*)xin)[row*D+t]) : ((const float*)xin)[row*D+t];
  __shared__ float sm[512];
  sm[t]=v; __syncthreads();
  float m=0; for(int i=0;i<D;i++) m+=sm[i]; m/=D;
  float var=0; for(int i=0;i<D;i++){ float d=sm[i]-m; var+=d*d;} var/=D;
  h[row*D+t]=(v-m)/sqrtf(var+1e-5f)*(1.f+scale[t])+shift[t];
}
__global__ void k_ab(const float* h, const u16* Wre, const u16* Wim, float* a, float* b){
  int s=blockIdx.x, e=threadIdx.x;
  __shared__ float hs[512]; hs[e]=h[s*D+e]; __syncthreads();
  float x=0,y=0;
  for(int d=0;d<D;d++){ x+=hs[d]*b2f(Wre[e*D+d]); y+=hs[d]*b2f(Wim[e*D+d]); }
  a[s*D+e]=x; b[s*D+e]=y;
}
__global__ void k_y(const u16* Sre, const u16* Sim, const float* a, const float* b, const u16* x, const float* ga, float* x1){
  int t=blockIdx.x, e=threadIdx.x;
  float p=0,q=0;
  for(int s=0;s<S;s++){ p+=b2f(Sre[t*S+s])*a[s*D+e]; q+=b2f(Sim[t*S+s])*b[s*D+e]; }
  x1[t*D+e]=b2f(x[t*D+e])+ga[e]*(p-q);
}
__global__ void k_gu(const float* h, const u16* wg, const u16* wu, float* act){
  int t=blockIdx.x; int f=blockIdx.y*256+threadIdx.x;
  float g=0,u=0;
  for(int d=0;d<D;d++){ float hv=h[t*D+d]; g+=hv*b2f(wg[d*F+f]); u+=hv*b2f(wu[d*F+f]); }
  float ge=0.5f*g*(1.f+tanhf(0.7978845608028654f*(g+0.044715f*g*g*g)));
  act[t*F+f]=ge*u;
}
__global__ void k_down(const float* act, const u16* wd, const float* x1, const float* gb, u16* out){
  int t=blockIdx.x, e=threadIdx.x;
  float p=0;
  for(int f=0;f<F;f++) p+=act[t*F+f]*b2f(wd[f*D+e]);
  out[t*D+e]=f2b(x1[t*D+e]+gb[e]*p);
}
#define PCH 2048

typedef float f4 __attribute__((ext_vector_type(4)));
__global__ void k_probe(const u16* A, const u16* B, unsigned* res){
  unsigned long long t0 = __builtin_amdgcn_s_memrealtime();
  int bid=blockIdx.x; int tid=threadIdx.x;
  const u16* base = (bid<128? A + (size_t)bid*PCH : B + (size_t)(bid-128)*PCH);
  f4 r[PCH/2048];
  #pragma unroll
  for(int i=0;i<PCH/2048;i++) r[i]=*(const f4*)(base + i*2048 + tid*8);
  #pragma unroll
  for(int i=0;i<PCH/2048;i++) asm volatile(""::"v"(r[i]));
  __syncthreads();
  unsigned long long t1 = __builtin_amdgcn_s_memrealtime();
  if(tid==0) atomicAdd(res, (unsigned)(t1-t0));
}
__global__ void k_spin(unsigned* res){
  unsigned long long t0 = __builtin_amdgcn_s_memrealtime();
  unsigned long long d = (unsigned long long)res[0]*100/256;
  while(__builtin_amdgcn_s_memrealtime()-t0 < d) __builtin_amdgcn_s_sleep(10);
}
extern "C" void kernel_launch(void* const* d_in, const int* in_sizes, int n_in,
                              void* d_out, int out_size, void* d_ws, size_t ws_size,
                              hipStream_t stream) {
  const u16* x=(const u16*)d_in[0]; const u16* Sre=(const u16*)d_in[1]; const u16* Sim=(const u16*)d_in[2];
  const u16* Dre=(const u16*)d_in[3]; const u16* Dim=(const u16*)d_in[4];
  const u16* wg=(const u16*)d_in[6]; const u16* wu=(const u16*)d_in[7]; const u16* wd=(const u16*)d_in[8];
  const u16* c=(const u16*)d_in[9]; const u16* wmod=(const u16*)d_in[10];
  float* ws=(float*)d_ws;
  float* mod=ws; float* h=ws+4096; float* a=h+S*D; float* b=a+S*D; float* x1=b+S*D; float* h2=x1+S*D; float* act=h2+S*D;
  k_mod<<<12,256,0,stream>>>(c,wmod,mod);
  k_ln<true><<<S,512,0,stream>>>(x,mod,mod+D,h);
  k_ab<<<S,512,0,stream>>>(h,Dre,Dim,a,b);
  k_y<<<S,512,0,stream>>>(Sre,Sim,a,b,x,mod+2*D,x1);
  k_ln<false><<<S,512,0,stream>>>(x1,mod+3*D,mod+4*D,h2);
  k_gu<<<dim3(S,7),256,0,stream>>>(h2,wg,wu,act);
  k_down<<<S,512,0,stream>>>(act,wd,x1,mod+5*D,(u16*)d_out);
}
```

```cpp
#include <hip/hip_runtime.h>
#include <utility>
#pragma clang diagnostic ignored "-Winline-asm"

typedef unsigned short u16;
typedef unsigned u32;
typedef unsigned long long u64;
typedef float f32x2 __attribute__((ext_vector_type(2)));
typedef float f32x4 __attribute__((ext_vector_type(4)));
typedef float f32x16 __attribute__((ext_vector_type(16)));
typedef unsigned u32x2 __attribute__((ext_vector_type(2)));
typedef unsigned u32x4 __attribute__((ext_vector_type(4)));
typedef short s16x4 __attribute__((ext_vector_type(4)));
typedef short s16x8 __attribute__((ext_vector_type(8)));
typedef __bf16 bf16x2 __attribute__((ext_vector_type(2)));
typedef __bf16 bf16x8 __attribute__((ext_vector_type(8)));
typedef __amdgpu_buffer_rsrc_t rsrc_t;
#define LAS __attribute__((address_space(3)))
#define GAS __attribute__((address_space(1)))
#define DI __device__ __forceinline__
#define RLX_AGENT __ATOMIC_RELAXED, __HIP_MEMORY_SCOPE_AGENT

constexpr int S = 2048, D = 512, F = 1792;
constexpr u32 WS_XN = 0;
constexpr u32 WS_PQ = WS_XN + (2u << 20);
constexpr u32 WS_X1 = WS_PQ + (8u << 20);
constexpr u32 WS_ACT = WS_X1 + (2u << 20);
constexpr u32 WS_RS = WS_ACT + (7u << 20);
constexpr u32 WS_ST = WS_RS + (256u << 10);
constexpr u32 WS_MODG = WS_ST + (256u << 10);
constexpr u32 WS_VG = WS_MODG + 4 * 3072 * 8;
constexpr u32 WS_UVG = WS_VG + 1024 * 8;
constexpr u32 WS_F1 = WS_UVG + 4 * 2 * 3584 * 8;
constexpr u32 WS_F2 = WS_F1 + 8 * 256 * 128;
constexpr u32 WS_F3 = WS_F2 + 256 * 128;
constexpr u32 WS_F4A = WS_F3 + 256 * 128;
constexpr u32 WS_F4B = WS_F4A + 256 * 128;
constexpr u32 WS_GEN = WS_F4B + 256 * 128;
constexpr u32 WS_TS = WS_GEN + 256;
constexpr u32 WS_TS2 = WS_TS + 256 * 16 * 8;
constexpr u32 WS_END = WS_TS2 + 256 * 256 * 4;
constexpr u32 L_EX = 128 * 1024;
constexpr u32 L_MODV = 144 * 1024;
constexpr u32 L_UV = L_MODV + 4096;
constexpr u32 L_UVP = L_UV + 4096;
constexpr u32 L_MP = L_UVP + 2048;
constexpr u32 L_RS = L_MP + 1024;
constexpr u32 L_CNT = L_RS + 2048;
constexpr u32 LDS_BYTES = 160 * 1024;
constexpr u32 SPIN_CAP = 200000;
#define POLL_SLEEP 2

template <class Fn, int... Is> DI void sfor_impl(Fn&& f, std::integer_sequence<int, Is...>) { (f(std::integral_constant<int, Is>{}), ...); }
template <int V> using ic = std::integral_constant<int, V>;
template <int N, class Fn> DI void sfor(Fn&& f) { sfor_impl(f, std::make_integer_sequence<int, N>{}); }

DI rsrc_t mk_rsrc(const void* p, u32 bytes) { return __builtin_amdgcn_make_buffer_rsrc((void*)p, 0, bytes, 0x00020000); }
DI float b_lo(u32 v) { return __uint_as_float(v << 16); }
DI float b_hi(u32 v) { return __uint_as_float(v & 0xffff0000u); }
DI u32 pack2(float a, float b) { f32x2 p = {a, b}; bf16x2 r = __builtin_convertvector(p, bf16x2); return __builtin_bit_cast(u32, r); }

template <int IMM> DI void ld_a(u32x4& d, u32 voff, rsrc_t r, u32 soff) {
  asm volatile("buffer_load_dwordx4 %0, %1, %2, %3 offen offset:%c4 sc1" : "=a"(d) : "v"(voff), "s"(r), "s"(soff), "i"(IMM));
}
template <int IMM> DI void ld_v4(u32x4& d, u32 voff, rsrc_t r, u32 soff) {
  asm volatile("buffer_load_dwordx4 %0, %1, %2, %3 offen offset:%c4 sc1" : "=v"(d) : "v"(voff), "s"(r), "s"(soff), "i"(IMM));
}
template <int IMM> DI void ld_v4p(u32x4& d, u32 voff, rsrc_t r, u32 soff) {
  asm volatile("buffer_load_dwordx4 %0, %1, %2, %3 offen offset:%c4" : "=v"(d) : "v"(voff), "s"(r), "s"(soff), "i"(IMM));
}
template <int IMM> DI void ld_v2(u32x2& d, u32 voff, rsrc_t r, u32 soff) {
  asm volatile("buffer_load_dwordx2 %0, %1, %2, %3 offen offset:%c4 sc1" : "=v"(d) : "v"(voff), "s"(r), "s"(soff), "i"(IMM));
}
template <int IMM> DI void ld_v2p(u32x2& d, u32 voff, rsrc_t r, u32 soff) {
  asm volatile("buffer_load_dwordx2 %0, %1, %2, %3 offen offset:%c4" : "=v"(d) : "v"(voff), "s"(r), "s"(soff), "i"(IMM));
}
template <int IMM> DI void ld_v1(u32& d, u32 voff, rsrc_t r, u32 soff) {
  asm volatile("buffer_load_dword %0, %1, %2, %3 offen offset:%c4 sc1" : "=v"(d) : "v"(voff), "s"(r), "s"(soff), "i"(IMM));
}
template <int IMM> DI void ld_u16p(u32& d, u32 voff, rsrc_t r, u32 soff) {
  asm volatile("buffer_load_ushort %0, %1, %2, %3 offen offset:%c4" : "=v"(d) : "v"(voff), "s"(r), "s"(soff), "i"(IMM));
}
template <int IMM> DI void set_m0(u32 base) { asm volatile("s_add_u32 m0, %0, %c1" :: "s"(base), "i"(IMM) : "m0", "scc"); }
template <int IMM> DI void set_m0n(u32 base) { asm volatile("s_add_u32 m0, %0, %c1\n\ts_nop 0" :: "s"(base), "i"(IMM) : "m0", "scc"); }
template <int ID, int OFF> DI void dma(u32 voff, rsrc_t r, u32 soff) {
  asm volatile("buffer_load_dwordx4 %0, %1, %2 offen offset:%c3 lds ; ID %c4" :: "v"(voff), "s"(r), "s"(soff), "i"(OFF), "i"(ID));
}
template <int ID, int OFF = 0> DI void pub16(u32x4 v, rsrc_t r, u32 voff, u32 soff) {
  asm volatile("buffer_store_dwordx4 %0, %1, %2, %3 offen offset:%c5 sc1 ; ID %c4\n\ts_nop 1" :: "v"(v), "v"(voff), "s"(r), "s"(soff), "i"(ID), "i"(OFF));
}
template <int ID> DI void pub8(u32x2 v, rsrc_t r, u32 voff, u32 soff) {
  asm volatile("buffer_store_dwordx2 %0, %1, %2, %3 offen sc1 ; ID %c4" :: "v"(v), "v"(voff), "s"(r), "s"(soff), "i"(ID));
}
template <int ID> DI void pub4(u32 v, rsrc_t r, u32 voff, u32 soff) {
  asm volatile("buffer_store_dword %0, %1, %2, %3 offen sc1 ; ID %c4" :: "v"(v), "v"(voff), "s"(r), "s"(soff), "i"(ID));
}
template <int N, int A = 0> DI void wait_vm() { static_assert(N >= 0 && N < 64, ""); asm volatile("s_waitcnt vmcnt(%c0) ; NEED %c1" :: "i"(N), "i"(A) : "memory"); }
template <int N> DI void wait_a(u32x4& a) { static_assert(N >= 0, ""); asm volatile("s_waitcnt vmcnt(%c1)" : "+a"(a) : "i"(N < 63 ? N : 63)); }
DI void tie_a(u32x4& a) { asm volatile("" : "+a"(a)); }
template <class T> DI void tie_v(T& a) { asm volatile("" : "+v"(a)); }
template <int N, int A = 0, int B = 0> DI void bar_vm() {
  static_assert(N >= 0, "");
  asm volatile("s_waitcnt vmcnt(%c0) lgkmcnt(0) ; NEED %c1 NEED %c2\n\ts_barrier" :: "i"(N < 63 ? N : 63), "i"(A), "i"(B) : "memory");
}
DI void bar() { asm volatile("s_waitcnt lgkmcnt(0)\n\ts_barrier" ::: "memory"); }
#define SCHED() __builtin_amdgcn_sched_barrier(0)

DI void st16(u32x4 v, rsrc_t r, u32 voff, u32 soff) { __builtin_amdgcn_raw_buffer_store_b128(v, r, voff, soff, 16); }
DI void st8(u32x2 v, rsrc_t r, u32 voff, u32 soff) { __builtin_amdgcn_raw_buffer_store_b64(v, r, voff, soff, 16); }
DI void st4(u32 v, rsrc_t r, u32 voff, u32 soff) { __builtin_amdgcn_raw_buffer_store_b32(v, r, voff, soff, 16); }

DI f32x16 mfma(u32x4 a, u32x4 b, f32x16 c) {
  return __builtin_amdgcn_mfma_f32_32x32x16_bf16(__builtin_bit_cast(bf16x8, a), __builtin_bit_cast(bf16x8, b), c, 0, 0, 0);
}
template <bool Z> DI void mm_va(f32x16& c, u32x4 a, u32x4 b) {
  if constexpr (Z) asm volatile("v_mfma_f32_32x32x16_bf16 %0, %1, %2, 0" : "=&v"(c) : "v"(a), "a"(b));
  else asm volatile("v_mfma_f32_32x32x16_bf16 %0, %1, %2, %0" : "+v"(c) : "v"(a), "a"(b));
}
template <bool Z> DI void mm_av(f32x16& c, u32x4 a, u32x4 b) {
  if constexpr (Z) asm volatile("v_mfma_f32_32x32x16_bf16 %0, %1, %2, 0" : "=&v"(c) : "a"(a), "v"(b));
  else asm volatile("v_mfma_f32_32x32x16_bf16 %0, %1, %2, %0" : "+v"(c) : "a"(a), "v"(b));
}
template <bool Z> DI void mm_vv(f32x16& c, u32x4 a, u32x4 b) {
  if constexpr (Z) asm volatile("v_mfma_f32_32x32x16_bf16 %0, %1, %2, 0" : "=&v"(c) : "v"(a), "v"(b));
  else asm volatile("v_mfma_f32_32x32x16_bf16 %0, %1, %2, %0" : "+v"(c) : "v"(a), "v"(b));
}
template <bool Z> DI void m16_va(f32x4& c, u32x4 a, u32x4 b) {
  if constexpr (Z) asm volatile("v_mfma_f32_16x16x32_bf16 %0, %1, %2, 0" : "=&v"(c) : "v"(a), "a"(b));
  else asm volatile("v_mfma_f32_16x16x32_bf16 %0, %1, %2, %0" : "+v"(c) : "v"(a), "a"(b));
}
DI void m16_fence(f32x4 (&y)[4]) { asm volatile("s_nop 11" : "+v"(y[0]), "+v"(y[1]), "+v"(y[2]), "+v"(y[3])); }
DI void mm_fence(f32x16& a, f32x16& b) { asm volatile("s_nop 15" : "+v"(a), "+v"(b)); }
DI void mm_tie(f32x16& a, f32x16& b) { asm volatile("" : "+v"(a), "+v"(b)); }
DI float smul(float a, float b) { float r; asm("v_mul_f32_e32 %0, %1, %2" : "=v"(r) : "v"(a), "v"(b)); return r; }
DI float sfma(float a, float b, float c) { float r; asm("v_fma_f32 %0, %1, %2, %3" : "=v"(r) : "v"(a), "v"(b), "v"(c)); return r; }
DI u32x4 lds16(LAS char* sm, u32 off) { return *(LAS u32x4*)(sm + off); }
DI f32x4 ldsf4(LAS char* sm, u32 off) { return *(LAS f32x4*)(sm + off); }
DI u32x4 lds_tr(LAS char* sm, u32 off0, u32 off1) {
  s16x4 lo = __builtin_amdgcn_ds_read_tr16_b64_v4i16((LAS s16x4*)(sm + off0));
  s16x4 hi = __builtin_amdgcn_ds_read_tr16_b64_v4i16((LAS s16x4*)(sm + off1));
  s16x8 w = __builtin_shufflevector(lo, hi, 0, 1, 2, 3, 4, 5, 6, 7);
  return __builtin_bit_cast(u32x4, w);
}
template <int CTRL> DI float dpp(float v) { return __int_as_float(__builtin_amdgcn_update_dpp(0, __float_as_int(v), CTRL, 0xF, 0xF, true)); }
DI float add_x16(float v) { auto r = __builtin_amdgcn_permlane16_swap(__float_as_uint(v), __float_as_uint(v), false, false); u32 a = r[0], b = r[1]; return __uint_as_float(a) + __uint_as_float(b); }
DI float add_x32(float v) { auto r = __builtin_amdgcn_permlane32_swap(__float_as_uint(v), __float_as_uint(v), false, false); u32 a = r[0], b = r[1]; return __uint_as_float(a) + __uint_as_float(b); }
DI float red_x32(float a, float b) { auto r = __builtin_amdgcn_permlane32_swap(__float_as_uint(a), __float_as_uint(b), false, false); u32 p = r[0], q = r[1]; return __uint_as_float(p) + __uint_as_float(q); }
DI float red_x16(float a, float b) { auto r = __builtin_amdgcn_permlane16_swap(__float_as_uint(a), __float_as_uint(b), false, false); u32 p = r[0], q = r[1]; return __uint_as_float(p) + __uint_as_float(q); }
DI float red_x8(float a, float b, bool hi) { float keep = hi ? b : a, give = hi ? a : b; return keep + dpp<0x128>(give); }
DI float wave_sum(float v) {
  v += dpp<0xB1>(v); v += dpp<0x4E>(v); v += dpp<0x141>(v); v += dpp<0x140>(v);
  return add_x32(add_x16(v));
}
DI u32x4 chunk_swap(u32 a0, u32 a1, u32 b0, u32 b1) {
  auto r0 = __builtin_amdgcn_permlane32_swap(a0, b0, false, false);
  auto r1 = __builtin_amdgcn_permlane32_swap(a1, b1, false, false);
  u32x4 o; o[0] = r0[0]; o[1] = r1[0]; o[2] = r0[1]; o[3] = r1[1];
  return o;
}
DI u32x4 chunk_swap16(u32 a0, u32 a1, u32 b0, u32 b1) {
  auto r0 = __builtin_amdgcn_permlane16_swap(a0, b0, false, false);
  auto r1 = __builtin_amdgcn_permlane16_swap(a1, b1, false, false);
  u32x4 o; o[0] = r0[0]; o[1] = r1[0]; o[2] = r0[1]; o[3] = r1[1];
  return o;
}
template <int NF> DI void poll_flags(const char* p, u64 tag, int lane) {
  const GAS u64* q = (const GAS u64*)p + (lane < NF ? lane : 0) * 16;
  for (u32 sp = 0; sp < SPIN_CAP; ++sp) {
    u64 v = __hip_atomic_load(q, RLX_AGENT);
    if (__all(v == tag)) break;
    __builtin_amdgcn_s_sleep(POLL_SLEEP);
  }
  asm volatile("" ::: "memory");
}
DI void post_flag(char* p, u64 tag) { __hip_atomic_store((GAS u64*)p, tag, RLX_AGENT); }

#define STAMP(i) do {} while (0)
#define FS(i) do {} while (0)

#define NINIT 6
#define NW1 20
#define ND1 4
constexpr int P1_MODC = 1, P1_MODLD = 8, P1_MODUSE = 13, P1_V = 14, P1_XR = 10;
constexpr int p1_extra(int k) { return (k == P1_MODLD || k == P1_MODLD + 1) ? 4 : k == P1_XR ? 2 : 0; }
struct Sched1 { int posx[16][4], posa[24], pose, posm, top[16][4], bar[16], ext[16], pre; };
constexpr Sched1 mk_sched1() {
  Sched1 s{}; int n = 2;
  for (int i = 0; i < NINIT; ++i) s.posa[i] = -1;
  for (int k = 0; k < 3; ++k) {
    for (int i = 0; i < 4; ++i) s.posx[k][i] = n++;
    if (k == 1) { n += 6; s.pose = n - 1; }
  }
  for (int st = NINIT; st < 8; ++st) { n += 4; s.posa[st] = n - 1; }
  s.pre = n;
  for (int k = 0; k < 16; ++k)
    for (int i = 0; i < 4; ++i) {
      s.top[k][i] = n;
      const int g = 4 * k + i - 3;
      if (i == 3) s.bar[k] = n;
      if (i == 3) { s.ext[k] = n; n += p1_extra(k); if (k == P1_MODLD + 1) s.posm = n - 1; }
      if (g >= 0 && (g >> 2) < 8 + ND1) s.posa[8 + (g >> 2)] = n++;
      if (k + 3 < 16) s.posx[k + 3][i] = n++;
    }
  return s;
}
constexpr Sched1 S1 = mk_sched1();
#define NG0 6
constexpr int P4_PRE = 8;
constexpr int P4_E0 = 34;
constexpr int P4_UG = 24, P4_UR = 31;
struct Sched4 { int posx[32], posg[24], posug, possto, wait[64], bar[16], urw, end; };
constexpr int p4_piece(int t, int m) { return (t >= P4_E0 && m < 3 && (t - P4_E0) * 3 + m < 80) ? (t - P4_E0) * 3 + m : -1; }
constexpr Sched4 mk_sched4() {
  Sched4 s{}; int n = 0;
  for (int i = 0; i < P4_PRE; ++i) s.posx[i] = n++;
  for (int t = 0; t < 64; ++t) {
    s.wait[t] = n;
    for (int m = 0; m < 4; ++m) {
      if (m == 0 && t == P4_UR) s.urw = n;
      if (m == 0 && t < 4 * (8 - NG0)) s.posg[NG0 + (t >> 2)] = n++;
      if (p4_piece(t, m) >= 0 && p4_piece(t, m) % 20 == 19) s.possto = n++;
      if (m == 1 && (t & 3) == 3) s.bar[t >> 2] = n;
      if (m == 1) { if (t + P4_PRE < 32) s.posx[t + P4_PRE] = n++; else if (t >= P4_UG && t < P4_UG + 4) s.posug = n++; }
      if (m == 3 && t >= 3) s.posg[8 + ((t - 3) >> 2)] = n++;
    }
  }
  s.end = n;
  return s;
}
constexpr Sched4 S4 = mk_sched4();
constexpr int P5_PRE = 10;
struct Sched5 { int posa[56], wait[56], end; };
constexpr Sched5 mk_sched5() {
  Sched5 s{}; int n = 0;
  for (int i = 0; i < P5_PRE; ++i) s.posa[i] = n++;
  for (int t = 0; t < 56; ++t) {
    if (t == 28) for (int i = 0; i < P5_PRE; ++i) s.posa[28 + i] = n++;
    s.wait[t] = n;
    if ((t % 28) + P5_PRE < 28) s.posa[t + P5_PRE] = n++;
    if (t >= 3 && t < 27) n++;
  }
  s.end = n;
  return s;
}
constexpr Sched5 S5 = mk_sched5();

struct Ge { float g0, g1, a0, a1, m0, m1; };
DI void ge_a(Ge& e, float q0, float q1, float r) {
  asm volatile("v_mul_f32 %0, %4, %5\n\tv_mul_f32 %1, %4, %6\n\tv_mul_f32 %2, %0, %0\n\tv_mul_f32 %3, %1, %1"
               : "=&v"(e.g0), "=&v"(e.g1), "=&v"(e.a0), "=&v"(e.a1) : "v"(r), "v"(q0), "v"(q1));
}
DI void ge_b(Ge& e, float c3, float c1) {
  asm volatile("v_fma_f32 %0, %0, %4, %5\n\tv_fma_f32 %1, %1, %4, %5\n\tv_mul_f32 %0, %0, %2\n\tv_mul_f32 %1, %1, %3"
               : "+v"(e.a0), "+v"(e.a1) : "v"(e.g0), "v"(e.g1), "v"(c3), "v"(c1));
}
DI void ge_c(Ge& e, float u0, float u1) {
  asm volatile("v_exp_f32 %0, %0\n\tv_exp_f32 %1, %1\n\tv_mul_f32 %2, %4, %6\n\tv_mul_f32 %3, %5, %7"
               : "+v"(e.a0), "+v"(e.a1), "=&v"(e.m0), "=&v"(e.m1) : "v"(e.g0), "v"(e.g1), "v"(u0), "v"(u1));
}
DI void ge_d(Ge& e, float sd) {
  asm volatile("v_fma_f32 %0, %0, %2, %2\n\tv_fma_f32 %1, %1, %2, %2\n\tv_rcp_f32 %0, %0\n\tv_rcp_f32 %1, %1\n\ts_nop 0"
               : "+v"(e.a0), "+v"(e.a1) : "v"(sd));
}
DI u32 ge_e(Ge& e) {
  u32 o;
  asm volatile("v_mul_f32 %1, %1, %3\n\tv_mul_f32 %2, %2, %4\n\tv_cvt_pk_bf16_f32 %0, %1, %2"
               : "=v"(o), "+v"(e.m0), "+v"(e.m1) : "v"(e.a0), "v"(e.a1));
  return o;
}

__global__ void __launch_bounds__(256, 1)
mk_block(const u16* __restrict__ x, const u16* __restrict__ Sre, const u16* __restrict__ Sim, const u16* __restrict__ Dre,
         const u16* __restrict__ Dim, const u16* __restrict__ Wg, const u16* __restrict__ Wu, const u16* __restrict__ Wn,
         const u16* __restrict__ cvec, const u16* __restrict__ wmod, u16* __restrict__ out, char* __restrict__ ws) {
  extern __shared__ __attribute__((aligned(16))) char smem_[];
  LAS char* sm = (LAS char*)smem_;
  const int tid = threadIdx.x, lane = tid & 63, w = __builtin_amdgcn_readfirstlane(tid >> 6);
  const int l31 = lane & 31, hh = lane >> 5, l15 = lane & 15, qq = lane >> 4;
  const int bid = blockIdx.x, xcd = bid & 7, loc = bid >> 3;
  const int tb = xcd * 4 + (loc >> 3), nt = loc & 7;
  const int cb = nt & 3, kh = nt >> 2;
  const int rb = (xcd >> 1) * 4 + loc / 7, ft = (xcd & 1) * 7 + loc % 7;
  const int w1 = w & 1, wk = w >> 1;
  const u32 lane16 = lane * 16, wl = w * 4096;
  const rsrc_t rX = mk_rsrc(x, S * D * 2);
  u32x4 xr[2];
  ld_v4p<0>(xr[0], lane16, rX, (bid * 8 + w * 2) * 1024);
  ld_v4p<1024>(xr[1], lane16, rX, (bid * 8 + w * 2) * 1024);
  STAMP(0); FS(90);
  const rsrc_t rW = mk_rsrc(ws, WS_END);
  u32 vA[4];
  sfor<4>([&](auto i) { vA[i] = (lane >> 3) * 4096 + (((lane & 7) ^ ((i & 1) * 4 + qq)) << 4) + i * (8 * 4096 - 1024); });
  auto kblk = [&](int st) { return kh * 16 + ((cb * 4 + st) & 15); };
  u64 tag;

  {
    const rsrc_t r0 = mk_rsrc(w1 == 0 ? Sre : Sim, S * S * 2);
    auto fill = [&](auto qc) {
      constexpr int q = qc, st = q >> 3, hf = (q >> 2) & 1, p = q & 3;
      if constexpr (p == 0) set_m0n<st * 16384 + hf * 4096>(w1 * 8192);
      dma<100 + st, p * 1024>(vA[p], r0, (tb * 64 + hf * 32) * 4096 + kblk(st) * 128);
    };
    asm volatile("s_load_dwordx2 %0, %1, 0x0" : "=s"(tag) : "s"(ws + WS_GEN));
    if (w >= 2) { sfor<NW1>([&](auto q) { fill(q); }); wait_vm<NW1>(); } else { wait_vm<0>(); }
    asm volatile("s_waitcnt lgkmcnt(0)" : "+s"(tag));
    tag += 1;
    *(LAS u32*)(sm + L_CNT) = 0;
    tie_v(xr[0]); tie_v(xr[1]);
    FS(91);
    float v[2][8], mu[2], rs[2];
    sfor<2>([&](auto i) {
      sfor<4>([&](auto j) { u32 d = xr[i][+j]; v[i][2 * j] = b_lo(d); v[i][2 * j + 1] = b_hi(d); });
      mu[i] = ((v[i][0] + v[i][1]) + (v[i][2] + v[i][3])) + ((v[i][4] + v[i][5]) + (v[i][6] + v[i][7]));
    });
    sfor<2>([&](auto i) { mu[i] = wave_sum(mu[i]) * (1.f / D); });
    sfor<2>([&](auto i) {
      float s = 0.f;
      sfor<8>([&](auto j) { v[i][j] -= mu[i]; s += v[i][j] * v[i][j]; });
      rs[i] = s;
    });
    sfor<2>([&](auto i) { rs[i] = __builtin_amdgcn_rsqf(wave_sum(rs[i]) * (1.f / D) + 1e-5f); });
    sfor<2>([&](auto i) {
      u32x4 o;
      sfor<4>([&](auto j) { o[+j] = pack2(v[i][2 * j] * rs[i], v[i][2 * j + 1] * rs[i]); });
      *(LAS u32x4*)(sm + L_EX + (w * 2 + i) * 1088 + lane16) = o;
    });
    bar();
    FS(92);
    if (w < 2) {
      const u32 q = (lane & 15) >> 2, pp = lane & 3;
      const u32 ta = L_EX + q * 1088 + (w * 256 + qq * 16 + pp * 4) * 2;
      sfor<4>([&](auto m) {
        u32x4 o = lds_tr(sm, ta + m * 128, ta + m * 128 + 4 * 1088);
        pub16<1>(o, rW, hh * 1024 + l31 * 16, WS_XN + (bid >> 1) * 16384 + (w * 8 + m * 2) * 1024 + (bid & 1) * 512);
      });
      bar();
      wait_vm<0, 1>();
      FS(93);
      u32 old = 0;
      if (lane == 0) old = __hip_atomic_fetch_add((LAS u32*)(sm + L_CNT), 1u, __ATOMIC_RELAXED, __HIP_MEMORY_SCOPE_WORKGROUP);
      old = __builtin_amdgcn_readfirstlane(old);
      if (old == 1 && lane < 8) post_flag(ws + WS_F1 + (lane * 256 + bid) * 128, tag);
      STAMP(14);
    } else {
      bar();
      sfor<NINIT * 8 - NW1>([&](auto q) { fill(ic<NW1 + q>{}); });
    }
  }
  const u32 tag32 = (u32)tag;
  const u32 sF = WS_F1 + (xcd * 256 + kh * 128) * 128;
  const u32 vF0 = ((cb * 32 + l31) & 127) * 128, vF1 = ((cb * 32 + 32 + lane) & 127) * 128, vF2 = ((cb * 32 + 96 + l31) & 127) * 128;
  {
    const GAS u64* q = (const GAS u64*)(ws + sF + vF0);
    for (u32 sp = 0; sp < SPIN_CAP; ++sp) {
      u64 a = __hip_atomic_load(q, RLX_AGENT);
      if (__all(a == tag)) break;
      __builtin_amdgcn_s_sleep(POLL_SLEEP);
    }
    asm volatile("" ::: "memory");
  }
  u32x2 fl[2];
  ld_v2<0>(fl[0], vF1, rW, sF);
  ld_v2<0>(fl[1], vF2, rW, sF);
  STAMP(1); FS(94);

  const rsrc_t rS = mk_rsrc(w < 2 ? Sre : Sim, S * S * 2);
  const rsrc_t rD = mk_rsrc(w < 2 ? Dre : Dim, D * D * 2);
  u32 vD[4], vX[4];
  sfor<4>([&](auto i) { vD[i] = (lane >> 3) * 1024 + (((lane & 7) ^ ((i & 1) * 4 + qq)) << 4) + i * (8 * 1024 - 1024); vX[i] = lane16 + i * 16384; });
  const u32 sA = (tb * 64 + w1 * 32) * 4096, sD = (nt * 64 + w1 * 32) * 1024;
  const u32 sXN = WS_XN + (cb * 4 + w) * 1024;
  auto a_dma = [&](auto stc, auto pc) { constexpr int st = stc, p = pc; dma<100 + st, p * 1024>(vA[p], rS, sA + kblk(st) * 128); };
  auto d_dma = [&](auto sc_, auto pc) { constexpr int s = sc_, p = pc; dma<2, p * 1024>(vD[p], rD, sD + s * 128); };
  u32x4 xw[4][4];
  auto xn_ld = [&](auto stc, auto ic_) { constexpr int st = stc, i = ic_; ld_a<0>(xw[st & 3][i], vX[i], rW, sXN + kblk(st) * 65536); };
  u32x4 mg[8], gvd, wm[4], xres[2];
  u32x2 cr;
  const int ukq = bid < 224 ? bid / 56 : 3, ucb = bid % 56;
  const int mkq = bid < 192 ? bid / 48 : 3, mcb = bid % 48;
  sfor<3>([&](auto kc) {
    sfor<4>([&](auto i) { xn_ld(kc, i); });
    if constexpr (kc == 1) {
      const rsrc_t rM = mk_rsrc(wmod, D * 6 * D * 2);
      const rsrc_t rC = mk_rsrc(cvec, D * 2);
      sfor<4>([&](auto r) { ld_v4p<0>(wm[r], (lane >> 3) * 4 * 6144 + (lane & 7) * 16, rM, (mkq * 128 + w * 32 + r) * 6144 + mcb * 128); });
      ld_v2p<0>(cr, (lane >> 3) * 8, rC, (mkq * 128 + w * 32) * 2);
      const rsrc_t rV = mk_rsrc(bid < 128 ? Dre : Dim, D * D * 2);
      ld_v4p<0>(gvd, lane16, rV, ((bid & 127) * 4 + w) * 1024);
    }
  });
  sfor<8 - NINIT>([&](auto s) { set_m0n<(NINIT + s) * 16384>(wl); sfor<4>([&](auto p) { a_dma(ic<NINIT + s>{}, p); }); });

  u32 aB[4][2];
  sfor<4>([&](auto i) { sfor<2>([&](auto hf) { aB[i][hf] = l31 * 128 + (((2 * i + hh) ^ ((l31 >> 1) & 7)) << 4) + hf * 65536; tie_v(aB[i][hf]); }); });
  u32x4 af[2][4], rf;
  auto a_rd = [&](int t, int j) { af[t & 1][j] = lds16(sm, aB[t & 3][(t >> 4) & 1] + ((t >> 2) & 3) * 16384 + j * 4096); };
  f32x16 acc[4], rsa;
  u32x4 ones = {0x3f803f80u, 0x3f803f80u, 0x3f803f80u, 0x3f803f80u};
  tie_v(ones);
  const bool h0 = (lane >> 3) & 1;
  u32 lM = L_MODV; tie_v(lM);
  const u32 vM2 = (w == 0 ? 512 + cb * 128 + 2 * lane : w == 1 ? 1024 + hh * 1024 + nt * 64 + 2 * l31 : (w == 2 ? 1536 : 2048) + ukq * 128 + 2 * lane) * 8;
  float ma[8];
  bar_vm<S1.pre - 1 - S1.posx[0][0], 100>();
  sfor<4>([&](auto j) { a_rd(0, j); });
  rf = lds16(sm, aB[0][0] + wl);
  tie_v(fl[0]); tie_v(fl[1]);
  for (u32 sp = 0; sp < SPIN_CAP; ++sp) {
    const u32 th = (u32)(tag >> 32);
    if (__all(fl[0][0] == tag32 && fl[0][1] == th && fl[1][0] == tag32 && fl[1][1] == th)) break;
    __builtin_amdgcn_s_sleep(POLL_SLEEP);
    ld_v2<0>(fl[0], vF1, rW, sF);
    ld_v2<0>(fl[1], vF2, rW, sF);
    wait_vm<0>();
    tie_v(fl[0]); tie_v(fl[1]);
  }
  sfor<64>([&](auto tc) {
    constexpr int t = tc, k = t >> 2, i = t & 3, g = t - 3;
    SCHED();
    wait_a<S1.top[k][i] - 1 - S1.posx[k][i]>(xw[k & 3][i]);
    sfor<4>([&](auto mc) {
      constexpr int m = mc;
      if constexpr (m == 2 && i == 3 && k < 8 + ND1) set_m0<(k & 7) * 16384>(wl);
      mm_av<t == 0>(acc[m], xw[k & 3][i], af[t & 1][m]);
      if constexpr (t < 16 && m == 0) mm_vv<t == 0>(rsa, ones, rf);
      SCHED();
      if constexpr (i != 3) a_rd(t + 1, m);
      if constexpr (m == 1 && i == 3) {
        constexpr int na = k < 15 ? S1.bar[k] - 1 - S1.posa[k + 1] : 63;
        constexpr int ne = k == P1_MODC ? S1.bar[k] - 1 - S1.pose : k == P1_MODUSE ? S1.bar[k] - 1 - S1.posm : 63;
        bar_vm<(na < ne ? na : ne), (k < 15 ? 101 + k : 0)>();
        FS(k);
        if constexpr (t < 63) { a_rd(t + 1, 0); a_rd(t + 1, 1); }
      }
      if constexpr (m >= 2 && i == 3 && t < 63) a_rd(t + 1, m);
      if constexpr (m == 2 && t < 15) rf = lds16(sm, aB[(t + 1) & 3][0] + ((t + 1) >> 2) * 16384 + wl);
      if constexpr (m == 2 && i == 3) {
        if constexpr (k == P1_MODC || k == P1_MODC + 1) {
          if constexpr (k == P1_MODC) { sfor<4>([&](auto r) { tie_v(wm[r]); }); tie_v(cr); sfor<8>([&](auto q) { ma[q] = 0.f; }); }
          sfor<2>([&](auto rr) {
            constexpr int r = 2 * (k - P1_MODC) + rr;
            u32 cd = cr[r >> 1];
            float cv = (r & 1) ? b_hi(cd) : b_lo(cd);
            float sc = cv * __builtin_amdgcn_rcpf(1.f + __builtin_amdgcn_exp2f(-1.4426950408889634f * cv));
            sfor<4>([&](auto jj) { u32 d = wm[r][+jj]; ma[2 * jj] += sc * b_lo(d); ma[2 * jj + 1] += sc * b_hi(d); });
          });
          if constexpr (k == P1_MODC + 1) {
            float b4[4], b2[2];
            sfor<4>([&](auto jj) { b4[jj] = red_x32(ma[jj], ma[4 + jj]); });
            sfor<2>([&](auto jj) { b2[jj] = red_x16(b4[jj], b4[2 + jj]); });
            *(LAS float*)(sm + lM + (L_MP - L_MODV) + w * 256 + ((lane & 7) * 8 + (lane >> 3)) * 4) = red_x8(b2[0], b2[1], h0);
          } else {
            sfor<8>([&](auto q) { tie_v(ma[q]); });
          }
        }
        if constexpr (k == P1_MODC + 2) {
          if (w == 0 && bid < 192) {
            float s = 0.f;
            sfor<4>([&](auto r) { s += *(LAS float*)(sm + lM + (L_MP - L_MODV) + r * 256 + lane * 4); });
            u32x2 gr = {__float_as_uint(s), tag32};
            pub8<0>(gr, rW, lane * 8, WS_MODG + (mkq * 3072 + mcb * 64) * 8);
          }
        }
        if constexpr (k == P1_MODLD || k == P1_MODLD + 1) {
          sfor<2>([&](auto qc) { constexpr int q = 2 * (k - P1_MODLD) + qc;
            ld_v4<0>(mg[2 * q], tid * 16, rW, WS_MODG + q * 3072 * 8); ld_v4<0>(mg[2 * q + 1], vM2, rW, WS_MODG + q * 3072 * 8); });
        }
        if constexpr (k == P1_XR)
          sfor<2>([&](auto p) { ld_v4p<p * 64>(xres[p], l15 * 1024 + (qq & 1) * 32 + (qq >> 1) * 16, rX, (tb * 64 + w * 16) * 1024 + nt * 128); });
        if constexpr (k == P1_MODUSE) {
          sfor<8>([&](auto q) { tie_v(mg[q]); });
          for (u32 sp = 0; sp < SPIN_CAP; ++sp) {
            bool ok = true;
            sfor<8>([&](auto q) { ok = ok && mg[q][1] == tag32 && mg[q][3] == tag32; });
            if (__all(ok)) break;
            __builtin_amdgcn_s_sleep(1);
            sfor<4>([&](auto q) { ld_v4<0>(mg[2 * q], tid * 16, rW, WS_MODG + q * 3072 * 8); ld_v4<0>(mg[2 * q + 1], vM2, rW, WS_MODG + q * 3072 * 8); });
            wait_vm<0>();
            sfor<8>([&](auto q) { tie_v(mg[q]); });
          }
          sfor<2>([&](auto e) {
            f32x2 mv;
            sfor<2>([&](auto o) {
              u32 a = mg[e][2 * o], b = mg[2 + e][2 * o], c = mg[4 + e][2 * o], d = mg[6 + e][2 * o];
              mv[+o] = (__uint_as_float(a) + __uint_as_float(b)) + (__uint_as_float(c) + __uint_as_float(d)); });
            *(LAS f32x2*)(sm + lM + e * 2048 + tid * 8) = mv;
          });
        }
        if constexpr (k == P1_V) {
          tie_v(gvd);
          f32x4 s0 = ldsf4(sm, lM + lane * 32), s1 = ldsf4(sm, lM + lane * 32 + 16);
          u32 d0 = gvd[0], d1 = gvd[1], d2 = gvd[2], d3 = gvd[3];
          float v = s0[0] * b_lo(d0) + s0[1] * b_hi(d0) + s0[2] * b_lo(d1) + s0[3] * b_hi(d1) + s1[0] * b_lo(d2) + s1[1] * b_hi(d2) + s1[2] * b_lo(d3) + s1[3] * b_hi(d3);
          v = wave_sum(v);
          if (lane == 0) { u32x2 gr = {__float_as_uint(v), tag32}; pub8<0>(gr, rW, 0, WS_VG + (bid * 4 + w) * 8); }
        }
      }
      if constexpr (m == 2 && g >= 0 && (g >> 2) < 8 + ND1) {
        if constexpr ((g >> 2) < 8) a_dma(ic<8 + (g >> 2)>{}, ic<(g & 3)>{}); else d_dma(ic<(g >> 2) - 8>{}, ic<(g & 3)>{});
      }
      if constexpr (m == 3 && k + 3 < 16) xn_ld(ic<k + 3>{}, ic<i>{});
      SCHED();
    });
  });
  STAMP(2); FS(32);
  u32x4 gvw[4], warm[2];
  {
    f32x4 sc[4];
    sfor<4>([&](auto g) { sc[g] = ldsf4(sm, lM + (512 + w * 32 + g * 8 + hh * 4) * 4) + 1.f; });
    mm_fence(acc[0], acc[1]); mm_tie(acc[2], acc[3]); mm_tie(rsa, rsa);
    const u32 vP = (l31 >> 4) * 65536 + hh * 256 + l15 * 16;
    sfor<4>([&](auto jc) {
      constexpr int j = jc;
      u32 pk[4][2];
      sfor<4>([&](auto g) {
        f32x4 m = {acc[j][4 * g], acc[j][4 * g + 1], acc[j][4 * g + 2], acc[j][4 * g + 3]};
        m = j < 2 ? m * sc[g] : -m * sc[g];
        pk[g][0] = pack2(m[0], m[1]); pk[g][1] = pack2(m[2], m[3]);
      });
      sfor<2>([&](auto q) {
        pub16<3, q * 512>(chunk_swap(pk[2 * q][0], pk[2 * q][1], pk[2 * q + 1][0], pk[2 * q + 1][1]), rW, vP,
                          WS_PQ + ((tb * 4 + (j & 1) * 2) * 64 + kh * 32 + (j >> 1) * 16 + cb * 4 + w) * 1024);
      });
    });
    pub4<3>(__float_as_uint(rsa[0]), rW, l31 * 4, WS_RS + ((tb * 8 + nt) * 128 + w * 32) * 4);
    bar();
    FS(39);
    sfor<2>([&](auto s) { set_m0n<(ND1 + s) * 16384>(wl); sfor<4>([&](auto p) { d_dma(ic<ND1 + s>{}, p); }); });
    bar_vm<8, 3>();
    if (tid == 0) post_flag(ws + WS_F2 + (tb * 8 + nt) * 128, tag);
    sfor<6 - ND1>([&](auto s) { set_m0n<(ND1 + 2 + s) * 16384>(wl); sfor<4>([&](auto p) { d_dma(ic<ND1 + 2 + s>{}, p); }); });
    const rsrc_t rU = mk_rsrc(ucb < 28 ? Wg : Wu, D * F * 2);
    const u32 vu = (tid >> 3) * (F * 2) + (lane & 7) * 16;
    sfor<4>([&](auto r) { ld_v4p<0>(gvw[r], vu, rU, (ukq * 128 + r * 32) * (F * 2) + (ucb % 28) * 128); });
    const rsrc_t rN = mk_rsrc(Wn, F * D * 2);
    ld_v4p<0>(warm[0], tid * 16, rN, bid * 7168);
    ld_v4p<0>(warm[1], tid * 16, rN, bid * 7168 + 4096);
  }
  STAMP(3); FS(33);
  poll_flags<8>(ws + WS_F2 + tb * 8 * 128, tag, lane);
  if (bid == 0 && tid == 0) post_flag(ws + WS_GEN, tag);
  STAMP(4); FS(34);

  f32x4 x1[4];
  {
    constexpr int PRE = 5;
    u32x4 pa[32][2];
    const u32 so = WS_PQ + (tb * 4 + w) * 65536;
    const u32 vP1 = lane16 + 32768;
    auto pa_ld = [&](auto sc_, auto hc) {
      constexpr int s = sc_, h = hc;
      ld_a<(s & 1) * 1024>(pa[s][h], h ? vP1 : lane16, rW, so + (((s >> 2) + nt) & 7) * 2048 + ((s >> 1) & 1) * 16384);
    };
    sfor<PRE>([&](auto s) { pa_ld(s, ic<0>{}); pa_ld(s, ic<1>{}); });
    u32 rp[4];
    u32x4 vg;
    u32 dB[2];
    sfor<2>([&](auto u) { dB[u] = l15 * 128 + (((4 * u + qq) ^ ((l15 >> 1) & 7)) << 4); });
    bar_vm<2 * PRE, 2>();
    f32x4 y[4];
    u32x4 df[2][4];
    u32 da;
    auto d_ad = [&](int s) { da = dB[s & 1] + (((s >> 2) + nt) & 7) * 16384; };
    auto d_rd = [&](int s, int c) { df[s & 1][c] = lds16(sm, da + ((s >> 1) & 1) * 8192 + c * 2048); };
    d_ad(0);
    sfor<4>([&](auto c) { d_rd(0, c); });
    sfor<32>([&](auto sc_) {
      constexpr int s = sc_;
      constexpr int issued = 2 * (s + PRE) < 69 ? 2 * (s + PRE) : 69;
      SCHED();
      if constexpr (s < 31) d_ad(s + 1);
      wait_a<issued - 1 - 2 * s>(pa[s][0]);
      if constexpr ((s & 3) == 0) FS(80 + (s >> 2));
      sfor<8>([&](auto mc) {
        constexpr int m = mc, c = m & 3, h = m >> 2, n = 2 * (s + PRE) + h;
        if constexpr (m == 4) wait_a<(issued < 69 ? issued + 1 : 69) - 1 - (2 * s + 1)>(pa[s][1]);
        m16_va<s == 0 && h == 0>(y[c], df[s & 1][c], pa[s][h]);
        SCHED();
        if constexpr (h == 0 && s < 31) d_rd(s + 1, c);
        if constexpr (c == 1) {
          if constexpr (n < 64) pa_ld(ic<s + PRE>{}, ic<h>{});
          else if constexpr (n < 68) ld_v1<(n - 64) * 512>(rp[n - 64], ((qq >> 1) * 512 + (qq & 1) * 64 + w * 16 + l15) * 4, rW, WS_RS + tb * 4096);
          else if constexpr (n == 68) ld_v4<0>(vg, (hh * 512 + nt * 64 + 2 * l31) * 8, rW, WS_VG);
        }
        SCHED();
      });
    });
    STAMP(5); FS(35);
    f32x4 ga[4], cbv[4];
    sfor<4>([&](auto c) { const u32 eo = (c * 16 + qq * 4) * 4; ga[c] = ldsf4(sm, lM + 640 * 4 + eo); cbv[c] = ldsf4(sm, lM + 704 * 4 + eo) + 1.f; });
    wait_vm<0>();
    sfor<4>([&](auto p) { tie_v(rp[p]); });
    tie_v(vg); tie_v(xres[0]); tie_v(xres[1]);
    for (u32 sp = 0; sp < SPIN_CAP; ++sp) {
      if (__all(vg[1] == tag32 && vg[3] == tag32)) break;
      __builtin_amdgcn_s_sleep(1);
      ld_v4<0>(vg, (hh * 512 + nt * 64 + 2 * l31) * 8, rW, WS_VG);
      wait_vm<0>();
      tie_v(vg);
    }
    { u32 a = vg[0], b = vg[2]; u32x2 o = {a, b}; *(LAS u32x2*)(sm + lM + (L_RS - L_MODV) + w * 512 + lane * 8) = o; }
    float rre, rim;
    {
      const float r4 = add_x32((__uint_as_float(rp[0]) + __uint_as_float(rp[1])) + (__uint_as_float(rp[2]) + __uint_as_float(rp[3])));
      auto r = __builtin_amdgcn_permlane16_swap(__float_as_uint(r4), __float_as_uint(r4), false, false);
      u32 a = r[0], b = r[1]; rre = __uint_as_float(a); rim = __uint_as_float(b);
    }
    u32 xg[4][2];
    sfor<2>([&](auto p) { u32x4 t = chunk_swap16(xres[p][0], xres[p][1], xres[p][2], xres[p][3]); xg[2 * p][0] = t[0]; xg[2 * p][1] = t[1]; xg[2 * p + 1][0] = t[2]; xg[2 * p + 1][1] = t[3]; });
    m16_fence(y);
    u32 pk[4][2];
    sfor<4>([&](auto c) {
      const u32 eo = (L_RS - L_MODV) + w * 512 + (c * 16 + qq * 4) * 4;
      f32x4 vre = ldsf4(sm, lM + eo), vim = ldsf4(sm, lM + 256 + eo);
      f32x4 xv = {b_lo(xg[c][0]), b_hi(xg[c][0]), b_lo(xg[c][1]), b_hi(xg[c][1])};
      x1[c] = xv + ga[c] * (y[c] + rre * vre - rim * vim);
      f32x4 s = x1[c] * cbv[c];
      pk[c][0] = pack2(s[0], s[1]); pk[c][1] = pack2(s[2], s[3]);
    });
    sfor<2>([&](auto p) {
      pub16<4, p * 2048>(chunk_swap16(pk[2 * p][0], pk[2 * p][1], pk[2 * p + 1][0], pk[2 * p + 1][1]), rW,
                         (qq & 1) * 1024 + (qq >> 1) * 512 + l15 * 16, WS_X1 + ((tb * 2 + wk) * 32 + nt * 4) * 1024 + w1 * 256);
    });
    const f32x4 s4 = (x1[0] + x1[1]) + (x1[2] + x1[3]);
    const float mean = add_x32(add_x16((s4[0] + s4[1]) + (s4[2] + s4[3]))) * (1.f / 64);
    f32x4 q4 = {0.f, 0.f, 0.f, 0.f};
    sfor<4>([&](auto c) { f32x4 d = x1[c] - mean; q4 += d * d; });
    const float m2 = add_x32(add_x16((q4[0] + q4[1]) + (q4[2] + q4[3])));
    u32x2 o = {__float_as_uint(mean), __float_as_uint(m2)};
    pub8<4>(o, rW, l15 * 8, WS_ST + ((tb * 2 + wk) * 8 + nt) * 256 + w1 * 128);
    bar();
  }
  FS(36);
  const rsrc_t rG = mk_rsrc(w < 2 ? Wg : Wu, D * F * 2);
  const rsrc_t rN = mk_rsrc(Wn, F * D * 2);
  u32 vG[4], vN[4];
  sfor<4>([&](auto i) {
    vG[i] = (lane >> 3) * (F * 2) + (((lane & 7) ^ ((qq & 1) << 2)) << 4) + i * (8 * F * 2 - 1024);
    vN[i] = (lane >> 3) * 1024 + (((lane & 7) ^ (((qq & 1) + 2 * (i & 1)) << 1)) << 4) + i * (8 * 1024 - 1024);
  });
  const u32 sG = w1 * 32 * (F * 2) + ft * 256, sN = (wk * 128 + w1 * 32) * 1024 + nt * 128;
  auto g_dma = [&](auto jc, auto ic_) {
    constexpr int j = jc, i = ic_;
    dma<200 + j, i * 1024>(vG[i], rG, sG + ((j + ft) & 7) * (64 * F * 2) + (j >> 3) * 128);
  };
  auto rot7 = [&](int j) { int pr = j + nt; pr = pr >= 7 ? pr - 7 : pr; return pr >= 7 ? pr - 7 : pr; };
  auto n_dma = [&](auto jc, auto ic_) {
    constexpr int j = jc, i = ic_;
    dma<300 + j, i * 1024>(vN[i], rN, sN + rot7(j % 7) * (256 * 1024) + (j / 7) * 65536);
  };
  const bool act4 = loc < 28;
  u32x4 ug[4];
  const u32 vUV = ((tid >> 7) * F + ft * 128 + (tid & 127)) * 16;
  if (act4) {
    sfor<3>([&](auto j) { set_m0n<j * 16384>(wl); sfor<4>([&](auto i) { g_dma(j, i); }); });
  } else {
    sfor<3>([&](auto j) { set_m0n<j * 16384>(wl); sfor<4>([&](auto i) { n_dma(j, i); }); });
  }
  bar_vm<12, 4>();
  if (tid == 0) post_flag(ws + WS_F3 + (tb * 8 + nt) * 128, tag);
  FS(100);
  if (act4) {
    sfor<NG0 - 3>([&](auto j) { set_m0n<(j + 3) * 16384>(wl); sfor<4>([&](auto i) { g_dma(ic<j + 3>{}, i); }); });
  } else {
    sfor<5>([&](auto j) { set_m0n<(j + 3) * 16384>(wl); sfor<4>([&](auto i) { n_dma(ic<j + 3>{}, i); }); });
  }
  FS(101);
  {
    sfor<4>([&](auto i) { tie_v(gvw[i]); });
    tie_v(warm[0]); tie_v(warm[1]);
    float a[16];
    sfor<16>([&](auto j) { a[j] = 0.f; });
    sfor<4>([&](auto i) {
      const float sh = *(LAS float*)(sm + lM + (768 + (tid >> 3) + i * 32) * 4);
      const float cs = 1.f + *(LAS float*)(sm + lM + (896 + (tid >> 3) + i * 32) * 4);
      sfor<4>([&](auto j) { u32 d = gvw[i][+j]; float lo = b_lo(d), hi = b_hi(d);
        a[2 * j] += cs * lo; a[2 * j + 1] += cs * hi; a[8 + 2 * j] += sh * lo; a[8 + 2 * j + 1] += sh * hi; });
    });
    float b8[8], b4[4];
    sfor<8>([&](auto j) { b8[j] = red_x32(a[j], a[8 + j]); });
    sfor<4>([&](auto j) { b4[j] = red_x16(b8[j], b8[4 + j]); });
    f32x2 o = {red_x8(b4[0], b4[2], h0), red_x8(b4[1], b4[3], h0)};
    *(LAS f32x2*)(sm + lM + (L_UVP - L_MODV) + w * 512 + (hh * 64 + (lane & 7) * 8 + ((lane >> 3) & 3) * 2) * 4) = o;
  }
  bar();
  if (w == 0 && bid < 224) {
    float su = 0.f, sv = 0.f;
    sfor<4>([&](auto i) { su += *(LAS float*)(sm + lM + (L_UVP - L_MODV) + i * 512 + lane * 4); sv += *(LAS float*)(sm + lM + (L_UVP - L_MODV) + i * 512 + 256 + lane * 4); });
    u32x4 g = {__float_as_uint(su), tag32, __float_as_uint(sv), tag32};
    pub16<0>(g, rW, lane16, WS_UVG + (ukq * 3584 + (ucb / 28) * F + (ucb % 28) * 64) * 16);
  }
  STAMP(6); FS(37);
  u32x4 gbg[4];
  const GAS u64* pF4A = (const GAS u64*)(ws + WS_F4A + ((tb >> 1) * 16 + (lane < 14 ? lane : 0)) * 128);
  const GAS u64* pF4B = (const GAS u64*)(ws + WS_F4B + ((tb >> 1) * 16 + (lane < 14 ? lane : 0)) * 128);

  if (act4) {
    u32 tB[2][2];
    {
      const u32 q = l15 >> 2, pp = lane & 3;
      sfor<2>([&](auto c) { sfor<2>([&](auto hf) {
        tB[c][hf] = (8 * hh + q) * 128 + ((4 * (c ^ (q >> 1)) + 2 * (qq & 1) + (pp >> 1)) << 4) + 8 * (pp & 1) + hf * 65536; tie_v(tB[c][hf]); }); });
    }
    poll_flags<16>(ws + WS_F3 + rb * 16 * 128, tag, lane);
    STAMP(7); FS(38);
    u32x2 stv[4];
    sfor<4>([&](auto i) { ld_v2<i * 256>(stv[i], hh * 1024 + l31 * 8, rW, WS_ST + (rb * 4 + w) * 2048); });
    u32x4 xa[32];
    const u32 so = WS_X1 + (rb * 4 + w) * 32 * 1024;
    auto xa_ld = [&](auto sc_) { constexpr int s = sc_; ld_a<(s & 3) * 1024>(xa[s], lane16, rW, so + (((s >> 2) + ft) & 7) * 4096); };
    sfor<P4_PRE>([&](auto s) { xa_ld(s); });
    bar_vm<P4_PRE, 200, 200 + NG0 - 1>();
    f32x16 z[2][4];
    u32x4 wf[2][4], uf[4];
    auto w_rd = [&](int t, int j) {
      const u32 o = tB[j & 1][(t >> 4) & 1] + ((t >> 2) & 3) * 16384 + (j >> 1) * 8192 + (t & 3) * 2048;
      wf[t & 1][j] = lds_tr(sm, o, o + 512);
    };
    sfor<4>([&](auto j) { w_rd(0, j); });
    float rr, sd;
    u32x4 xst;
    {
      sfor<4>([&](auto i) { tie_v(stv[i]); });
      float ms = 0.f;
      sfor<4>([&](auto i) { u32 a = stv[i][0]; ms += __uint_as_float(a); });
      const float mean = add_x32(ms) * (1.f / 8);
      float q = 0.f;
      sfor<4>([&](auto i) { u32 a = stv[i][0], b = stv[i][1]; float d = __uint_as_float(a) - mean; q += __uint_as_float(b) + 64.f * d * d; });
      const float var = add_x32(q) * (1.f / D) + 1e-5f;
      rr = __builtin_amdgcn_rsqf(var);
      sd = var * rr;
      const float mh = b_lo(pack2(mean, 0.f)), sh = b_lo(pack2(sd, 0.f));
      xst[0] = hh ? 0u : pack2(-mh, -mh); xst[1] = hh ? 0u : pack2(mh - mean, mh - mean);
      xst[2] = hh ? 0u : pack2(sh, sh); xst[3] = hh ? 0u : pack2(sd - sh, sd - sh);
      tie_v(xst); tie_v(rr); tie_v(sd);
    }
    auto u_rd = [&](int p, int j) { uf[j] = lds16(sm, lM + (L_UV - L_MODV) + ((j >> 1) * 128 + p * 64 + (j & 1) * 32 + l31) * 16); };
    constexpr float C1 = -2.f * 0.7978845608028654f * 1.4426950408889634f, C3 = C1 * 0.044715f;
    float c1v = C1, c3v = C3;
    tie_v(c1v); tie_v(c3v);
    const u32 sAct = WS_ACT + ((rb * 8 + 2 * w) * 56 + ft * 4) * 1024;
    const u32 vAct = (l31 >> 4) * (56 * 1024) + hh * 256 + l15 * 16;
    u32 pk[4];
    Ge ge;
    sfor<64>([&](auto tc) {
      constexpr int t = tc, j = t >> 2, s4 = t & 3, p = j >> 3;
      constexpr bool many = t < 4 * (8 - NG0) + 4;
      SCHED();
      if constexpr (p == 0) wait_a<S4.wait[t] - 1 - S4.posx[t]>(xa[t]);
      sfor<4>([&](auto mc) {
        constexpr int m = mc;
        if constexpr (m == 0 && t < 4 * (8 - NG0)) set_m0<(NG0 + (t >> 2)) * 16384>(wl);
        if constexpr (m == 3 && t >= 3 && (many || s4 == 3)) set_m0<(((t - 3) >> 2) & 7) * 16384>(wl);
        mm_va<(t & 31) == 0>(z[p][m], wf[t & 1][m], xa[t & 31]);
        if constexpr (t == 33) mm_vv<false>(z[0][m], uf[m], xst);
        SCHED();
        if constexpr (s4 != 3) { w_rd(t + 1, m); }
        if constexpr (t == 32) u_rd(0, m);
        if constexpr (m == 0 && t == P4_UR) {
          wait_vm<S4.urw - 1 - S4.posug>();
          sfor<4>([&](auto q) { tie_v(ug[q]); });
          for (u32 sp = 0; sp < SPIN_CAP; ++sp) {
            bool ok = true;
            sfor<4>([&](auto q) { ok = ok && ug[q][1] == tag32 && ug[q][3] == tag32; });
            if (__all(ok)) break;
            __builtin_amdgcn_s_sleep(1);
            sfor<4>([&](auto q) { ld_v4<0>(ug[q], vUV, rW, WS_UVG + q * 3584 * 16); });
            wait_vm<0>();
            sfor<4>([&](auto q) { tie_v(ug[q]); });
          }
          u32 d[2];
          sfor<2>([&](auto e) { u32 a = ug[0][2 * e], b = ug[1][2 * e], c = ug[2][2 * e], dd = ug[3][2 * e];
            const float s = (__uint_as_float(a) + __uint_as_float(b)) + (__uint_as_float(c) + __uint_as_float(dd));
            const float hi = b_lo(pack2(s, 0.f));
            d[e] = pack2(hi, s - hi); });
          u32x4 rec = {d[0], d[0], d[1], d[1]};
          *(LAS u32x4*)(sm + lM + (L_UV - L_MODV) + tid * 16) = rec;
        }
        if constexpr (m == 0 && t < 4 * (8 - NG0)) g_dma(ic<NG0 + (t >> 2)>{}, ic<(t & 3)>{});
        if constexpr (p4_piece(t, m) >= 0) {
          constexpr int e = p4_piece(t, m) / 5, st = p4_piece(t, m) % 5;
          if constexpr (st == 0) ge_a(ge, z[0][e >> 3][2 * (e & 7)], z[0][e >> 3][2 * (e & 7) + 1], rr);
          if constexpr (st == 1) ge_b(ge, c3v, c1v);
          if constexpr (st == 2) ge_c(ge, z[0][2 + (e >> 3)][2 * (e & 7)], z[0][2 + (e >> 3)][2 * (e & 7) + 1]);
          if constexpr (st == 3) ge_d(ge, sd);
          if constexpr (st == 4) {
            pk[e & 3] = ge_e(ge);
            if constexpr ((e & 3) == 3) pub16<5, (e >> 3) * 1024 + ((e >> 2) & 1) * 512>(chunk_swap(pk[0], pk[1], pk[2], pk[3]), rW, vAct, sAct);
          }
        }
        if constexpr (m == 1 && s4 == 3) {
          constexpr int need = j + 1 >= NG0 && j < 15 ? S4.bar[j] - 1 - S4.posg[j + 1] : 63;
          bar_vm<need, (j < 15 ? 201 + j : 0)>();
          FS(40 + j);
          if constexpr (t < 63) { w_rd(t + 1, 0); w_rd(t + 1, 1); } else { u_rd(1, 0); u_rd(1, 1); }
        }
        if constexpr (m == 1) {
          if constexpr (t + P4_PRE < 32) xa_ld(ic<t + P4_PRE>{});
          else if constexpr (t >= P4_UG && t < P4_UG + 4) ld_v4<0>(ug[t - P4_UG], vUV, rW, WS_UVG + (t - P4_UG) * 3584 * 16);
        }
        if constexpr (m >= 2 && s4 == 3) { if constexpr (t < 63) w_rd(t + 1, m); else u_rd(1, m); }
        if constexpr (m == 3 && t >= 3) {
          constexpr int gj = (t - 3) >> 2, gi = (t - 3) & 3;
          if constexpr (gj < 8) g_dma(ic<8 + gj>{}, ic<gi>{}); else n_dma(ic<gj - 8>{}, ic<gi>{});
        }
        SCHED();
      });
    });
    sfor<4>([&](auto m) { mm_vv<false>(z[1][m], uf[m], xst); if constexpr (m < 3) n_dma(ic<7>{}, ic<1 + m>{}); });
    STAMP(8); FS(56);
    mm_fence(z[1][0], z[1][2]); mm_tie(z[1][1], z[1][3]);
    sfor<4>([&](auto qc) {
      constexpr int c = qc >> 1, g0 = (qc & 1) * 2;
      u32 pq[4];
      sfor<2>([&](auto gg) {
        constexpr int g = g0 + gg;
        f32x4 zg = {z[1][c][4 * g], z[1][c][4 * g + 1], z[1][c][4 * g + 2], z[1][c][4 * g + 3]};
        f32x4 zu = {z[1][2 + c][4 * g], z[1][2 + c][4 * g + 1], z[1][2 + c][4 * g + 2], z[1][2 + c][4 * g + 3]};
        f32x4 gt = rr * zg;
        f32x4 ex = gt * (C1 + C3 * gt * gt);
        sfor<4>([&](auto i) { ex[+i] = __builtin_amdgcn_exp2f(ex[+i]); });
        ex = ex * sd + sd;
        sfor<4>([&](auto i) { ex[+i] = __builtin_amdgcn_rcpf(ex[+i]); });
        f32x4 a = gt * zu * ex;
        pq[2 * gg] = pack2(a[0], a[1]); pq[2 * gg + 1] = pack2(a[2], a[3]);
      });
      pub16<6, 2048 + c * 1024 + (qc & 1) * 512>(chunk_swap(pq[0], pq[1], pq[2], pq[3]), rW, vAct, sAct);
      if constexpr (qc == 0) {
        bar_vm<S4.end + 3 + 1 - 1 - S4.possto, 5>();
        if (tid == 0) post_flag(ws + WS_F4A + (rb * 16 + ft) * 128, tag);
      }
    });
  }
  STAMP(9); FS(57);

  {
    bar();
    for (u32 sp = 0; sp < SPIN_CAP; ++sp) {
      u64 fa = __hip_atomic_load(pF4A, RLX_AGENT);
      if (__all(fa == tag)) break;
      __builtin_amdgcn_s_sleep(POLL_SLEEP);
    }
    asm volatile("" ::: "memory");
    FS(102);
    u32x4 aa[56];
    const u32 so = WS_ACT + (tb * 4 + w) * 56 * 1024;
    const u32 vA2 = lane16 + 4096;
    auto aa_ld = [&](auto sc_) {
      constexpr int s = sc_;
      ld_a<(s & 1) * 1024 + (s / 28) * 2048>(aa[s], (s & 2) ? vA2 : lane16, rW, so + rot7((s >> 2) % 7) * 8192);
    };
    sfor<P5_PRE>([&](auto s) { aa_ld(s); });
    bar_vm<P5_PRE, 6, 307>();
    if (act4 && tid == 0) post_flag(ws + WS_F4B + (rb * 16 + ft) * 128, tag);
    STAMP(10); FS(58);
    u32 tN[4][2];
    {
      const u32 q = l15 >> 2, pp = lane & 3, f = (q >> 1) + 2 * (qq & 1);
      sfor<4>([&](auto c) { sfor<2>([&](auto hf) {
        tN[c][hf] = (8 * qq + q) * 128 + ((2 * (c ^ f) + (pp >> 1)) << 4) + 8 * (pp & 1) + hf * 65536; tie_v(tN[c][hf]); }); });
    }
    f32x4 y[4];
    u32x4 nf[2][4];
    auto n_rd = [&](int t, int c) {
      const u32 o = tN[c][(t >> 4) & 1] + ((t >> 2) & 3) * 16384 + (t & 3) * 4096;
      nf[t & 1][c] = lds_tr(sm, o, o + 512);
    };
    sfor<4>([&](auto c) { n_rd(0, c); });
    sfor<4>([&](auto q) { ld_v4<0>(gbg[q], (2560 + nt * 64 + 2 * l31) * 8, rW, WS_MODG + q * 3072 * 8); });
    sfor<56>([&](auto tc) {
      constexpr int t = tc, i = t >> 2, s4 = t & 3;
      SCHED();
      if constexpr (t == 28) {
        for (u32 sp = 0; sp < SPIN_CAP; ++sp) {
          u64 fb = __hip_atomic_load(pF4B, RLX_AGENT);
          if (__all(fb == tag)) break;
          __builtin_amdgcn_s_sleep(POLL_SLEEP);
        }
        asm volatile("" ::: "memory");
        STAMP(11); FS(59);
        sfor<P5_PRE>([&](auto s) { aa_ld(ic<28 + s>{}); });
      }
      wait_a<S5.wait[t] - 1 - S5.posa[t] + (t < P5_PRE ? 4 : 0)>(aa[t]);
      sfor<4>([&](auto mc) {
        constexpr int m = mc;
        if constexpr (m == 2 && s4 == 3 && t < 27) set_m0<(i & 7) * 16384>(wl);
        m16_va<t == 0>(y[m], nf[t & 1][m], aa[t]);
        SCHED();
        if constexpr (s4 != 3) n_rd(t + 1, m);
        if constexpr (m == 1 && s4 == 3 && i < 13) { bar_vm<63, (i >= 7 ? 301 + i : 0)>(); FS(60 + i); n_rd(t + 1, 0); n_rd(t + 1, 1); }
        if constexpr (m >= 2 && s4 == 3 && t < 55) n_rd(t + 1, m);
        if constexpr (m == 1 && (t % 28) + P5_PRE < 28) aa_ld(ic<t + P5_PRE>{});
        if constexpr (m == 3 && t >= 3 && t < 27) n_dma(ic<8 + ((t - 3) >> 2)>{}, ic<((t - 3) & 3)>{});
        SCHED();
      });
    });
    STAMP(12); FS(73);
    sfor<4>([&](auto q) { tie_v(gbg[q]); });
    {
      for (u32 sp = 0; sp < SPIN_CAP; ++sp) {
        bool ok = true;
        sfor<4>([&](auto q) { ok = ok && gbg[q][1] == tag32 && gbg[q][3] == tag32; });
        if (__all(ok)) break;
        sfor<4>([&](auto q) { ld_v4<0>(gbg[q], (2560 + nt * 64 + 2 * l31) * 8, rW, WS_MODG + q * 3072 * 8); });
        wait_vm<0>();
        sfor<4>([&](auto q) { tie_v(gbg[q]); });
      }
      f32x2 mv;
      sfor<2>([&](auto o) {
        u32 a = gbg[0][2 * o], b = gbg[1][2 * o], c = gbg[2][2 * o], d = gbg[3][2 * o];
        mv[+o] = (__uint_as_float(a) + __uint_as_float(b)) + (__uint_as_float(c) + __uint_as_float(d)); });
      *(LAS f32x2*)(sm + lM + (L_RS - L_MODV) + w * 512 + l31 * 8) = mv;
    }
    m16_fence(y);
    u32 pk[4][2];
    sfor<4>([&](auto c) {
      f32x4 m = x1[c] + ldsf4(sm, lM + (L_RS - L_MODV) + w * 512 + (c * 16 + qq * 4) * 4) * y[c];
      pk[c][0] = pack2(m[0], m[1]); pk[c][1] = pack2(m[2], m[3]);
    });
    const rsrc_t rO = mk_rsrc(out, S * D * 2);
    sfor<2>([&](auto p) {
      __builtin_amdgcn_raw_buffer_store_b128(chunk_swap16(pk[2 * p][0], pk[2 * p][1], pk[2 * p + 1][0], pk[2 * p + 1][1]), rO,
                                             l15 * 1024 + (qq & 1) * 32 + (qq >> 1) * 16 + p * 64, (tb * 64 + w * 16) * 1024 + nt * 128, 0);
    });
  }
  STAMP(13); FS(74);
}

extern "C" void kernel_launch(void* const* d_in, const int* in_sizes, int n_in, void* d_out, int out_size, void* d_ws,
                              size_t ws_size, hipStream_t stream) {
  (void)in_sizes; (void)n_in; (void)out_size; (void)ws_size;
  (void)hipFuncSetAttribute((const void*)mk_block, hipFuncAttributeMaxDynamicSharedMemorySize, LDS_BYTES);
  mk_block<<<256, 256, LDS_BYTES, stream>>>((const u16*)d_in[0], (const u16*)d_in[1], (const u16*)d_in[2], (const u16*)d_in[3],
                                            (const u16*)d_in[4], (const u16*)d_in[6], (const u16*)d_in[7], (const u16*)d_in[8],
                                            (const u16*)d_in[9], (const u16*)d_in[10], (u16*)d_out, (char*)d_ws);
}
```

```cpp
#include <hip/hip_runtime.h>
#include <utility>
#pragma clang diagnostic ignored "-Winline-asm"

typedef unsigned short u16;
typedef unsigned u32;
typedef unsigned long long u64;
typedef float f32x2 __attribute__((ext_vector_type(2)));
typedef float f32x4 __attribute__((ext_vector_type(4)));
typedef float f32x16 __attribute__((ext_vector_type(16)));
typedef unsigned u32x2 __attribute__((ext_vector_type(2)));
typedef unsigned u32x4 __attribute__((ext_vector_type(4)));
typedef short s16x4 __attribute__((ext_vector_type(4)));
typedef short s16x8 __attribute__((ext_vector_type(8)));
typedef __bf16 bf16x2 __attribute__((ext_vector_type(2)));
typedef __bf16 bf16x8 __attribute__((ext_vector_type(8)));
typedef __amdgpu_buffer_rsrc_t rsrc_t;
#define LAS __attribute__((address_space(3)))
#define GAS __attribute__((address_space(1)))
#define DI __device__ __forceinline__
#define RLX_AGENT __ATOMIC_RELAXED, __HIP_MEMORY_SCOPE_AGENT

constexpr int S = 2048, D = 512, F = 1792;
constexpr u32 WS_XN = 0;
constexpr u32 WS_PQ = WS_XN + (2u << 20);
constexpr u32 WS_X1 = WS_PQ + (8u << 20);
constexpr u32 WS_ACT = WS_X1 + (2u << 20);
constexpr u32 WS_RS = WS_ACT + (7u << 20);
constexpr u32 WS_ST = WS_RS + (256u << 10);
constexpr u32 WS_MODG = WS_ST + (256u << 10);
constexpr u32 WS_VG = WS_MODG + 4 * 3072 * 8;
constexpr u32 WS_UVG = WS_VG + 1024 * 8;
constexpr u32 WS_F1 = WS_UVG + 4 * 2 * 3584 * 8;
constexpr u32 WS_F2 = WS_F1 + 8 * 256 * 128;
constexpr u32 WS_F3 = WS_F2 + 256 * 128;
constexpr u32 WS_F4A = WS_F3 + 256 * 128;
constexpr u32 WS_F4B = WS_F4A + 256 * 128;
constexpr u32 WS_GEN = WS_F4B + 256 * 128;
constexpr u32 WS_TS = WS_GEN + 256;
constexpr u32 WS_TS2 = WS_TS + 256 * 16 * 8;
constexpr u32 WS_END = WS_TS2 + 256 * 256 * 4;
constexpr u32 L_EX = 128 * 1024;
constexpr u32 L_MODV = 144 * 1024;
constexpr u32 L_UV = L_MODV + 4096;
constexpr u32 L_UVP = L_UV + 4096;
constexpr u32 L_MP = L_UVP + 2048;
constexpr u32 L_RS = L_MP + 1024;
constexpr u32 L_CNT = L_RS + 2048;
constexpr u32 LDS_BYTES = 160 * 1024;
constexpr u32 SPIN_CAP = 200000;
#define POLL_SLEEP 2

template <class Fn, int... Is> DI void sfor_impl(Fn&& f, std::integer_sequence<int, Is...>) { (f(std::integral_constant<int, Is>{}), ...); }
template <int V> using ic = std::integral_constant<int, V>;
template <int N, class Fn> DI void sfor(Fn&& f) { sfor_impl(f, std::make_integer_sequence<int, N>{}); }

DI rsrc_t mk_rsrc(const void* p, u32 bytes) { return __builtin_amdgcn_make_buffer_rsrc((void*)p, 0, bytes, 0x00020000); }
DI float b_lo(u32 v) { return __uint_as_float(v << 16); }
DI float b_hi(u32 v) { return __uint_as_float(v & 0xffff0000u); }
DI u32 pack2(float a, float b) { f32x2 p = {a, b}; bf16x2 r = __builtin_convertvector(p, bf16x2); return __builtin_bit_cast(u32, r); }

template <int IMM> DI void ld_a(u32x4& d, u32 voff, rsrc_t r, u32 soff) {
  asm volatile("buffer_load_dwordx4 %0, %1, %2, %3 offen offset:%c4 sc1" : "=a"(d) : "v"(voff), "s"(r), "s"(soff), "i"(IMM));
}
template <int IMM> DI void ld_v4(u32x4& d, u32 voff, rsrc_t r, u32 soff) {
  asm volatile("buffer_load_dwordx4 %0, %1, %2, %3 offen offset:%c4 sc1" : "=v"(d) : "v"(voff), "s"(r), "s"(soff), "i"(IMM));
}
template <int IMM> DI void ld_v4p(u32x4& d, u32 voff, rsrc_t r, u32 soff) {
  asm volatile("buffer_load_dwordx4 %0, %1, %2, %3 offen offset:%c4" : "=v"(d) : "v"(voff), "s"(r), "s"(soff), "i"(IMM));
}
template <int IMM> DI void ld_v2(u32x2& d, u32 voff, rsrc_t r, u32 soff) {
  asm volatile("buffer_load_dwordx2 %0, %1, %2, %3 offen offset:%c4 sc1" : "=v"(d) : "v"(voff), "s"(r), "s"(soff), "i"(IMM));
}
template <int IMM> DI void ld_v2p(u32x2& d, u32 voff, rsrc_t r, u32 soff) {
  asm volatile("buffer_load_dwordx2 %0, %1, %2, %3 offen offset:%c4" : "=v"(d) : "v"(voff), "s"(r), "s"(soff), "i"(IMM));
}
template <int IMM> DI void ld_v1(u32& d, u32 voff, rsrc_t r, u32 soff) {
  asm volatile("buffer_load_dword %0, %1, %2, %3 offen offset:%c4 sc1" : "=v"(d) : "v"(voff), "s"(r), "s"(soff), "i"(IMM));
}
template <int IMM> DI void ld_u16p(u32& d, u32 voff, rsrc_t r, u32 soff) {
  asm volatile("buffer_load_ushort %0, %1, %2, %3 offen offset:%c4" : "=v"(d) : "v"(voff), "s"(r), "s"(soff), "i"(IMM));
}
template <int IMM> DI void set_m0(u32 base) { asm volatile("s_add_u32 m0, %0, %c1" :: "s"(base), "i"(IMM) : "m0", "scc"); }
template <int IMM> DI void set_m0n(u32 base) { asm volatile("s_add_u32 m0, %0, %c1\n\ts_nop 0" :: "s"(base), "i"(IMM) : "m0", "scc"); }
template <int ID, int OFF> DI void dma(u32 voff, rsrc_t r, u32 soff) {
  asm volatile("buffer_load_dwordx4 %0, %1, %2 offen offset:%c3 lds ; ID %c4" :: "v"(voff), "s"(r), "s"(soff), "i"(OFF), "i"(ID));
}
template <int ID, int OFF = 0> DI void pub16(u32x4 v, rsrc_t r, u32 voff, u32 soff) {
  asm volatile("buffer_store_dwordx4 %0, %1, %2, %3 offen offset:%c5 sc1 ; ID %c4\n\ts_nop 1" :: "v"(v), "v"(voff), "s"(r), "s"(soff), "i"(ID), "i"(OFF));
}
template <int ID> DI void pub8(u32x2 v, rsrc_t r, u32 voff, u32 soff) {
  asm volatile("buffer_store_dwordx2 %0, %1, %2, %3 offen sc1 ; ID %c4" :: "v"(v), "v"(voff), "s"(r), "s"(soff), "i"(ID));
}
template <int ID> DI void pub4(u32 v, rsrc_t r, u32 voff, u32 soff) {
  asm volatile("buffer_store_dword %0, %1, %2, %3 offen sc1 ; ID %c4" :: "v"(v), "v"(voff), "s"(r), "s"(soff), "i"(ID));
}
template <int N, int A = 0> DI void wait_vm() { static_assert(N >= 0 && N < 64, ""); asm volatile("s_waitcnt vmcnt(%c0) ; NEED %c1" :: "i"(N), "i"(A) : "memory"); }
template <int N> DI void wait_a(u32x4& a) { static_assert(N >= 0, ""); asm volatile("s_waitcnt vmcnt(%c1)" : "+a"(a) : "i"(N < 63 ? N : 63)); }
DI void tie_a(u32x4& a) { asm volatile("" : "+a"(a)); }
template <class T> DI void tie_v(T& a) { asm volatile("" : "+v"(a)); }
template <int N, int A = 0, int B = 0> DI void bar_vm() {
  static_assert(N >= 0, "");
  asm volatile("s_waitcnt vmcnt(%c0) lgkmcnt(0) ; NEED %c1 NEED %c2\n\ts_barrier" :: "i"(N < 63 ? N : 63), "i"(A), "i"(B) : "memory");
}
DI void bar() { asm volatile("s_waitcnt lgkmcnt(0)\n\ts_barrier" ::: "memory"); }
#define SCHED() __builtin_amdgcn_sched_barrier(0)

DI void st16(u32x4 v, rsrc_t r, u32 voff, u32 soff) { __builtin_amdgcn_raw_buffer_store_b128(v, r, voff, soff, 16); }
DI void st8(u32x2 v, rsrc_t r, u32 voff, u32 soff) { __builtin_amdgcn_raw_buffer_store_b64(v, r, voff, soff, 16); }
DI void st4(u32 v, rsrc_t r, u32 voff, u32 soff) { __builtin_amdgcn_raw_buffer_store_b32(v, r, voff, soff, 16); }

DI f32x16 mfma(u32x4 a, u32x4 b, f32x16 c) {
  return __builtin_amdgcn_mfma_f32_32x32x16_bf16(__builtin_bit_cast(bf16x8, a), __builtin_bit_cast(bf16x8, b), c, 0, 0, 0);
}
template <bool Z> DI void mm_va(f32x16& c, u32x4 a, u32x4 b) {
  if constexpr (Z) asm volatile("v_mfma_f32_32x32x16_bf16 %0, %1, %2, 0" : "=&v"(c) : "v"(a), "a"(b));
  else asm volatile("v_mfma_f32_32x32x16_bf16 %0, %1, %2, %0" : "+v"(c) : "v"(a), "a"(b));
}
template <bool Z> DI void mm_av(f32x16& c, u32x4 a, u32x4 b) {
  if constexpr (Z) asm volatile("v_mfma_f32_32x32x16_bf16 %0, %1, %2, 0" : "=&v"(c) : "a"(a), "v"(b));
  else asm volatile("v_mfma_f32_32x32x16_bf16 %0, %1, %2, %0" : "+v"(c) : "a"(a), "v"(b));
}
template <bool Z> DI void mm_vv(f32x16& c, u32x4 a, u32x4 b) {
  if constexpr (Z) asm volatile("v_mfma_f32_32x32x16_bf16 %0, %1, %2, 0" : "=&v"(c) : "v"(a), "v"(b));
  else asm volatile("v_mfma_f32_32x32x16_bf16 %0, %1, %2, %0" : "+v"(c) : "v"(a), "v"(b));
}
template <bool Z> DI void m16_va(f32x4& c, u32x4 a, u32x4 b) {
  if constexpr (Z) asm volatile("v_mfma_f32_16x16x32_bf16 %0, %1, %2, 0" : "=&v"(c) : "v"(a), "a"(b));
  else asm volatile("v_mfma_f32_16x16x32_bf16 %0, %1, %2, %0" : "+v"(c) : "v"(a), "a"(b));
}
DI void m16_fence(f32x4 (&y)[4]) { asm volatile("s_nop 11" : "+v"(y[0]), "+v"(y[1]), "+v"(y[2]), "+v"(y[3])); }
DI void mm_fence(f32x16& a, f32x16& b) { asm volatile("s_nop 15" : "+v"(a), "+v"(b)); }
DI void mm_tie(f32x16& a, f32x16& b) { asm volatile("" : "+v"(a), "+v"(b)); }
DI float smul(float a, float b) { float r; asm("v_mul_f32_e32 %0, %1, %2" : "=v"(r) : "v"(a), "v"(b)); return r; }
DI float sfma(float a, float b, float c) { float r; asm("v_fma_f32 %0, %1, %2, %3" : "=v"(r) : "v"(a), "v"(b), "v"(c)); return r; }
DI u32x4 lds16(LAS char* sm, u32 off) { return *(LAS u32x4*)(sm + off); }
DI f32x4 ldsf4(LAS char* sm, u32 off) { return *(LAS f32x4*)(sm + off); }
DI u32x4 lds_tr(LAS char* sm, u32 off0, u32 off1) {
  s16x4 lo = __builtin_amdgcn_ds_read_tr16_b64_v4i16((LAS s16x4*)(sm + off0));
  s16x4 hi = __builtin_amdgcn_ds_read_tr16_b64_v4i16((LAS s16x4*)(sm + off1));
  s16x8 w = __builtin_shufflevector(lo, hi, 0, 1, 2, 3, 4, 5, 6, 7);
  return __builtin_bit_cast(u32x4, w);
}
template <int CTRL> DI float dpp(float v) { return __int_as_float(__builtin_amdgcn_update_dpp(0, __float_as_int(v), CTRL, 0xF, 0xF, true)); }
DI float add_x16(float v) { auto r = __builtin_amdgcn_permlane16_swap(__float_as_uint(v), __float_as_uint(v), false, false); u32 a = r[0], b = r[1]; return __uint_as_float(a) + __uint_as_float(b); }
DI float add_x32(float v) { auto r = __builtin_amdgcn_permlane32_swap(__float_as_uint(v), __float_as_uint(v), false, false); u32 a = r[0], b = r[1]; return __uint_as_float(a) + __uint_as_float(b); }
DI float red_x32(float a, float b) { auto r = __builtin_amdgcn_permlane32_swap(__float_as_uint(a), __float_as_uint(b), false, false); u32 p = r[0], q = r[1]; return __uint_as_float(p) + __uint_as_float(q); }
DI float red_x16(float a, float b) { auto r = __builtin_amdgcn_permlane16_swap(__float_as_uint(a), __float_as_uint(b), false, false); u32 p = r[0], q = r[1]; return __uint_as_float(p) + __uint_as_float(q); }
DI float red_x8(float a, float b, bool hi) { float keep = hi ? b : a, give = hi ? a : b; return keep + dpp<0x128>(give); }
DI float wave_sum(float v) {
  v += dpp<0xB1>(v); v += dpp<0x4E>(v); v += dpp<0x141>(v); v += dpp<0x140>(v);
  return add_x32(add_x16(v));
}
DI u32x4 chunk_swap(u32 a0, u32 a1, u32 b0, u32 b1) {
  auto r0 = __builtin_amdgcn_permlane32_swap(a0, b0, false, false);
  auto r1 = __builtin_amdgcn_permlane32_swap(a1, b1, false, false);
  u32x4 o; o[0] = r0[0]; o[1] = r1[0]; o[2] = r0[1]; o[3] = r1[1];
  return o;
}
DI u32x4 chunk_swap16(u32 a0, u32 a1, u32 b0, u32 b1) {
  auto r0 = __builtin_amdgcn_permlane16_swap(a0, b0, false, false);
  auto r1 = __builtin_amdgcn_permlane16_swap(a1, b1, false, false);
  u32x4 o; o[0] = r0[0]; o[1] = r1[0]; o[2] = r0[1]; o[3] = r1[1];
  return o;
}
template <int NF> DI void poll_flags(const char* p, u64 tag, int lane) {
  const GAS u64* q = (const GAS u64*)p + (lane < NF ? lane : 0) * 16;
  for (u32 sp = 0; sp < SPIN_CAP; ++sp) {
    u64 v = __hip_atomic_load(q, RLX_AGENT);
    if (__all(v == tag)) break;
    __builtin_amdgcn_s_sleep(POLL_SLEEP);
  }
  asm volatile("" ::: "memory");
}
DI void post_flag(char* p, u64 tag) { __hip_atomic_store((GAS u64*)p, tag, RLX_AGENT); }

#define AMPK 1
#define STAMP(i) do { if ((i) == 0) amp0 = __builtin_amdgcn_s_memrealtime(); if ((i) == AMPK) amp1 = __builtin_amdgcn_s_memrealtime(); if ((i) == 13) { u64 d_ = (amp1 - amp0) * 20, t_ = __builtin_amdgcn_s_memrealtime(); if (bid == AMPB) while (__builtin_amdgcn_s_memrealtime() - t_ < d_) __builtin_amdgcn_s_sleep(4); } } while (0)
#define AMPB 9
#define FS(i) do {} while (0)

#define NINIT 6
#define NW1 20
#define ND1 4
constexpr int P1_MODC = 1, P1_MODLD = 8, P1_MODUSE = 13, P1_V = 14, P1_XR = 10;
constexpr int p1_extra(int k) { return (k == P1_MODLD || k == P1_MODLD + 1) ? 4 : k == P1_XR ? 2 : 0; }
struct Sched1 { int posx[16][4], posa[24], pose, posm, top[16][4], bar[16], ext[16], pre; };
constexpr Sched1 mk_sched1() {
  Sched1 s{}; int n = 2;
  for (int i = 0; i < NINIT; ++i) s.posa[i] = -1;
  for (int k = 0; k < 3; ++k) {
    for (int i = 0; i < 4; ++i) s.posx[k][i] = n++;
    if (k == 1) { n += 6; s.pose = n - 1; }
  }
  for (int st = NINIT; st < 8; ++st) { n += 4; s.posa[st] = n - 1; }
  s.pre = n;
  for (int k = 0; k < 16; ++k)
    for (int i = 0; i < 4; ++i) {
      s.top[k][i] = n;
      const int g = 4 * k + i - 3;
      if (i == 3) s.bar[k] = n;
      if (i == 3) { s.ext[k] = n; n += p1_extra(k); if (k == P1_MODLD + 1) s.posm = n - 1; }
      if (g >= 0 && (g >> 2) < 8 + ND1) s.posa[8 + (g >> 2)] = n++;
      if (k + 3 < 16) s.posx[k + 3][i] = n++;
    }
  return s;
}
constexpr Sched1 S1 = mk_sched1();
#define NG0 6
constexpr int P4_PRE = 8;
constexpr int P4_E0 = 34;
constexpr int P4_UG = 24, P4_UR = 31;
struct Sched4 { int posx[32], posg[24], posug, possto, wait[64], bar[16], urw, end; };
constexpr int p4_piece(int t, int m) { return (t >= P4_E0 && m < 3 && (t - P4_E0) * 3 + m < 80) ? (t - P4_E0) * 3 + m : -1; }
constexpr Sched4 mk_sched4() {
  Sched4 s{}; int n = 0;
  for (int i = 0; i < P4_PRE; ++i) s.posx[i] = n++;
  for (int t = 0; t < 64; ++t) {
    s.wait[t] = n;
    for (int m = 0; m < 4; ++m) {
      if (m == 0 && t == P4_UR) s.urw = n;
      if (m == 0 && t < 4 * (8 - NG0)) s.posg[NG0 + (t >> 2)] = n++;
      if (p4_piece(t, m) >= 0 && p4_piece(t, m) % 20 == 19) s.possto = n++;
      if (m == 1 && (t & 3) == 3) s.bar[t >> 2] = n;
      if (m == 1) { if (t + P4_PRE < 32) s.posx[t + P4_PRE] = n++; else if (t >= P4_UG && t < P4_UG + 4) s.posug = n++; }
      if (m == 3 && t >= 3) s.posg[8 + ((t - 3) >> 2)] = n++;
    }
  }
  s.end = n;
  return s;
}
constexpr Sched4 S4 = mk_sched4();
constexpr int P5_PRE = 10;
struct Sched5 { int posa[56], wait[56], end; };
constexpr Sched5 mk_sched5() {
  Sched5 s{}; int n = 0;
  for (int i = 0; i < P5_PRE; ++i) s.posa[i] = n++;
  for (int t = 0; t < 56; ++t) {
    if (t == 28) for (int i = 0; i < P5_PRE; ++i) s.posa[28 + i] = n++;
    s.wait[t] = n;
    if ((t % 28) + P5_PRE < 28) s.posa[t + P5_PRE] = n++;
    if (t >= 3 && t < 27) n++;
  }
  s.end = n;
  return s;
}
constexpr Sched5 S5 = mk_sched5();

struct Ge { float g0, g1, a0, a1, m0, m1; };
DI void ge_a(Ge& e, float q0, float q1, float r) {
  asm volatile("v_mul_f32 %0, %4, %5\n\tv_mul_f32 %1, %4, %6\n\tv_mul_f32 %2, %0, %0\n\tv_mul_f32 %3, %1, %1"
               : "=&v"(e.g0), "=&v"(e.g1), "=&v"(e.a0), "=&v"(e.a1) : "v"(r), "v"(q0), "v"(q1));
}
DI void ge_b(Ge& e, float c3, float c1) {
  asm volatile("v_fma_f32 %0, %0, %4, %5\n\tv_fma_f32 %1, %1, %4, %5\n\tv_mul_f32 %0, %0, %2\n\tv_mul_f32 %1, %1, %3"
               : "+v"(e.a0), "+v"(e.a1) : "v"(e.g0), "v"(e.g1), "v"(c3), "v"(c1));
}
DI void ge_c(Ge& e, float u0, float u1) {
  asm volatile("v_exp_f32 %0, %0\n\tv_exp_f32 %1, %1\n\tv_mul_f32 %2, %4, %6\n\tv_mul_f32 %3, %5, %7"
               : "+v"(e.a0), "+v"(e.a1), "=&v"(e.m0), "=&v"(e.m1) : "v"(e.g0), "v"(e.g1), "v"(u0), "v"(u1));
}
DI void ge_d(Ge& e, float sd) {
  asm volatile("v_fma_f32 %0, %0, %2, %2\n\tv_fma_f32 %1, %1, %2, %2\n\tv_rcp_f32 %0, %0\n\tv_rcp_f32 %1, %1\n\ts_nop 0"
               : "+v"(e.a0), "+v"(e.a1) : "v"(sd));
}
DI u32 ge_e(Ge& e) {
  u32 o;
  asm volatile("v_mul_f32 %1, %1, %3\n\tv_mul_f32 %2, %2, %4\n\tv_cvt_pk_bf16_f32 %0, %1, %2"
               : "=v"(o), "+v"(e.m0), "+v"(e.m1) : "v"(e.a0), "v"(e.a1));
  return o;
}

__global__ void __launch_bounds__(256, 1)
mk_block(const u16* __restrict__ x, const u16* __restrict__ Sre, const u16* __restrict__ Sim, const u16* __restrict__ Dre,
         const u16* __restrict__ Dim, const u16* __restrict__ Wg, const u16* __restrict__ Wu, const u16* __restrict__ Wn,
         const u16* __restrict__ cvec, const u16* __restrict__ wmod, u16* __restrict__ out, char* __restrict__ ws) {
  extern __shared__ __attribute__((aligned(16))) char smem_[];
  LAS char* sm = (LAS char*)smem_;
  u64 amp0 = 0, amp1 = 0;
  const int tid = threadIdx.x, lane = tid & 63, w = __builtin_amdgcn_readfirstlane(tid >> 6);
  const int l31 = lane & 31, hh = lane >> 5, l15 = lane & 15, qq = lane >> 4;
  const int bid = blockIdx.x, xcd = bid & 7, loc = bid >> 3;
  const int tb = xcd * 4 + (loc >> 3), nt = loc & 7;
  const int cb = nt & 3, kh = nt >> 2;
  const int rb = (xcd >> 1) * 4 + loc / 7, ft = (xcd & 1) * 7 + loc % 7;
  const int w1 = w & 1, wk = w >> 1;
  const u32 lane16 = lane * 16, wl = w * 4096;
  const rsrc_t rX = mk_rsrc(x, S * D * 2);
  u32x4 xr[2];
  ld_v4p<0>(xr[0], lane16, rX, (bid * 8 + w * 2) * 1024);
  ld_v4p<1024>(xr[1], lane16, rX, (bid * 8 + w * 2) * 1024);
  STAMP(0); FS(90);
  const rsrc_t rW = mk_rsrc(ws, WS_END);
  u32 vA[4];
  sfor<4>([&](auto i) { vA[i] = (lane >> 3) * 4096 + (((lane & 7) ^ ((i & 1) * 4 + qq)) << 4) + i * (8 * 4096 - 1024); });
  auto kblk = [&](int st) { return kh * 16 + ((cb * 4 + st) & 15); };
  u64 tag;

  {
    const rsrc_t r0 = mk_rsrc(w1 == 0 ? Sre : Sim, S * S * 2);
    auto fill = [&](auto qc) {
      constexpr int q = qc, st = q >> 3, hf = (q >> 2) & 1, p = q & 3;
      if constexpr (p == 0) set_m0n<st * 16384 + hf * 4096>(w1 * 8192);
      dma<100 + st, p * 1024>(vA[p], r0, (tb * 64 + hf * 32) * 4096 + kblk(st) * 128);
    };
    asm volatile("s_load_dwordx2 %0, %1, 0x0" : "=s"(tag) : "s"(ws + WS_GEN));
    if (w >= 2) { sfor<NW1>([&](auto q) { fill(q); }); wait_vm<NW1>(); } else { wait_vm<0>(); }
    asm volatile("s_waitcnt lgkmcnt(0)" : "+s"(tag));
    tag += 1;
    *(LAS u32*)(sm + L_CNT) = 0;
    tie_v(xr[0]); tie_v(xr[1]);
    FS(91);
    float v[2][8], mu[2], rs[2];
    sfor<2>([&](auto i) {
      sfor<4>([&](auto j) { u32 d = xr[i][+j]; v[i][2 * j] = b_lo(d); v[i][2 * j + 1] = b_hi(d); });
      mu[i] = ((v[i][0] + v[i][1]) + (v[i][2] + v[i][3])) + ((v[i][4] + v[i][5]) + (v[i][6] + v[i][7]));
    });
    sfor<2>([&](auto i) { mu[i] = wave_sum(mu[i]) * (1.f / D); });
    sfor<2>([&](auto i) {
      float s = 0.f;
      sfor<8>([&](auto j) { v[i][j] -= mu[i]; s += v[i][j] * v[i][j]; });
      rs[i] = s;
    });
    sfor<2>([&](auto i) { rs[i] = __builtin_amdgcn_rsqf(wave_sum(rs[i]) * (1.f / D) + 1e-5f); });
    sfor<2>([&](auto i) {
      u32x4 o;
      sfor<4>([&](auto j) { o[+j] = pack2(v[i][2 * j] * rs[i], v[i][2 * j + 1] * rs[i]); });
      *(LAS u32x4*)(sm + L_EX + (w * 2 + i) * 1088 + lane16) = o;
    });
    bar();
    FS(92);
    if (w < 2) {
      const u32 q = (lane & 15) >> 2, pp = lane & 3;
      const u32 ta = L_EX + q * 1088 + (w * 256 + qq * 16 + pp * 4) * 2;
      sfor<4>([&](auto m) {
        u32x4 o = lds_tr(sm, ta + m * 128, ta + m * 128 + 4 * 1088);
        pub16<1>(o, rW, hh * 1024 + l31 * 16, WS_XN + (bid >> 1) * 16384 + (w * 8 + m * 2) * 1024 + (bid & 1) * 512);
      });
      bar();
      wait_vm<0, 1>();
      FS(93);
      u32 old = 0;
      if (lane == 0) old = __hip_atomic_fetch_add((LAS u32*)(sm + L_CNT), 1u, __ATOMIC_RELAXED, __HIP_MEMORY_SCOPE_WORKGROUP);
      old = __builtin_amdgcn_readfirstlane(old);
      if (old == 1 && lane < 8) post_flag(ws + WS_F1 + (lane * 256 + bid) * 128, tag);
      STAMP(14);
    } else {
      bar();
      sfor<NINIT * 8 - NW1>([&](auto q) { fill(ic<NW1 + q>{}); });
    }
  }
  const u32 tag32 = (u32)tag;
  const u32 sF = WS_F1 + (xcd * 256 + kh * 128) * 128;
  const u32 vF0 = ((cb * 32 + l31) & 127) * 128, vF1 = ((cb * 32 + 32 + lane) & 127) * 128, vF2 = ((cb * 32 + 96 + l31) & 127) * 128;
  {
    const GAS u64* q = (const GAS u64*)(ws + sF + vF0);
    for (u32 sp = 0; sp < SPIN_CAP; ++sp) {
      u64 a = __hip_atomic_load(q, RLX_AGENT);
      if (__all(a == tag)) break;
      __builtin_amdgcn_s_sleep(POLL_SLEEP);
    }
    asm volatile("" ::: "memory");
  }
  u32x2 fl[2];
  ld_v2<0>(fl[0], vF1, rW, sF);
  ld_v2<0>(fl[1], vF2, rW, sF);
  STAMP(1); FS(94);

  const rsrc_t rS = mk_rsrc(w < 2 ? Sre : Sim, S * S * 2);
  const rsrc_t rD = mk_rsrc(w < 2 ? Dre : Dim, D * D * 2);
  u32 vD[4], vX[4];
  sfor<4>([&](auto i) { vD[i] = (lane >> 3) * 1024 + (((lane & 7) ^ ((i & 1) * 4 + qq)) << 4) + i * (8 * 1024 - 1024); vX[i] = lane16 + i * 16384; });
  const u32 sA = (tb * 64 + w1 * 32) * 4096, sD = (nt * 64 + w1 * 32) * 1024;
  const u32 sXN = WS_XN + (cb * 4 + w) * 1024;
  auto a_dma = [&](auto stc, auto pc) { constexpr int st = stc, p = pc; dma<100 + st, p * 1024>(vA[p], rS, sA + kblk(st) * 128); };
  auto d_dma = [&](auto sc_, auto pc) { constexpr int s = sc_, p = pc; dma<2, p * 1024>(vD[p], rD, sD + s * 128); };
  u32x4 xw[4][4];
  auto xn_ld = [&](auto stc, auto ic_) { constexpr int st = stc, i = ic_; ld_a<0>(xw[st & 3][i], vX[i], rW, sXN + kblk(st) * 65536); };
  u32x4 mg[8], gvd, wm[4], xres[2];
  u32x2 cr;
  const int ukq = bid < 224 ? bid / 56 : 3, ucb = bid % 56;
  const int mkq = bid < 192 ? bid / 48 : 3, mcb = bid % 48;
  sfor<3>([&](auto kc) {
    sfor<4>([&](auto i) { xn_ld(kc, i); });
    if constexpr (kc == 1) {
      const rsrc_t rM = mk_rsrc(wmod, D * 6 * D * 2);
      const rsrc_t rC = mk_rsrc(cvec, D * 2);
      sfor<4>([&](auto r) { ld_v4p<0>(wm[r], (lane >> 3) * 4 * 6144 + (lane & 7) * 16, rM, (mkq * 128 + w * 32 + r) * 6144 + mcb * 128); });
      ld_v2p<0>(cr, (lane >> 3) * 8, rC, (mkq * 128 + w * 32) * 2);
      const rsrc_t rV = mk_rsrc(bid < 128 ? Dre : Dim, D * D * 2);
      ld_v4p<0>(gvd, lane16, rV, ((bid & 127) * 4 + w) * 1024);
    }
  });
  sfor<8 - NINIT>([&](auto s) { set_m0n<(NINIT + s) * 16384>(wl); sfor<4>([&](auto p) { a_dma(ic<NINIT + s>{}, p); }); });

  u32 aB[4][2];
  sfor<4>([&](auto i) { sfor<2>([&](auto hf) { aB[i][hf] = l31 * 128 + (((2 * i + hh) ^ ((l31 >> 1) & 7)) << 4) + hf * 65536; tie_v(aB[i][hf]); }); });
  u32x4 af[2][4], rf;
  auto a_rd = [&](int t, int j) { af[t & 1][j] = lds16(sm, aB[t & 3][(t >> 4) & 1] + ((t >> 2) & 3) * 16384 + j * 4096); };
  f32x16 acc[4], rsa;
  u32x4 ones = {0x3f803f80u, 0x3f803f80u, 0x3f803f80u, 0x3f803f80u};
  tie_v(ones);
  const bool h0 = (lane >> 3) & 1;
  u32 lM = L_MODV; tie_v(lM);
  const u32 vM2 = (w == 0 ? 512 + cb * 128 + 2 * lane : w == 1 ? 1024 + hh * 1024 + nt * 64 + 2 * l31 : (w == 2 ? 1536 : 2048) + ukq * 128 + 2 * lane) * 8;
  float ma[8];
  bar_vm<S1.pre - 1 - S1.posx[0][0], 100>();
  sfor<4>([&](auto j) { a_rd(0, j); });
  rf = lds16(sm, aB[0][0] + wl);
  tie_v(fl[0]); tie_v(fl[1]);
  for (u32 sp = 0; sp < SPIN_CAP; ++sp) {
    const u32 th = (u32)(tag >> 32);
    if (__all(fl[0][0] == tag32 && fl[0][1] == th && fl[1][0] == tag32 && fl[1][1] == th)) break;
    __builtin_amdgcn_s_sleep(POLL_SLEEP);
    ld_v2<0>(fl[0], vF1, rW, sF);
    ld_v2<0>(fl[1], vF2, rW, sF);
    wait_vm<0>();
    tie_v(fl[0]); tie_v(fl[1]);
  }
  sfor<64>([&](auto tc) {
    constexpr int t = tc, k = t >> 2, i = t & 3, g = t - 3;
    SCHED();
    wait_a<S1.top[k][i] - 1 - S1.posx[k][i]>(xw[k & 3][i]);
    sfor<4>([&](auto mc) {
      constexpr int m = mc;
      if constexpr (m == 2 && i == 3 && k < 8 + ND1) set_m0<(k & 7) * 16384>(wl);
      mm_av<t == 0>(acc[m], xw[k & 3][i], af[t & 1][m]);
      if constexpr (t < 16 && m == 0) mm_vv<t == 0>(rsa, ones, rf);
      SCHED();
      if constexpr (i != 3) a_rd(t + 1, m);
      if constexpr (m == 1 && i == 3) {
        constexpr int na = k < 15 ? S1.bar[k] - 1 - S1.posa[k + 1] : 63;
        constexpr int ne = k == P1_MODC ? S1.bar[k] - 1 - S1.pose : k == P1_MODUSE ? S1.bar[k] - 1 - S1.posm : 63;
        bar_vm<(na < ne ? na : ne), (k < 15 ? 101 + k : 0)>();
        FS(k);
        if constexpr (t < 63) { a_rd(t + 1, 0); a_rd(t + 1, 1); }
      }
      if constexpr (m >= 2 && i == 3 && t < 63) a_rd(t + 1, m);
      if constexpr (m == 2 && t < 15) rf = lds16(sm, aB[(t + 1) & 3][0] + ((t + 1) >> 2) * 16384 + wl);
      if constexpr (m == 2 && i == 3) {
        if constexpr (k == P1_MODC || k == P1_MODC + 1) {
          if constexpr (k == P1_MODC) { sfor<4>([&](auto r) { tie_v(wm[r]); }); tie_v(cr); sfor<8>([&](auto q) { ma[q] = 0.f; }); }
          sfor<2>([&](auto rr) {
            constexpr int r = 2 * (k - P1_MODC) + rr;
            u32 cd = cr[r >> 1];
            float cv = (r & 1) ? b_hi(cd) : b_lo(cd);
            float sc = cv * __builtin_amdgcn_rcpf(1.f + __builtin_amdgcn_exp2f(-1.4426950408889634f * cv));
            sfor<4>([&](auto jj) { u32 d = wm[r][+jj]; ma[2 * jj] += sc * b_lo(d); ma[2 * jj + 1] += sc * b_hi(d); });
          });
          if constexpr (k == P1_MODC + 1) {
            float b4[4], b2[2];
            sfor<4>([&](auto jj) { b4[jj] = red_x32(ma[jj], ma[4 + jj]); });
            sfor<2>([&](auto jj) { b2[jj] = red_x16(b4[jj], b4[2 + jj]); });
            *(LAS float*)(sm + lM + (L_MP - L_MODV) + w * 256 + ((lane & 7) * 8 + (lane >> 3)) * 4) = red_x8(b2[0], b2[1], h0);
          } else {
            sfor<8>([&](auto q) { tie_v(ma[q]); });
          }
        }
        if constexpr (k == P1_MODC + 2) {
          if (w == 0 && bid < 192) {
            float s = 0.f;
            sfor<4>([&](auto r) { s += *(LAS float*)(sm + lM + (L_MP - L_MODV) + r * 256 + lane * 4); });
            u32x2 gr = {__float_as_uint(s), tag32};
            pub8<0>(gr, rW, lane * 8, WS_MODG + (mkq * 3072 + mcb * 64) * 8);
          }
        }
        if constexpr (k == P1_MODLD || k == P1_MODLD + 1) {
          sfor<2>([&](auto qc) { constexpr int q = 2 * (k - P1_MODLD) + qc;
            ld_v4<0>(mg[2 * q], tid * 16, rW, WS_MODG + q * 3072 * 8); ld_v4<0>(mg[2 * q + 1], vM2, rW, WS_MODG + q * 3072 * 8); });
        }
        if constexpr (k == P1_XR)
          sfor<2>([&](auto p) { ld_v4p<p * 64>(xres[p], l15 * 1024 + (qq & 1) * 32 + (qq >> 1) * 16, rX, (tb * 64 + w * 16) * 1024 + nt * 128); });
        if constexpr (k == P1_MODUSE) {
          sfor<8>([&](auto q) { tie_v(mg[q]); });
          for (u32 sp = 0; sp < SPIN_CAP; ++sp) {
            bool ok = true;
            sfor<8>([&](auto q) { ok = ok && mg[q][1] == tag32 && mg[q][3] == tag32; });
            if (__all(ok)) break;
            __builtin_amdgcn_s_sleep(1);
            sfor<4>([&](auto q) { ld_v4<0>(mg[2 * q], tid * 16, rW, WS_MODG + q * 3072 * 8); ld_v4<0>(mg[2 * q + 1], vM2, rW, WS_MODG + q * 3072 * 8); });
            wait_vm<0>();
            sfor<8>([&](auto q) { tie_v(mg[q]); });
          }
          sfor<2>([&](auto e) {
            f32x2 mv;
            sfor<2>([&](auto o) {
              u32 a = mg[e][2 * o], b = mg[2 + e][2 * o], c = mg[4 + e][2 * o], d = mg[6 + e][2 * o];
              mv[+o] = (__uint_as_float(a) + __uint_as_float(b)) + (__uint_as_float(c) + __uint_as_float(d)); });
            *(LAS f32x2*)(sm + lM + e * 2048 + tid * 8) = mv;
          });
        }
        if constexpr (k == P1_V) {
          tie_v(gvd);
          f32x4 s0 = ldsf4(sm, lM + lane * 32), s1 = ldsf4(sm, lM + lane * 32 + 16);
          u32 d0 = gvd[0], d1 = gvd[1], d2 = gvd[2], d3 = gvd[3];
          float v = s0[0] * b_lo(d0) + s0[1] * b_hi(d0) + s0[2] * b_lo(d1) + s0[3] * b_hi(d1) + s1[0] * b_lo(d2) + s1[1] * b_hi(d2) + s1[2] * b_lo(d3) + s1[3] * b_hi(d3);
          v = wave_sum(v);
          if (lane == 0) { u32x2 gr = {__float_as_uint(v), tag32}; pub8<0>(gr, rW, 0, WS_VG + (bid * 4 + w) * 8); }
        }
      }
      if constexpr (m == 2 && g >= 0 && (g >> 2) < 8 + ND1) {
        if constexpr ((g >> 2) < 8) a_dma(ic<8 + (g >> 2)>{}, ic<(g & 3)>{}); else d_dma(ic<(g >> 2) - 8>{}, ic<(g & 3)>{});
      }
      if constexpr (m == 3 && k + 3 < 16) xn_ld(ic<k + 3>{}, ic<i>{});
      SCHED();
    });
  });
  STAMP(2); FS(32);
  u32x4 gvw[4], warm[2];
  {
    f32x4 sc[4];
    sfor<4>([&](auto g) { sc[g] = ldsf4(sm, lM + (512 + w * 32 + g * 8 + hh * 4) * 4) + 1.f; });
    mm_fence(acc[0], acc[1]); mm_tie(acc[2], acc[3]); mm_tie(rsa, rsa);
    const u32 vP = (l31 >> 4) * 65536 + hh * 256 + l15 * 16;
    sfor<4>([&](auto jc) {
      constexpr int j = jc;
      u32 pk[4][2];
      sfor<4>([&](auto g) {
        f32x4 m = {acc[j][4 * g], acc[j][4 * g + 1], acc[j][4 * g + 2], acc[j][4 * g + 3]};
        m = j < 2 ? m * sc[g] : -m * sc[g];
        pk[g][0] = pack2(m[0], m[1]); pk[g][1] = pack2(m[2], m[3]);
      });
      sfor<2>([&](auto q) {
        pub16<3, q * 512>(chunk_swap(pk[2 * q][0], pk[2 * q][1], pk[2 * q + 1][0], pk[2 * q + 1][1]), rW, vP,
                          WS_PQ + ((tb * 4 + (j & 1) * 2) * 64 + kh * 32 + (j >> 1) * 16 + cb * 4 + w) * 1024);
      });
    });
    pub4<3>(__float_as_uint(rsa[0]), rW, l31 * 4, WS_RS + ((tb * 8 + nt) * 128 + w * 32) * 4);
    bar();
    FS(39);
    sfor<2>([&](auto s) { set_m0n<(ND1 + s) * 16384>(wl); sfor<4>([&](auto p) { d_dma(ic<ND1 + s>{}, p); }); });
    bar_vm<8, 3>();
    if (tid == 0) post_flag(ws + WS_F2 + (tb * 8 + nt) * 128, tag);
    sfor<6 - ND1>([&](auto s) { set_m0n<(ND1 + 2 + s) * 16384>(wl); sfor<4>([&](auto p) { d_dma(ic<ND1 + 2 + s>{}, p); }); });
    const rsrc_t rU = mk_rsrc(ucb < 28 ? Wg : Wu, D * F * 2);
    const u32 vu = (tid >> 3) * (F * 2) + (lane & 7) * 16;
    sfor<4>([&](auto r) { ld_v4p<0>(gvw[r], vu, rU, (ukq * 128 + r * 32) * (F * 2) + (ucb % 28) * 128); });
    const rsrc_t rN = mk_rsrc(Wn, F * D * 2);
    ld_v4p<0>(warm[0], tid * 16, rN, bid * 7168);
    ld_v4p<0>(warm[1], tid * 16, rN, bid * 7168 + 4096);
  }
  STAMP(3); FS(33);
  poll_flags<8>(ws + WS_F2 + tb * 8 * 128, tag, lane);
  if (bid == 0 && tid == 0) post_flag(ws + WS_GEN, tag);
  STAMP(4); FS(34);

  f32x4 x1[4];
  {
    constexpr int PRE = 5;
    u32x4 pa[32][2];
    const u32 so = WS_PQ + (tb * 4 + w) * 65536;
    const u32 vP1 = lane16 + 32768;
    auto pa_ld = [&](auto sc_, auto hc) {
      constexpr int s = sc_, h = hc;
      ld_a<(s & 1) * 1024>(pa[s][h], h ? vP1 : lane16, rW, so + (((s >> 2) + nt) & 7) * 2048 + ((s >> 1) & 1) * 16384);
    };
    sfor<PRE>([&](auto s) { pa_ld(s, ic<0>{}); pa_ld(s, ic<1>{}); });
    u32 rp[4];
    u32x4 vg;
    u32 dB[2];
    sfor<2>([&](auto u) { dB[u] = l15 * 128 + (((4 * u + qq) ^ ((l15 >> 1) & 7)) << 4); });
    bar_vm<2 * PRE, 2>();
    f32x4 y[4];
    u32x4 df[2][4];
    u32 da;
    auto d_ad = [&](int s) { da = dB[s & 1] + (((s >> 2) + nt) & 7) * 16384; };
    auto d_rd = [&](int s, int c) { df[s & 1][c] = lds16(sm, da + ((s >> 1) & 1) * 8192 + c * 2048); };
    d_ad(0);
    sfor<4>([&](auto c) { d_rd(0, c); });
    sfor<32>([&](auto sc_) {
      constexpr int s = sc_;
      constexpr int issued = 2 * (s + PRE) < 69 ? 2 * (s + PRE) : 69;
      SCHED();
      if constexpr (s < 31) d_ad(s + 1);
      wait_a<issued - 1 - 2 * s>(pa[s][0]);
      if constexpr ((s & 3) == 0) FS(80 + (s >> 2));
      sfor<8>([&](auto mc) {
        constexpr int m = mc, c = m & 3, h = m >> 2, n = 2 * (s + PRE) + h;
        if constexpr (m == 4) wait_a<(issued < 69 ? issued + 1 : 69) - 1 - (2 * s + 1)>(pa[s][1]);
        m16_va<s == 0 && h == 0>(y[c], df[s & 1][c], pa[s][h]);
        SCHED();
        if constexpr (h == 0 && s < 31) d_rd(s + 1, c);
        if constexpr (c == 1) {
          if constexpr (n < 64) pa_ld(ic<s + PRE>{}, ic<h>{});
          else if constexpr (n < 68) ld_v1<(n - 64) * 512>(rp[n - 64], ((qq >> 1) * 512 + (qq & 1) * 64 + w * 16 + l15) * 4, rW, WS_RS + tb * 4096);
          else if constexpr (n == 68) ld_v4<0>(vg, (hh * 512 + nt * 64 + 2 * l31) * 8, rW, WS_VG);
        }
        SCHED();
      });
    });
    STAMP(5); FS(35);
    f32x4 ga[4], cbv[4];
    sfor<4>([&](auto c) { const u32 eo = (c * 16 + qq * 4) * 4; ga[c] = ldsf4(sm, lM + 640 * 4 + eo); cbv[c] = ldsf4(sm, lM + 704 * 4 + eo) + 1.f; });
    wait_vm<0>();
    sfor<4>([&](auto p) { tie_v(rp[p]); });
    tie_v(vg); tie_v(xres[0]); tie_v(xres[1]);
    for (u32 sp = 0; sp < SPIN_CAP; ++sp) {
      if (__all(vg[1] == tag32 && vg[3] == tag32)) break;
      __builtin_amdgcn_s_sleep(1);
      ld_v4<0>(vg, (hh * 512 + nt * 64 + 2 * l31) * 8, rW, WS_VG);
      wait_vm<0>();
      tie_v(vg);
    }
    { u32 a = vg[0], b = vg[2]; u32x2 o = {a, b}; *(LAS u32x2*)(sm + lM + (L_RS - L_MODV) + w * 512 + lane * 8) = o; }
    float rre, rim;
    {
      const float r4 = add_x32((__uint_as_float(rp[0]) + __uint_as_float(rp[1])) + (__uint_as_float(rp[2]) + __uint_as_float(rp[3])));
      auto r = __builtin_amdgcn_permlane16_swap(__float_as_uint(r4), __float_as_uint(r4), false, false);
      u32 a = r[0], b = r[1]; rre = __uint_as_float(a); rim = __uint_as_float(b);
    }
    u32 xg[4][2];
    sfor<2>([&](auto p) { u32x4 t = chunk_swap16(xres[p][0], xres[p][1], xres[p][2], xres[p][3]); xg[2 * p][0] = t[0]; xg[2 * p][1] = t[1]; xg[2 * p + 1][0] = t[2]; xg[2 * p + 1][1] = t[3]; });
    m16_fence(y);
    u32 pk[4][2];
    sfor<4>([&](auto c) {
      const u32 eo = (L_RS - L_MODV) + w * 512 + (c * 16 + qq * 4) * 4;
      f32x4 vre = ldsf4(sm, lM + eo), vim = ldsf4(sm, lM + 256 + eo);
      f32x4 xv = {b_lo(xg[c][0]), b_hi(xg[c][0]), b_lo(xg[c][1]), b_hi(xg[c][1])};
      x1[c] = xv + ga[c] * (y[c] + rre * vre - rim * vim);
      f32x4 s = x1[c] * cbv[c];
      pk[c][0] = pack2(s[0], s[1]); pk[c][1] = pack2(s[2], s[3]);
    });
    sfor<2>([&](auto p) {
      pub16<4, p * 2048>(chunk_swap16(pk[2 * p][0], pk[2 * p][1], pk[2 * p + 1][0], pk[2 * p + 1][1]), rW,
                         (qq & 1) * 1024 + (qq >> 1) * 512 + l15 * 16, WS_X1 + ((tb * 2 + wk) * 32 + nt * 4) * 1024 + w1 * 256);
    });
    const f32x4 s4 = (x1[0] + x1[1]) + (x1[2] + x1[3]);
    const float mean = add_x32(add_x16((s4[0] + s4[1]) + (s4[2] + s4[3]))) * (1.f / 64);
    f32x4 q4 = {0.f, 0.f, 0.f, 0.f};
    sfor<4>([&](auto c) { f32x4 d = x1[c] - mean; q4 += d * d; });
    const float m2 = add_x32(add_x16((q4[0] + q4[1]) + (q4[2] + q4[3])));
    u32x2 o = {__float_as_uint(mean), __float_as_uint(m2)};
    pub8<4>(o, rW, l15 * 8, WS_ST + ((tb * 2 + wk) * 8 + nt) * 256 + w1 * 128);
    bar();
  }
  FS(36);
  const rsrc_t rG = mk_rsrc(w < 2 ? Wg : Wu, D * F * 2);
  const rsrc_t rN = mk_rsrc(Wn, F * D * 2);
  u32 vG[4], vN[4];
  sfor<4>([&](auto i) {
    vG[i] = (lane >> 3) * (F * 2) + (((lane & 7) ^ ((qq & 1) << 2)) << 4) + i * (8 * F * 2 - 1024);
    vN[i] = (lane >> 3) * 1024 + (((lane & 7) ^ (((qq & 1) + 2 * (i & 1)) << 1)) << 4) + i * (8 * 1024 - 1024);
  });
  const u32 sG = w1 * 32 * (F * 2) + ft * 256, sN = (wk * 128 + w1 * 32) * 1024 + nt * 128;
  auto g_dma = [&](auto jc, auto ic_) {
    constexpr int j = jc, i = ic_;
    dma<200 + j, i * 1024>(vG[i], rG, sG + ((j + ft) & 7) * (64 * F * 2) + (j >> 3) * 128);
  };
  auto rot7 = [&](int j) { int pr = j + nt; pr = pr >= 7 ? pr - 7 : pr; return pr >= 7 ? pr - 7 : pr; };
  auto n_dma = [&](auto jc, auto ic_) {
    constexpr int j = jc, i = ic_;
    dma<300 + j, i * 1024>(vN[i], rN, sN + rot7(j % 7) * (256 * 1024) + (j / 7) * 65536);
  };
  const bool act4 = loc < 28;
  u32x4 ug[4];
  const u32 vUV = ((tid >> 7) * F + ft * 128 + (tid & 127)) * 16;
  if (act4) {
    sfor<3>([&](auto j) { set_m0n<j * 16384>(wl); sfor<4>([&](auto i) { g_dma(j, i); }); });
  } else {
    sfor<3>([&](auto j) { set_m0n<j * 16384>(wl); sfor<4>([&](auto i) { n_dma(j, i); }); });
  }
  bar_vm<12, 4>();
  if (tid == 0) post_flag(ws + WS_F3 + (tb * 8 + nt) * 128, tag);
  FS(100);
  if (act4) {
    sfor<NG0 - 3>([&](auto j) { set_m0n<(j + 3) * 16384>(wl); sfor<4>([&](auto i) { g_dma(ic<j + 3>{}, i); }); });
  } else {
    sfor<5>([&](auto j) { set_m0n<(j + 3) * 16384>(wl); sfor<4>([&](auto i) { n_dma(ic<j + 3>{}, i); }); });
  }
  FS(101);
  {
    sfor<4>([&](auto i) { tie_v(gvw[i]); });
    tie_v(warm[0]); tie_v(warm[1]);
    float a[16];
    sfor<16>([&](auto j) { a[j] = 0.f; });
    sfor<4>([&](auto i) {
      const float sh = *(LAS float*)(sm + lM + (768 + (tid >> 3) + i * 32) * 4);
      const float cs = 1.f + *(LAS float*)(sm + lM + (896 + (tid >> 3) + i * 32) * 4);
      sfor<4>([&](auto j) { u32 d = gvw[i][+j]; float lo = b_lo(d), hi = b_hi(d);
        a[2 * j] += cs * lo; a[2 * j + 1] += cs * hi; a[8 + 2 * j] += sh * lo; a[8 + 2 * j + 1] += sh * hi; });
    });
    float b8[8], b4[4];
    sfor<8>([&](auto j) { b8[j] = red_x32(a[j], a[8 + j]); });
    sfor<4>([&](auto j) { b4[j] = red_x16(b8[j], b8[4 + j]); });
    f32x2 o = {red_x8(b4[0], b4[2], h0), red_x8(b4[1], b4[3], h0)};
    *(LAS f32x2*)(sm + lM + (L_UVP - L_MODV) + w * 512 + (hh * 64 + (lane & 7) * 8 + ((lane >> 3) & 3) * 2) * 4) = o;
  }
  bar();
  if (w == 0 && bid < 224) {
    float su = 0.f, sv = 0.f;
    sfor<4>([&](auto i) { su += *(LAS float*)(sm + lM + (L_UVP - L_MODV) + i * 512 + lane * 4); sv += *(LAS float*)(sm + lM + (L_UVP - L_MODV) + i * 512 + 256 + lane * 4); });
    u32x4 g = {__float_as_uint(su), tag32, __float_as_uint(sv), tag32};
    pub16<0>(g, rW, lane16, WS_UVG + (ukq * 3584 + (ucb / 28) * F + (ucb % 28) * 64) * 16);
  }
  STAMP(6); FS(37);
  u32x4 gbg[4];
  const GAS u64* pF4A = (const GAS u64*)(ws + WS_F4A + ((tb >> 1) * 16 + (lane < 14 ? lane : 0)) * 128);
  const GAS u64* pF4B = (const GAS u64*)(ws + WS_F4B + ((tb >> 1) * 16 + (lane < 14 ? lane : 0)) * 128);

  if (act4) {
    u32 tB[2][2];
    {
      const u32 q = l15 >> 2, pp = lane & 3;
      sfor<2>([&](auto c) { sfor<2>([&](auto hf) {
        tB[c][hf] = (8 * hh + q) * 128 + ((4 * (c ^ (q >> 1)) + 2 * (qq & 1) + (pp >> 1)) << 4) + 8 * (pp & 1) + hf * 65536; tie_v(tB[c][hf]); }); });
    }
    poll_flags<16>(ws + WS_F3 + rb * 16 * 128, tag, lane);
    STAMP(7); FS(38);
    u32x2 stv[4];
    sfor<4>([&](auto i) { ld_v2<i * 256>(stv[i], hh * 1024 + l31 * 8, rW, WS_ST + (rb * 4 + w) * 2048); });
    u32x4 xa[32];
    const u32 so = WS_X1 + (rb * 4 + w) * 32 * 1024;
    auto xa_ld = [&](auto sc_) { constexpr int s = sc_; ld_a<(s & 3) * 1024>(xa[s], lane16, rW, so + (((s >> 2) + ft) & 7) * 4096); };
    sfor<P4_PRE>([&](auto s) { xa_ld(s); });
    bar_vm<P4_PRE, 200, 200 + NG0 - 1>();
    f32x16 z[2][4];
    u32x4 wf[2][4], uf[4];
    auto w_rd = [&](int t, int j) {
      const u32 o = tB[j & 1][(t >> 4) & 1] + ((t >> 2) & 3) * 16384 + (j >> 1) * 8192 + (t & 3) * 2048;
      wf[t & 1][j] = lds_tr(sm, o, o + 512);
    };
    sfor<4>([&](auto j) { w_rd(0, j); });
    float rr, sd;
    u32x4 xst;
    {
      sfor<4>([&](auto i) { tie_v(stv[i]); });
      float ms = 0.f;
      sfor<4>([&](auto i) { u32 a = stv[i][0]; ms += __uint_as_float(a); });
      const float mean = add_x32(ms) * (1.f / 8);
      float q = 0.f;
      sfor<4>([&](auto i) { u32 a = stv[i][0], b = stv[i][1]; float d = __uint_as_float(a) - mean; q += __uint_as_float(b) + 64.f * d * d; });
      const float var = add_x32(q) * (1.f / D) + 1e-5f;
      rr = __builtin_amdgcn_rsqf(var);
      sd = var * rr;
      const float mh = b_lo(pack2(mean, 0.f)), sh = b_lo(pack2(sd, 0.f));
      xst[0] = hh ? 0u : pack2(-mh, -mh); xst[1] = hh ? 0u : pack2(mh - mean, mh - mean);
      xst[2] = hh ? 0u : pack2(sh, sh); xst[3] = hh ? 0u : pack2(sd - sh, sd - sh);
      tie_v(xst); tie_v(rr); tie_v(sd);
    }
    auto u_rd = [&](int p, int j) { uf[j] = lds16(sm, lM + (L_UV - L_MODV) + ((j >> 1) * 128 + p * 64 + (j & 1) * 32 + l31) * 16); };
    constexpr float C1 = -2.f * 0.7978845608028654f * 1.4426950408889634f, C3 = C1 * 0.044715f;
    float c1v = C1, c3v = C3;
    tie_v(c1v); tie_v(c3v);
    const u32 sAct = WS_ACT + ((rb * 8 + 2 * w) * 56 + ft * 4) * 1024;
    const u32 vAct = (l31 >> 4) * (56 * 1024) + hh * 256 + l15 * 16;
    u32 pk[4];
    Ge ge;
    sfor<64>([&](auto tc) {
      constexpr int t = tc, j = t >> 2, s4 = t & 3, p = j >> 3;
      constexpr bool many = t < 4 * (8 - NG0) + 4;
      SCHED();
      if constexpr (p == 0) wait_a<S4.wait[t] - 1 - S4.posx[t]>(xa[t]);
      sfor<4>([&](auto mc) {
        constexpr int m = mc;
        if constexpr (m == 0 && t < 4 * (8 - NG0)) set_m0<(NG0 + (t >> 2)) * 16384>(wl);
        if constexpr (m == 3 && t >= 3 && (many || s4 == 3)) set_m0<(((t - 3) >> 2) & 7) * 16384>(wl);
        mm_va<(t & 31) == 0>(z[p][m], wf[t & 1][m], xa[t & 31]);
        if constexpr (t == 33) mm_vv<false>(z[0][m], uf[m], xst);
        SCHED();
        if constexpr (s4 != 3) { w_rd(t + 1, m); }
        if constexpr (t == 32) u_rd(0, m);
        if constexpr (m == 0 && t == P4_UR) {
          wait_vm<S4.urw - 1 - S4.posug>();
          sfor<4>([&](auto q) { tie_v(ug[q]); });
          for (u32 sp = 0; sp < SPIN_CAP; ++sp) {
            bool ok = true;
            sfor<4>([&](auto q) { ok = ok && ug[q][1] == tag32 && ug[q][3] == tag32; });
            if (__all(ok)) break;
            __builtin_amdgcn_s_sleep(1);
            sfor<4>([&](auto q) { ld_v4<0>(ug[q], vUV, rW, WS_UVG + q * 3584 * 16); });
            wait_vm<0>();
            sfor<4>([&](auto q) { tie_v(ug[q]); });
          }
          u32 d[2];
          sfor<2>([&](auto e) { u32 a = ug[0][2 * e], b = ug[1][2 * e], c = ug[2][2 * e], dd = ug[3][2 * e];
            const float s = (__uint_as_float(a) + __uint_as_float(b)) + (__uint_as_float(c) + __uint_as_float(dd));
            const float hi = b_lo(pack2(s, 0.f));
            d[e] = pack2(hi, s - hi); });
          u32x4 rec = {d[0], d[0], d[1], d[1]};
          *(LAS u32x4*)(sm + lM + (L_UV - L_MODV) + tid * 16) = rec;
        }
        if constexpr (m == 0 && t < 4 * (8 - NG0)) g_dma(ic<NG0 + (t >> 2)>{}, ic<(t & 3)>{});
        if constexpr (p4_piece(t, m) >= 0) {
          constexpr int e = p4_piece(t, m) / 5, st = p4_piece(t, m) % 5;
          if constexpr (st == 0) ge_a(ge, z[0][e >> 3][2 * (e & 7)], z[0][e >> 3][2 * (e & 7) + 1], rr);
          if constexpr (st == 1) ge_b(ge, c3v, c1v);
          if constexpr (st == 2) ge_c(ge, z[0][2 + (e >> 3)][2 * (e & 7)], z[0][2 + (e >> 3)][2 * (e & 7) + 1]);
          if constexpr (st == 3) ge_d(ge, sd);
          if constexpr (st == 4) {
            pk[e & 3] = ge_e(ge);
            if constexpr ((e & 3) == 3) pub16<5, (e >> 3) * 1024 + ((e >> 2) & 1) * 512>(chunk_swap(pk[0], pk[1], pk[2], pk[3]), rW, vAct, sAct);
          }
        }
        if constexpr (m == 1 && s4 == 3) {
          constexpr int need = j + 1 >= NG0 && j < 15 ? S4.bar[j] - 1 - S4.posg[j + 1] : 63;
          bar_vm<need, (j < 15 ? 201 + j : 0)>();
          FS(40 + j);
          if constexpr (t < 63) { w_rd(t + 1, 0); w_rd(t + 1, 1); } else { u_rd(1, 0); u_rd(1, 1); }
        }
        if constexpr (m == 1) {
          if constexpr (t + P4_PRE < 32) xa_ld(ic<t + P4_PRE>{});
          else if constexpr (t >= P4_UG && t < P4_UG + 4) ld_v4<0>(ug[t - P4_UG], vUV, rW, WS_UVG + (t - P4_UG) * 3584 * 16);
        }
        if constexpr (m >= 2 && s4 == 3) { if constexpr (t < 63) w_rd(t + 1, m); else u_rd(1, m); }
        if constexpr (m == 3 && t >= 3) {
          constexpr int gj = (t - 3) >> 2, gi = (t - 3) & 3;
          if constexpr (gj < 8) g_dma(ic<8 + gj>{}, ic<gi>{}); else n_dma(ic<gj - 8>{}, ic<gi>{});
        }
        SCHED();
      });
    });
    sfor<4>([&](auto m) { mm_vv<false>(z[1][m], uf[m], xst); if constexpr (m < 3) n_dma(ic<7>{}, ic<1 + m>{}); });
    STAMP(8); FS(56);
    mm_fence(z[1][0], z[1][2]); mm_tie(z[1][1], z[1][3]);
    sfor<4>([&](auto qc) {
      constexpr int c = qc >> 1, g0 = (qc & 1) * 2;
      u32 pq[4];
      sfor<2>([&](auto gg) {
        constexpr int g = g0 + gg;
        f32x4 zg = {z[1][c][4 * g], z[1][c][4 * g + 1], z[1][c][4 * g + 2], z[1][c][4 * g + 3]};
        f32x4 zu = {z[1][2 + c][4 * g], z[1][2 + c][4 * g + 1], z[1][2 + c][4 * g + 2], z[1][2 + c][4 * g + 3]};
        f32x4 gt = rr * zg;
        f32x4 ex = gt * (C1 + C3 * gt * gt);
        sfor<4>([&](auto i) { ex[+i] = __builtin_amdgcn_exp2f(ex[+i]); });
        ex = ex * sd + sd;
        sfor<4>([&](auto i) { ex[+i] = __builtin_amdgcn_rcpf(ex[+i]); });
        f32x4 a = gt * zu * ex;
        pq[2 * gg] = pack2(a[0], a[1]); pq[2 * gg + 1] = pack2(a[2], a[3]);
      });
      pub16<6, 2048 + c * 1024 + (qc & 1) * 512>(chunk_swap(pq[0], pq[1], pq[2], pq[3]), rW, vAct, sAct);
      if constexpr (qc == 0) {
        bar_vm<S4.end + 3 + 1 - 1 - S4.possto, 5>();
        if (tid == 0) post_flag(ws + WS_F4A + (rb * 16 + ft) * 128, tag);
      }
    });
  }
  STAMP(9); FS(57);

  {
    bar();
    for (u32 sp = 0; sp < SPIN_CAP; ++sp) {
      u64 fa = __hip_atomic_load(pF4A, RLX_AGENT);
      if (__all(fa == tag)) break;
      __builtin_amdgcn_s_sleep(POLL_SLEEP);
    }
    asm volatile("" ::: "memory");
    FS(102);
    u32x4 aa[56];
    const u32 so = WS_ACT + (tb * 4 + w) * 56 * 1024;
    const u32 vA2 = lane16 + 4096;
    auto aa_ld = [&](auto sc_) {
      constexpr int s = sc_;
      ld_a<(s & 1) * 1024 + (s / 28) * 2048>(aa[s], (s & 2) ? vA2 : lane16, rW, so + rot7((s >> 2) % 7) * 8192);
    };
    sfor<P5_PRE>([&](auto s) { aa_ld(s); });
    bar_vm<P5_PRE, 6, 307>();
    if (act4 && tid == 0) post_flag(ws + WS_F4B + (rb * 16 + ft) * 128, tag);
    STAMP(10); FS(58);
    u32 tN[4][2];
    {
      const u32 q = l15 >> 2, pp = lane & 3, f = (q >> 1) + 2 * (qq & 1);
      sfor<4>([&](auto c) { sfor<2>([&](auto hf) {
        tN[c][hf] = (8 * qq + q) * 128 + ((2 * (c ^ f) + (pp >> 1)) << 4) + 8 * (pp & 1) + hf * 65536; tie_v(tN[c][hf]); }); });
    }
    f32x4 y[4];
    u32x4 nf[2][4];
    auto n_rd = [&](int t, int c) {
      const u32 o = tN[c][(t >> 4) & 1] + ((t >> 2) & 3) * 16384 + (t & 3) * 4096;
      nf[t & 1][c] = lds_tr(sm, o, o + 512);
    };
    sfor<4>([&](auto c) { n_rd(0, c); });
    sfor<4>([&](auto q) { ld_v4<0>(gbg[q], (2560 + nt * 64 + 2 * l31) * 8, rW, WS_MODG + q * 3072 * 8); });
    sfor<56>([&](auto tc) {
      constexpr int t = tc, i = t >> 2, s4 = t & 3;
      SCHED();
      if constexpr (t == 28) {
        for (u32 sp = 0; sp < SPIN_CAP; ++sp) {
          u64 fb = __hip_atomic_load(pF4B, RLX_AGENT);
          if (__all(fb == tag)) break;
          __builtin_amdgcn_s_sleep(POLL_SLEEP);
        }
        asm volatile("" ::: "memory");
        STAMP(11); FS(59);
        sfor<P5_PRE>([&](auto s) { aa_ld(ic<28 + s>{}); });
      }
      wait_a<S5.wait[t] - 1 - S5.posa[t] + (t < P5_PRE ? 4 : 0)>(aa[t]);
      sfor<4>([&](auto mc) {
        constexpr int m = mc;
        if constexpr (m == 2 && s4 == 3 && t < 27) set_m0<(i & 7) * 16384>(wl);
        m16_va<t == 0>(y[m], nf[t & 1][m], aa[t]);
        SCHED();
        if constexpr (s4 != 3) n_rd(t + 1, m);
        if constexpr (m == 1 && s4 == 3 && i < 13) { bar_vm<63, (i >= 7 ? 301 + i : 0)>(); FS(60 + i); n_rd(t + 1, 0); n_rd(t + 1, 1); }
        if constexpr (m >= 2 && s4 == 3 && t < 55) n_rd(t + 1, m);
        if constexpr (m == 1 && (t % 28) + P5_PRE < 28) aa_ld(ic<t + P5_PRE>{});
        if constexpr (m == 3 && t >= 3 && t < 27) n_dma(ic<8 + ((t - 3) >> 2)>{}, ic<((t - 3) & 3)>{});
        SCHED();
      });
    });
    STAMP(12); FS(73);
    sfor<4>([&](auto q) { tie_v(gbg[q]); });
    {
      for (u32 sp = 0; sp < SPIN_CAP; ++sp) {
        bool ok = true;
        sfor<4>([&](auto q) { ok = ok && gbg[q][1] == tag32 && gbg[q][3] == tag32; });
        if (__all(ok)) break;
        sfor<4>([&](auto q) { ld_v4<0>(gbg[q], (2560 + nt * 64 + 2 * l31) * 8, rW, WS_MODG + q * 3072 * 8); });
        wait_vm<0>();
        sfor<4>([&](auto q) { tie_v(gbg[q]); });
      }
      f32x2 mv;
      sfor<2>([&](auto o) {
        u32 a = gbg[0][2 * o], b = gbg[1][2 * o], c = gbg[2][2 * o], d = gbg[3][2 * o];
        mv[+o] = (__uint_as_float(a) + __uint_as_float(b)) + (__uint_as_float(c) + __uint_as_float(d)); });
      *(LAS f32x2*)(sm + lM + (L_RS - L_MODV) + w * 512 + l31 * 8) = mv;
    }
    m16_fence(y);
    u32 pk[4][2];
    sfor<4>([&](auto c) {
      f32x4 m = x1[c] + ldsf4(sm, lM + (L_RS - L_MODV) + w * 512 + (c * 16 + qq * 4) * 4) * y[c];
      pk[c][0] = pack2(m[0], m[1]); pk[c][1] = pack2(m[2], m[3]);
    });
    const rsrc_t rO = mk_rsrc(out, S * D * 2);
    sfor<2>([&](auto p) {
      __builtin_amdgcn_raw_buffer_store_b128(chunk_swap16(pk[2 * p][0], pk[2 * p][1], pk[2 * p + 1][0], pk[2 * p + 1][1]), rO,
                                             l15 * 1024 + (qq & 1) * 32 + (qq >> 1) * 16 + p * 64, (tb * 64 + w * 16) * 1024 + nt * 128, 0);
    });
  }
  STAMP(13); FS(74);
}

extern "C" void kernel_launch(void* const* d_in, const int* in_sizes, int n_in, void* d_out, int out_size, void* d_ws,
                              size_t ws_size, hipStream_t stream) {
  (void)in_sizes; (void)n_in; (void)out_size; (void)ws_size;
  (void)hipFuncSetAttribute((const void*)mk_block, hipFuncAttributeMaxDynamicSharedMemorySize, LDS_BYTES);
  mk_block<<<256, 256, LDS_BYTES, stream>>>((const u16*)d_in[0], (const u16*)d_in[1], (const u16*)d_in[2], (const u16*)d_in[3],
                                            (const u16*)d_in[4], (const u16*)d_in[6], (const u16*)d_in[7], (const u16*)d_in[8],
                                            (const u16*)d_in[9], (const u16*)d_in[10], (u16*)d_out, (char*)d_ws);
}
```

```cpp
#include <hip/hip_runtime.h>
#include <utility>
#pragma clang diagnostic ignored "-Winline-asm"

typedef unsigned short u16;
typedef unsigned u32;
typedef unsigned long long u64;
typedef float f32x2 __attribute__((ext_vector_type(2)));
typedef float f32x4 __attribute__((ext_vector_type(4)));
typedef float f32x16 __attribute__((ext_vector_type(16)));
typedef unsigned u32x2 __attribute__((ext_vector_type(2)));
typedef unsigned u32x4 __attribute__((ext_vector_type(4)));
typedef short s16x4 __attribute__((ext_vector_type(4)));
typedef short s16x8 __attribute__((ext_vector_type(8)));
typedef __bf16 bf16x2 __attribute__((ext_vector_type(2)));
typedef __bf16 bf16x8 __attribute__((ext_vector_type(8)));
typedef __amdgpu_buffer_rsrc_t rsrc_t;
#define LAS __attribute__((address_space(3)))
#define GAS __attribute__((address_space(1)))
#define DI __device__ __forceinline__
#define RLX_AGENT __ATOMIC_RELAXED, __HIP_MEMORY_SCOPE_AGENT

constexpr int S = 2048, D = 512, F = 1792;
constexpr u32 WS_XN = 0;
constexpr u32 WS_PQ = WS_XN + (2u << 20);
constexpr u32 WS_X1 = WS_PQ + (8u << 20);
constexpr u32 WS_ACT = WS_X1 + (2u << 20);
constexpr u32 WS_RS = WS_ACT + (7u << 20);
constexpr u32 WS_ST = WS_RS + (256u << 10);
constexpr u32 WS_MODG = WS_ST + (256u << 10);
constexpr u32 WS_VG = WS_MODG + 4 * 3072 * 8;
constexpr u32 WS_UVG = WS_VG + 1024 * 8;
constexpr u32 WS_F1 = WS_UVG + 4 * 2 * 3584 * 8;
constexpr u32 WS_F2 = WS_F1 + 8 * 256 * 128;
constexpr u32 WS_F3 = WS_F2 + 256 * 128;
constexpr u32 WS_F4A = WS_F3 + 256 * 128;
constexpr u32 WS_F4B = WS_F4A + 256 * 128;
constexpr u32 WS_GEN = WS_F4B + 256 * 128;
constexpr u32 WS_TS = WS_GEN + 256;
constexpr u32 WS_TS2 = WS_TS + 256 * 16 * 8;
constexpr u32 WS_END = WS_TS2 + 256 * 256 * 4;
constexpr u32 L_EX = 128 * 1024;
constexpr u32 L_MODV = 144 * 1024;
constexpr u32 L_UV = L_MODV + 4096;
constexpr u32 L_UVP = L_UV + 4096;
constexpr u32 L_MP = L_UVP + 2048;
constexpr u32 L_RS = L_MP + 1024;
constexpr u32 L_CNT = L_RS + 2048;
constexpr u32 LDS_BYTES = 160 * 1024;
constexpr u32 SPIN_CAP = 200000;
#define POLL_SLEEP 2

template <class Fn, int... Is> DI void sfor_impl(Fn&& f, std::integer_sequence<int, Is...>) { (f(std::integral_constant<int, Is>{}), ...); }
template <int V> using ic = std::integral_constant<int, V>;
template <int N, class Fn> DI void sfor(Fn&& f) { sfor_impl(f, std::make_integer_sequence<int, N>{}); }

DI rsrc_t mk_rsrc(const void* p, u32 bytes) { return __builtin_amdgcn_make_buffer_rsrc((void*)p, 0, bytes, 0x00020000); }
DI float b_lo(u32 v) { return __uint_as_float(v << 16); }
DI float b_hi(u32 v) { return __uint_as_float(v & 0xffff0000u); }
DI u32 pack2(float a, float b) { f32x2 p = {a, b}; bf16x2 r = __builtin_convertvector(p, bf16x2); return __builtin_bit_cast(u32, r); }

template <int IMM> DI void ld_a(u32x4& d, u32 voff, rsrc_t r, u32 soff) {
  asm volatile("buffer_load_dwordx4 %0, %1, %2, %3 offen offset:%c4 sc1" : "=a"(d) : "v"(voff), "s"(r), "s"(soff), "i"(IMM));
}
template <int IMM> DI void ld_v4(u32x4& d, u32 voff, rsrc_t r, u32 soff) {
  asm volatile("buffer_load_dwordx4 %0, %1, %2, %3 offen offset:%c4 sc1" : "=v"(d) : "v"(voff), "s"(r), "s"(soff), "i"(IMM));
}
template <int IMM> DI void ld_v4p(u32x4& d, u32 voff, rsrc_t r, u32 soff) {
  asm volatile("buffer_load_dwordx4 %0, %1, %2, %3 offen offset:%c4" : "=v"(d) : "v"(voff), "s"(r), "s"(soff), "i"(IMM));
}
template <int IMM> DI void ld_v2(u32x2& d, u32 voff, rsrc_t r, u32 soff) {
  asm volatile("buffer_load_dwordx2 %0, %1, %2, %3 offen offset:%c4 sc1" : "=v"(d) : "v"(voff), "s"(r), "s"(soff), "i"(IMM));
}
template <int IMM> DI void ld_v2p(u32x2& d, u32 voff, rsrc_t r, u32 soff) {
  asm volatile("buffer_load_dwordx2 %0, %1, %2, %3 offen offset:%c4" : "=v"(d) : "v"(voff), "s"(r), "s"(soff), "i"(IMM));
}
template <int IMM> DI void ld_v1(u32& d, u32 voff, rsrc_t r, u32 soff) {
  asm volatile("buffer_load_dword %0, %1, %2, %3 offen offset:%c4 sc1" : "=v"(d) : "v"(voff), "s"(r), "s"(soff), "i"(IMM));
}
template <int IMM> DI void ld_u16p(u32& d, u32 voff, rsrc_t r, u32 soff) {
  asm volatile("buffer_load_ushort %0, %1, %2, %3 offen offset:%c4" : "=v"(d) : "v"(voff), "s"(r), "s"(soff), "i"(IMM));
}
template <int IMM> DI void set_m0(u32 base) { asm volatile("s_add_u32 m0, %0, %c1" :: "s"(base), "i"(IMM) : "m0", "scc"); }
template <int IMM> DI void set_m0n(u32 base) { asm volatile("s_add_u32 m0, %0, %c1\n\ts_nop 0" :: "s"(base), "i"(IMM) : "m0", "scc"); }
template <int ID, int OFF> DI void dma(u32 voff, rsrc_t r, u32 soff) {
  asm volatile("buffer_load_dwordx4 %0, %1, %2 offen offset:%c3 lds ; ID %c4" :: "v"(voff), "s"(r), "s"(soff), "i"(OFF), "i"(ID));
}
template <int ID, int OFF = 0> DI void pub16(u32x4 v, rsrc_t r, u32 voff, u32 soff) {
  asm volatile("buffer_store_dwordx4 %0, %1, %2, %3 offen offset:%c5 sc1 ; ID %c4\n\ts_nop 1" :: "v"(v), "v"(voff), "s"(r), "s"(soff), "i"(ID), "i"(OFF));
}
template <int ID> DI void pub8(u32x2 v, rsrc_t r, u32 voff, u32 soff) {
  asm volatile("buffer_store_dwordx2 %0, %1, %2, %3 offen sc1 ; ID %c4" :: "v"(v), "v"(voff), "s"(r), "s"(soff), "i"(ID));
}
template <int ID> DI void pub4(u32 v, rsrc_t r, u32 voff, u32 soff) {
  asm volatile("buffer_store_dword %0, %1, %2, %3 offen sc1 ; ID %c4" :: "v"(v), "v"(voff), "s"(r), "s"(soff), "i"(ID));
}
template <int N, int A = 0> DI void wait_vm() { static_assert(N >= 0 && N < 64, ""); asm volatile("s_waitcnt vmcnt(%c0) ; NEED %c1" :: "i"(N), "i"(A) : "memory"); }
template <int N> DI void wait_a(u32x4& a) { static_assert(N >= 0, ""); asm volatile("s_waitcnt vmcnt(%c1)" : "+a"(a) : "i"(N < 63 ? N : 63)); }
DI void tie_a(u32x4& a) { asm volatile("" : "+a"(a)); }
template <class T> DI void tie_v(T& a) { asm volatile("" : "+v"(a)); }
template <int N, int A = 0, int B = 0> DI void bar_vm() {
  static_assert(N >= 0, "");
  asm volatile("s_waitcnt vmcnt(%c0) lgkmcnt(0) ; NEED %c1 NEED %c2\n\ts_barrier" :: "i"(N < 63 ? N : 63), "i"(A), "i"(B) : "memory");
}
DI void bar() { asm volatile("s_waitcnt lgkmcnt(0)\n\ts_barrier" ::: "memory"); }
#define SCHED() __builtin_amdgcn_sched_barrier(0)

DI void st16(u32x4 v, rsrc_t r, u32 voff, u32 soff) { __builtin_amdgcn_raw_buffer_store_b128(v, r, voff, soff, 16); }
DI void st8(u32x2 v, rsrc_t r, u32 voff, u32 soff) { __builtin_amdgcn_raw_buffer_store_b64(v, r, voff, soff, 16); }
DI void st4(u32 v, rsrc_t r, u32 voff, u32 soff) { __builtin_amdgcn_raw_buffer_store_b32(v, r, voff, soff, 16); }

DI f32x16 mfma(u32x4 a, u32x4 b, f32x16 c) {
  return __builtin_amdgcn_mfma_f32_32x32x16_bf16(__builtin_bit_cast(bf16x8, a), __builtin_bit_cast(bf16x8, b), c, 0, 0, 0);
}
template <bool Z> DI void mm_va(f32x16& c, u32x4 a, u32x4 b) {
  if constexpr (Z) asm volatile("v_mfma_f32_32x32x16_bf16 %0, %1, %2, 0" : "=&v"(c) : "v"(a), "a"(b));
  else asm volatile("v_mfma_f32_32x32x16_bf16 %0, %1, %2, %0" : "+v"(c) : "v"(a), "a"(b));
}
template <bool Z> DI void mm_av(f32x16& c, u32x4 a, u32x4 b) {
  if constexpr (Z) asm volatile("v_mfma_f32_32x32x16_bf16 %0, %1, %2, 0" : "=&v"(c) : "a"(a), "v"(b));
  else asm volatile("v_mfma_f32_32x32x16_bf16 %0, %1, %2, %0" : "+v"(c) : "a"(a), "v"(b));
}
template <bool Z> DI void mm_vv(f32x16& c, u32x4 a, u32x4 b) {
  if constexpr (Z) asm volatile("v_mfma_f32_32x32x16_bf16 %0, %1, %2, 0" : "=&v"(c) : "v"(a), "v"(b));
  else asm volatile("v_mfma_f32_32x32x16_bf16 %0, %1, %2, %0" : "+v"(c) : "v"(a), "v"(b));
}
template <bool Z> DI void m16_va(f32x4& c, u32x4 a, u32x4 b) {
  if constexpr (Z) asm volatile("v_mfma_f32_16x16x32_bf16 %0, %1, %2, 0" : "=&v"(c) : "v"(a), "a"(b));
  else asm volatile("v_mfma_f32_16x16x32_bf16 %0, %1, %2, %0" : "+v"(c) : "v"(a), "a"(b));
}
DI void m16_fence(f32x4 (&y)[4]) { asm volatile("s_nop 11" : "+v"(y[0]), "+v"(y[1]), "+v"(y[2]), "+v"(y[3])); }
DI void mm_fence(f32x16& a, f32x16& b) { asm volatile("s_nop 15" : "+v"(a), "+v"(b)); }
DI void mm_tie(f32x16& a, f32x16& b) { asm volatile("" : "+v"(a), "+v"(b)); }
DI float smul(float a, float b) { float r; asm("v_mul_f32_e32 %0, %1, %2" : "=v"(r) : "v"(a), "v"(b)); return r; }
DI float sfma(float a, float b, float c) { float r; asm("v_fma_f32 %0, %1, %2, %3" : "=v"(r) : "v"(a), "v"(b), "v"(c)); return r; }
DI u32x4 lds16(LAS char* sm, u32 off) { return *(LAS u32x4*)(sm + off); }
DI f32x4 ldsf4(LAS char* sm, u32 off) { return *(LAS f32x4*)(sm + off); }
DI u32x4 lds_tr(LAS char* sm, u32 off0, u32 off1) {
  s16x4 lo = __builtin_amdgcn_ds_read_tr16_b64_v4i16((LAS s16x4*)(sm + off0));
  s16x4 hi = __builtin_amdgcn_ds_read_tr16_b64_v4i16((LAS s16x4*)(sm + off1));
  s16x8 w = __builtin_shufflevector(lo, hi, 0, 1, 2, 3, 4, 5, 6, 7);
  return __builtin_bit_cast(u32x4, w);
}
template <int CTRL> DI float dpp(float v) { return __int_as_float(__builtin_amdgcn_update_dpp(0, __float_as_int(v), CTRL, 0xF, 0xF, true)); }
DI float add_x16(float v) { auto r = __builtin_amdgcn_permlane16_swap(__float_as_uint(v), __float_as_uint(v), false, false); u32 a = r[0], b = r[1]; return __uint_as_float(a) + __uint_as_float(b); }
DI float add_x32(float v) { auto r = __builtin_amdgcn_permlane32_swap(__float_as_uint(v), __float_as_uint(v), false, false); u32 a = r[0], b = r[1]; return __uint_as_float(a) + __uint_as_float(b); }
DI float red_x32(float a, float b) { auto r = __builtin_amdgcn_permlane32_swap(__float_as_uint(a), __float_as_uint(b), false, false); u32 p = r[0], q = r[1]; return __uint_as_float(p) + __uint_as_float(q); }
DI float red_x16(float a, float b) { auto r = __builtin_amdgcn_permlane16_swap(__float_as_uint(a), __float_as_uint(b), false, false); u32 p = r[0], q = r[1]; return __uint_as_float(p) + __uint_as_float(q); }
DI float red_x8(float a, float b, bool hi) { float keep = hi ? b : a, give = hi ? a : b; return keep + dpp<0x128>(give); }
DI float wave_sum(float v) {
  v += dpp<0xB1>(v); v += dpp<0x4E>(v); v += dpp<0x141>(v); v += dpp<0x140>(v);
  return add_x32(add_x16(v));
}
DI u32x4 chunk_swap(u32 a0, u32 a1, u32 b0, u32 b1) {
  auto r0 = __builtin_amdgcn_permlane32_swap(a0, b0, false, false);
  auto r1 = __builtin_amdgcn_permlane32_swap(a1, b1, false, false);
  u32x4 o; o[0] = r0[0]; o[1] = r1[0]; o[2] = r0[1]; o[3] = r1[1];
  return o;
}
DI u32x4 chunk_swap16(u32 a0, u32 a1, u32 b0, u32 b1) {
  auto r0 = __builtin_amdgcn_permlane16_swap(a0, b0, false, false);
  auto r1 = __builtin_amdgcn_permlane16_swap(a1, b1, false, false);
  u32x4 o; o[0] = r0[0]; o[1] = r1[0]; o[2] = r0[1]; o[3] = r1[1];
  return o;
}
template <int NF> DI void poll_flags(const char* p, u64 tag, int lane) {
  const GAS u64* q = (const GAS u64*)p + (lane < NF ? lane : 0) * 16;
  for (u32 sp = 0; sp < SPIN_CAP; ++sp) {
    u64 v = __hip_atomic_load(q, RLX_AGENT);
    if (__all(v == tag)) break;
    __builtin_amdgcn_s_sleep(POLL_SLEEP);
  }
  asm volatile("" ::: "memory");
}
DI void post_flag(char* p, u64 tag) { __hip_atomic_store((GAS u64*)p, tag, RLX_AGENT); }

#define STAMP(i) do {} while (0)
#define FS(i) do {} while (0)

#define NINIT 6
#define NW1 20
#define ND1 4
constexpr int P1_MODC = 1, P1_MODLD = 8, P1_MODUSE = 13, P1_V = 14, P1_XR = 10;
constexpr int p1_extra(int k) { return (k == P1_MODLD || k == P1_MODLD + 1) ? 4 : k == P1_XR ? 2 : 0; }
struct Sched1 { int posx[16][4], posa[24], pose, posm, top[16][4], bar[16], ext[16], pre; };
constexpr Sched1 mk_sched1() {
  Sched1 s{}; int n = 2;
  for (int i = 0; i < NINIT; ++i) s.posa[i] = -1;
  for (int k = 0; k < 3; ++k) {
    for (int i = 0; i < 4; ++i) s.posx[k][i] = n++;
    if (k == 1) { n += 6; s.pose = n - 1; }
  }
  for (int st = NINIT; st < 8; ++st) { n += 4; s.posa[st] = n - 1; }
  s.pre = n;
  for (int k = 0; k < 16; ++k)
    for (int i = 0; i < 4; ++i) {
      s.top[k][i] = n;
      const int g = 4 * k + i - 3;
      if (i == 3) s.bar[k] = n;
      if (i == 3) { s.ext[k] = n; n += p1_extra(k); if (k == P1_MODLD + 1) s.posm = n - 1; }
      if (g >= 0 && (g >> 2) < 8 + ND1) s.posa[8 + (g >> 2)] = n++;
      if (k + 3 < 16) s.posx[k + 3][i] = n++;
    }
  return s;
}
constexpr Sched1 S1 = mk_sched1();
#define NG0 6
constexpr int P4_PRE = 8;
constexpr int P4_E0 = 34;
constexpr int P4_UG = 24, P4_UR = 31;
struct Sched4 { int posx[32], posg[24], posug, possto, wait[64], bar[16], urw, end; };
constexpr int p4_piece(int t, int m) { return (t >= P4_E0 && m < 3 && (t - P4_E0) * 3 + m < 80) ? (t - P4_E0) * 3 + m : -1; }
constexpr Sched4 mk_sched4() {
  Sched4 s{}; int n = 0;
  for (int i = 0; i < P4_PRE; ++i) s.posx[i] = n++;
  for (int t = 0; t < 64; ++t) {
    s.wait[t] = n;
    for (int m = 0; m < 4; ++m) {
      if (m == 0 && t == P4_UR) s.urw = n;
      if (m == 0 && t < 4 * (8 - NG0)) s.posg[NG0 + (t >> 2)] = n++;
      if (p4_piece(t, m) >= 0 && p4_piece(t, m) % 20 == 19) s.possto = n++;
      if (m == 1 && (t & 3) == 3) s.bar[t >> 2] = n;
      if (m == 1) { if (t + P4_PRE < 32) s.posx[t + P4_PRE] = n++; else if (t >= P4_UG && t < P4_UG + 4) s.posug = n++; }
      if (m == 3 && t >= 3) s.posg[8 + ((t - 3) >> 2)] = n++;
    }
  }
  s.end = n;
  return s;
}
constexpr Sched4 S4 = mk_sched4();
constexpr int P5_PRE = 10;
struct Sched5 { int posa[56], wait[56], end; };
constexpr Sched5 mk_sched5() {
  Sched5 s{}; int n = 0;
  for (int i = 0; i < P5_PRE; ++i) s.posa[i] = n++;
  for (int t = 0; t < 56; ++t) {
    if (t == 28) for (int i = 0; i < P5_PRE; ++i) s.posa[28 + i] = n++;
    s.wait[t] = n;
    if ((t % 28) + P5_PRE < 28) s.posa[t + P5_PRE] = n++;
    if (t >= 3 && t < 27) n++;
  }
  s.end = n;
  return s;
}
constexpr Sched5 S5 = mk_sched5();

struct Ge { float g0, g1, a0, a1, m0, m1; };
DI void ge_a(Ge& e, float q0, float q1, float r) {
  asm volatile("v_mul_f32 %0, %4, %5\n\tv_mul_f32 %1, %4, %6\n\tv_mul_f32 %2, %0, %0\n\tv_mul_f32 %3, %1, %1"
               : "=&v"(e.g0), "=&v"(e.g1), "=&v"(e.a0), "=&v"(e.a1) : "v"(r), "v"(q0), "v"(q1));
}
DI void ge_b(Ge& e, float c3, float c1) {
  asm volatile("v_fma_f32 %0, %0, %4, %5\n\tv_fma_f32 %1, %1, %4, %5\n\tv_mul_f32 %0, %0, %2\n\tv_mul_f32 %1, %1, %3"
               : "+v"(e.a0), "+v"(e.a1) : "v"(e.g0), "v"(e.g1), "v"(c3), "v"(c1));
}
DI void ge_c(Ge& e, float u0, float u1) {
  asm volatile("v_exp_f32 %0, %0\n\tv_exp_f32 %1, %1\n\tv_mul_f32 %2, %4, %6\n\tv_mul_f32 %3, %5, %7"
               : "+v"(e.a0), "+v"(e.a1), "=&v"(e.m0), "=&v"(e.m1) : "v"(e.g0), "v"(e.g1), "v"(u0), "v"(u1));
}
DI void ge_d(Ge& e, float sd) {
  asm volatile("v_fma_f32 %0, %0, %2, %2\n\tv_fma_f32 %1, %1, %2, %2\n\tv_rcp_f32 %0, %0\n\tv_rcp_f32 %1, %1\n\ts_nop 0"
               : "+v"(e.a0), "+v"(e.a1) : "v"(sd));
}
DI u32 ge_e(Ge& e) {
  u32 o;
  asm volatile("v_mul_f32 %1, %1, %3\n\tv_mul_f32 %2, %2, %4\n\tv_cvt_pk_bf16_f32 %0, %1, %2"
               : "=v"(o), "+v"(e.m0), "+v"(e.m1) : "v"(e.a0), "v"(e.a1));
  return o;
}

__global__ void __launch_bounds__(256, 1)
mk_block(const u16* __restrict__ x, const u16* __restrict__ Sre, const u16* __restrict__ Sim, const u16* __restrict__ Dre,
         const u16* __restrict__ Dim, const u16* __restrict__ Wg, const u16* __restrict__ Wu, const u16* __restrict__ Wn,
         const u16* __restrict__ cvec, const u16* __restrict__ wmod, u16* __restrict__ out, char* __restrict__ ws) {
  extern __shared__ __attribute__((aligned(16))) char smem_[];
  LAS char* sm = (LAS char*)smem_;
  const int tid = threadIdx.x, lane = tid & 63, w = __builtin_amdgcn_readfirstlane(tid >> 6);
  const int l31 = lane & 31, hh = lane >> 5, l15 = lane & 15, qq = lane >> 4;
  const int bid = blockIdx.x, xcd = bid & 7, loc = bid >> 3;
  const int tb = xcd * 4 + (loc >> 3), nt = loc & 7;
  const int cb = nt & 3, kh = nt >> 2;
  const int rb = (xcd >> 1) * 4 + loc / 7, ft = (xcd & 1) * 7 + loc % 7;
  const int w1 = w & 1, wk = w >> 1;
  const u32 lane16 = lane * 16, wl = w * 4096;
  const rsrc_t rX = mk_rsrc(x, S * D * 2);
  u32x4 xr[2];
  ld_v4p<0>(xr[0], lane16, rX, (bid * 8 + w * 2) * 1024);
  ld_v4p<1024>(xr[1], lane16, rX, (bid * 8 + w * 2) * 1024);
  STAMP(0); FS(90);
  const rsrc_t rW = mk_rsrc(ws, WS_END);
  u32 vA[4];
  sfor<4>([&](auto i) { vA[i] = (lane >> 3) * 4096 + (((lane & 7) ^ ((i & 1) * 4 + qq)) << 4) + i * (8 * 4096 - 1024); });
  auto kblk = [&](int st) { return kh * 16 + ((cb * 4 + st) & 15); };
  u64 tag;

  {
    const rsrc_t r0 = mk_rsrc(w1 == 0 ? Sre : Sim, S * S * 2);
    auto fill = [&](auto qc) {
      constexpr int q = qc, st = q >> 3, hf = (q >> 2) & 1, p = q & 3;
      if constexpr (p == 0) set_m0n<st * 16384 + hf * 4096>(w1 * 8192);
      dma<100 + st, p * 1024>(vA[p], r0, (tb * 64 + hf * 32) * 4096 + kblk(st) * 128);
    };
    asm volatile("s_load_dwordx2 %0, %1, 0x0" : "=s"(tag) : "s"(ws + WS_GEN));
    if (w >= 2) { sfor<NW1>([&](auto q) { fill(q); }); wait_vm<NW1>(); } else { wait_vm<0>(); }
    asm volatile("s_waitcnt lgkmcnt(0)" : "+s"(tag));
    tag += 1;
    *(LAS u32*)(sm + L_CNT) = 0;
    tie_v(xr[0]); tie_v(xr[1]);
    FS(91);
    float v[2][8], mu[2], rs[2];
    sfor<2>([&](auto i) {
      sfor<4>([&](auto j) { u32 d = xr[i][+j]; v[i][2 * j] = b_lo(d); v[i][2 * j + 1] = b_hi(d); });
      mu[i] = ((v[i][0] + v[i][1]) + (v[i][2] + v[i][3])) + ((v[i][4] + v[i][5]) + (v[i][6] + v[i][7]));
    });
    sfor<2>([&](auto i) { mu[i] = wave_sum(mu[i]) * (1.f / D); });
    sfor<2>([&](auto i) {
      float s = 0.f;
      sfor<8>([&](auto j) { v[i][j] -= mu[i]; s += v[i][j] * v[i][j]; });
      rs[i] = s;
    });
    sfor<2>([&](auto i) { rs[i] = __builtin_amdgcn_rsqf(wave_sum(rs[i]) * (1.f / D) + 1e-5f); });
    sfor<2>([&](auto i) {
      u32x4 o;
      sfor<4>([&](auto j) { o[+j] = pack2(v[i][2 * j] * rs[i], v[i][2 * j + 1] * rs[i]); });
      *(LAS u32x4*)(sm + L_EX + (w * 2 + i) * 1088 + lane16) = o;
    });
    bar();
    FS(92);
    if (w < 2) {
      const u32 q = (lane & 15) >> 2, pp = lane & 3;
      const u32 ta = L_EX + q * 1088 + (w * 256 + qq * 16 + pp * 4) * 2;
      sfor<4>([&](auto m) {
        u32x4 o = lds_tr(sm, ta + m * 128, ta + m * 128 + 4 * 1088);
        pub16<1>(o, rW, hh * 1024 + l31 * 16, WS_XN + (bid >> 1) * 16384 + (w * 8 + m * 2) * 1024 + (bid & 1) * 512);
      });
      bar();
      wait_vm<0, 1>();
      FS(93);
      u32 old = 0;
      if (lane == 0) old = __hip_atomic_fetch_add((LAS u32*)(sm + L_CNT), 1u, __ATOMIC_RELAXED, __HIP_MEMORY_SCOPE_WORKGROUP);
      old = __builtin_amdgcn_readfirstlane(old);
      if (old == 1 && lane < 8) post_flag(ws + WS_F1 + (lane * 256 + bid) * 128, tag);
      STAMP(14);
    } else {
      bar();
      sfor<NINIT * 8 - NW1>([&](auto q) { fill(ic<NW1 + q>{}); });
    }
  }
  const u32 tag32 = (u32)tag;
  const u32 sF = WS_F1 + (xcd * 256 + kh * 128) * 128;
  const u32 vF0 = ((cb * 32 + l31) & 127) * 128, vF1 = ((cb * 32 + 32 + lane) & 127) * 128, vF2 = ((cb * 32 + 96 + l31) & 127) * 128;
  {
    const GAS u64* q = (const GAS u64*)(ws + sF + vF0);
    for (u32 sp = 0; sp < SPIN_CAP; ++sp) {
      u64 a = __hip_atomic_load(q, RLX_AGENT);
      if (__all(a == tag)) break;
      __builtin_amdgcn_s_sleep(POLL_SLEEP);
    }
    asm volatile("" ::: "memory");
  }
  u32x2 fl[2];
  ld_v2<0>(fl[0], vF1, rW, sF);
  ld_v2<0>(fl[1], vF2, rW, sF);
  STAMP(1); FS(94);

  const rsrc_t rS = mk_rsrc(w < 2 ? Sre : Sim, S * S * 2);
  const rsrc_t rD = mk_rsrc(w < 2 ? Dre : Dim, D * D * 2);
  u32 vD[4], vX[4];
  sfor<4>([&](auto i) { vD[i] = (lane >> 3) * 1024 + (((lane & 7) ^ ((i & 1) * 4 + qq)) << 4) + i * (8 * 1024 - 1024); vX[i] = lane16 + i * 16384; });
  const u32 sA = (tb * 64 + w1 * 32) * 4096, sD = (nt * 64 + w1 * 32) * 1024;
  const u32 sXN = WS_XN + (cb * 4 + w) * 1024;
  auto a_dma = [&](auto stc, auto pc) { constexpr int st = stc, p = pc; dma<100 + st, p * 1024>(vA[p], rS, sA + kblk(st) * 128); };
  auto d_dma = [&](auto sc_, auto pc) { constexpr int s = sc_, p = pc; dma<2, p * 1024>(vD[p], rD, sD + s * 128); };
  u32x4 xw[4][4];
  auto xn_ld = [&](auto stc, auto ic_) { constexpr int st = stc, i = ic_; ld_a<0>(xw[st & 3][i], vX[i], rW, sXN + kblk(st) * 65536); };
  u32x4 mg[8], gvd, wm[4], xres[2];
  u32x2 cr;
  const int ukq = bid < 224 ? bid / 56 : 3, ucb = bid % 56;
  const int mkq = bid < 192 ? bid / 48 : 3, mcb = bid % 48;
  sfor<3>([&](auto kc) {
    sfor<4>([&](auto i) { xn_ld(kc, i); });
    if constexpr (kc == 1) {
      const rsrc_t rM = mk_rsrc(wmod, D * 6 * D * 2);
      const rsrc_t rC = mk_rsrc(cvec, D * 2);
      sfor<4>([&](auto r) { ld_v4p<0>(wm[r], (lane >> 3) * 4 * 6144 + (lane & 7) * 16, rM, (mkq * 128 + w * 32 + r) * 6144 + mcb * 128); });
      ld_v2p<0>(cr, (lane >> 3) * 8, rC, (mkq * 128 + w * 32) * 2);
      const rsrc_t rV = mk_rsrc(bid < 128 ? Dre : Dim, D * D * 2);
      ld_v4p<0>(gvd, lane16, rV, ((bid & 127) * 4 + w) * 1024);
    }
  });
  sfor<8 - NINIT>([&](auto s) { set_m0n<(NINIT + s) * 16384>(wl); sfor<4>([&](auto p) { a_dma(ic<NINIT + s>{}, p); }); });

  u32 aB[4][2];
  sfor<4>([&](auto i) { sfor<2>([&](auto hf) { aB[i][hf] = l31 * 128 + (((2 * i + hh) ^ ((l31 >> 1) & 7)) << 4) + hf * 65536; tie_v(aB[i][hf]); }); });
  u32x4 af[2][4], rf;
  auto a_rd = [&](int t, int j) { af[t & 1][j] = lds16(sm, aB[t & 3][(t >> 4) & 1] + ((t >> 2) & 3) * 16384 + j * 4096); };
  f32x16 acc[4], rsa;
  u32x4 ones = {0x3f803f80u, 0x3f803f80u, 0x3f803f80u, 0x3f803f80u};
  tie_v(ones);
  const bool h0 = (lane >> 3) & 1;
  u32 lM = L_MODV; tie_v(lM);
  const u32 vM2 = (w == 0 ? 512 + cb * 128 + 2 * lane : w == 1 ? 1024 + hh * 1024 + nt * 64 + 2 * l31 : (w == 2 ? 1536 : 2048) + ukq * 128 + 2 * lane) * 8;
  float ma[8];
  bar_vm<S1.pre - 1 - S1.posx[0][0], 100>();
  sfor<4>([&](auto j) { a_rd(0, j); });
  rf = lds16(sm, aB[0][0] + wl);
  tie_v(fl[0]); tie_v(fl[1]);
  for (u32 sp = 0; sp < SPIN_CAP; ++sp) {
    const u32 th = (u32)(tag >> 32);
    if (__all(fl[0][0] == tag32 && fl[0][1] == th && fl[1][0] == tag32 && fl[1][1] == th)) break;
    __builtin_amdgcn_s_sleep(POLL_SLEEP);
    ld_v2<0>(fl[0], vF1, rW, sF);
    ld_v2<0>(fl[1], vF2, rW, sF);
    wait_vm<0>();
    tie_v(fl[0]); tie_v(fl[1]);
  }
  sfor<64>([&](auto tc) {
    constexpr int t = tc, k = t >> 2, i = t & 3, g = t - 3;
    SCHED();
    wait_a<S1.top[k][i] - 1 - S1.posx[k][i]>(xw[k & 3][i]);
    sfor<4>([&](auto mc) {
      constexpr int m = mc;
      if constexpr (m == 2 && i == 3 && k < 8 + ND1) set_m0<(k & 7) * 16384>(wl);
      mm_av<t == 0>(acc[m], xw[k & 3][i], af[t & 1][m]);
      if constexpr (t < 16 && m == 0) mm_vv<t == 0>(rsa, ones, rf);
      SCHED();
      if constexpr (i != 3) a_rd(t + 1, m);
      if constexpr (m == 1 && i == 3) {
        constexpr int na = k < 15 ? S1.bar[k] - 1 - S1.posa[k + 1] : 63;
        constexpr int ne = k == P1_MODC ? S1.bar[k] - 1 - S1.pose : k == P1_MODUSE ? S1.bar[k] - 1 - S1.posm : 63;
        bar_vm<(na < ne ? na : ne), (k < 15 ? 101 + k : 0)>();
        FS(k);
        if constexpr (t < 63) { a_rd(t + 1, 0); a_rd(t + 1, 1); }
      }
      if constexpr (m >= 2 && i == 3 && t < 63) a_rd(t + 1, m);
      if constexpr (m == 2 && t < 15) rf = lds16(sm, aB[(t + 1) & 3][0] + ((t + 1) >> 2) * 16384 + wl);
      if constexpr (m == 2 && i == 3) {
        if constexpr (k == P1_MODC || k == P1_MODC + 1) {
          if constexpr (k == P1_MODC) { sfor<4>([&](auto r) { tie_v(wm[r]); }); tie_v(cr); sfor<8>([&](auto q) { ma[q] = 0.f; }); }
          sfor<2>([&](auto rr) {
            constexpr int r = 2 * (k - P1_MODC) + rr;
            u32 cd = cr[r >> 1];
            float cv = (r & 1) ? b_hi(cd) : b_lo(cd);
            float sc = cv * __builtin_amdgcn_rcpf(1.f + __builtin_amdgcn_exp2f(-1.4426950408889634f * cv));
            sfor<4>([&](auto jj) { u32 d = wm[r][+jj]; ma[2 * jj] += sc * b_lo(d); ma[2 * jj + 1] += sc * b_hi(d); });
          });
          if constexpr (k == P1_MODC + 1) {
            float b4[4], b2[2];
            sfor<4>([&](auto jj) { b4[jj] = red_x32(ma[jj], ma[4 + jj]); });
            sfor<2>([&](auto jj) { b2[jj] = red_x16(b4[jj], b4[2 + jj]); });
            *(LAS float*)(sm + lM + (L_MP - L_MODV) + w * 256 + ((lane & 7) * 8 + (lane >> 3)) * 4) = red_x8(b2[0], b2[1], h0);
          } else {
            sfor<8>([&](auto q) { tie_v(ma[q]); });
          }
        }
        if constexpr (k == P1_MODC + 2) {
          if (w == 0 && bid < 192) {
            float s = 0.f;
            sfor<4>([&](auto r) { s += *(LAS float*)(sm + lM + (L_MP - L_MODV) + r * 256 + lane * 4); });
            u32x2 gr = {__float_as_uint(s), tag32};
            pub8<0>(gr, rW, lane * 8, WS_MODG + (mkq * 3072 + mcb * 64) * 8);
          }
        }
        if constexpr (k == P1_MODLD || k == P1_MODLD + 1) {
          sfor<2>([&](auto qc) { constexpr int q = 2 * (k - P1_MODLD) + qc;
            ld_v4<0>(mg[2 * q], tid * 16, rW, WS_MODG + q * 3072 * 8); ld_v4<0>(mg[2 * q + 1], vM2, rW, WS_MODG + q * 3072 * 8); });
        }
        if constexpr (k == P1_XR)
          sfor<2>([&](auto p) { ld_v4p<p * 64>(xres[p], l15 * 1024 + (qq & 1) * 32 + (qq >> 1) * 16, rX, (tb * 64 + w * 16) * 1024 + nt * 128); });
        if constexpr (k == P1_MODUSE) {
          sfor<8>([&](auto q) { tie_v(mg[q]); });
          for (u32 sp = 0; sp < SPIN_CAP; ++sp) {
            bool ok = true;
            sfor<8>([&](auto q) { ok = ok && mg[q][1] == tag32 && mg[q][3] == tag32; });
            if (__all(ok)) break;
            __builtin_amdgcn_s_sleep(1);
            sfor<4>([&](auto q) { ld_v4<0>(mg[2 * q], tid * 16, rW, WS_MODG + q * 3072 * 8); ld_v4<0>(mg[2 * q + 1], vM2, rW, WS_MODG + q * 3072 * 8); });
            wait_vm<0>();
            sfor<8>([&](auto q) { tie_v(mg[q]); });
          }
          sfor<2>([&](auto e) {
            f32x2 mv;
            sfor<2>([&](auto o) {
              u32 a = mg[e][2 * o], b = mg[2 + e][2 * o], c = mg[4 + e][2 * o], d = mg[6 + e][2 * o];
              mv[+o] = (__uint_as_float(a) + __uint_as_float(b)) + (__uint_as_float(c) + __uint_as_float(d)); });
            *(LAS f32x2*)(sm + lM + e * 2048 + tid * 8) = mv;
          });
        }
        if constexpr (k == P1_V) {
          tie_v(gvd);
          f32x4 s0 = ldsf4(sm, lM + lane * 32), s1 = ldsf4(sm, lM + lane * 32 + 16);
          u32 d0 = gvd[0], d1 = gvd[1], d2 = gvd[2], d3 = gvd[3];
          float v = s0[0] * b_lo(d0) + s0[1] * b_hi(d0) + s0[2] * b_lo(d1) + s0[3] * b_hi(d1) + s1[0] * b_lo(d2) + s1[1] * b_hi(d2) + s1[2] * b_lo(d3) + s1[3] * b_hi(d3);
          v = wave_sum(v);
          if (lane == 0) { u32x2 gr = {__float_as_uint(v), tag32}; pub8<0>(gr, rW, 0, WS_VG + (bid * 4 + w) * 8); }
        }
      }
      if constexpr (m == 2 && g >= 0 && (g >> 2) < 8 + ND1) {
        if constexpr ((g >> 2) < 8) a_dma(ic<8 + (g >> 2)>{}, ic<(g & 3)>{}); else d_dma(ic<(g >> 2) - 8>{}, ic<(g & 3)>{});
      }
      if constexpr (m == 3 && k + 3 < 16) xn_ld(ic<k + 3>{}, ic<i>{});
      SCHED();
    });
  });
  STAMP(2); FS(32);
  u32x4 gvw[4], warm[2];
  {
    f32x4 sc[4];
    sfor<4>([&](auto g) { sc[g] = ldsf4(sm, lM + (512 + w * 32 + g * 8 + hh * 4) * 4) + 1.f; });
    mm_fence(acc[0], acc[1]); mm_tie(acc[2], acc[3]); mm_tie(rsa, rsa);
    const u32 vP = (l31 >> 4) * 65536 + hh * 256 + l15 * 16;
    sfor<4>([&](auto jc) {
      constexpr int j = jc;
      u32 pk[4][2];
      sfor<4>([&](auto g) {
        f32x4 m = {acc[j][4 * g], acc[j][4 * g + 1], acc[j][4 * g + 2], acc[j][4 * g + 3]};
        m = j < 2 ? m * sc[g] : -m * sc[g];
        pk[g][0] = pack2(m[0], m[1]); pk[g][1] = pack2(m[2], m[3]);
      });
      sfor<2>([&](auto q) {
        pub16<3, q * 512>(chunk_swap(pk[2 * q][0], pk[2 * q][1], pk[2 * q + 1][0], pk[2 * q + 1][1]), rW, vP,
                          WS_PQ + ((tb * 4 + (j & 1) * 2) * 64 + kh * 32 + (j >> 1) * 16 + cb * 4 + w) * 1024);
      });
    });
    pub4<3>(__float_as_uint(rsa[0]), rW, l31 * 4, WS_RS + ((tb * 8 + nt) * 128 + w * 32) * 4);
    bar();
    FS(39);
    sfor<2>([&](auto s) { set_m0n<(ND1 + s) * 16384>(wl); sfor<4>([&](auto p) { d_dma(ic<ND1 + s>{}, p); }); });
    bar_vm<8, 3>();
    if (tid == 0) post_flag(ws + WS_F2 + (tb * 8 + nt) * 128, tag);
    sfor<6 - ND1>([&](auto s) { set_m0n<(ND1 + 2 + s) * 16384>(wl); sfor<4>([&](auto p) { d_dma(ic<ND1 + 2 + s>{}, p); }); });
    const rsrc_t rU = mk_rsrc(ucb < 28 ? Wg : Wu, D * F * 2);
    const u32 vu = (tid >> 3) * (F * 2) + (lane & 7) * 16;
    sfor<4>([&](auto r) { ld_v4p<0>(gvw[r], vu, rU, (ukq * 128 + r * 32) * (F * 2) + (ucb % 28) * 128); });
    const rsrc_t rN = mk_rsrc(Wn, F * D * 2);
    ld_v4p<0>(warm[0], tid * 16, rN, bid * 7168);
    ld_v4p<0>(warm[1], tid * 16, rN, bid * 7168 + 4096);
  }
  STAMP(3); FS(33);
  poll_flags<8>(ws + WS_F2 + tb * 8 * 128, tag, lane);
  if (bid == 0 && tid == 0) post_flag(ws + WS_GEN, tag);
  STAMP(4); FS(34);

  f32x4 x1[4];
  {
    constexpr int PRE = 5;
    u32x4 pa[32][2];
    const u32 so = WS_PQ + (tb * 4 + w) * 65536;
    const u32 vP1 = lane16 + 32768;
    auto pa_ld = [&](auto sc_, auto hc) {
      constexpr int s = sc_, h = hc;
      ld_a<(s & 1) * 1024>(pa[s][h], h ? vP1 : lane16, rW, so + (((s >> 2) + nt) & 7) * 2048 + ((s >> 1) & 1) * 16384);
    };
    sfor<PRE>([&](auto s) { pa_ld(s, ic<0>{}); pa_ld(s, ic<1>{}); });
    u32 rp[4];
    u32x4 vg;
    u32 dB[2];
    sfor<2>([&](auto u) { dB[u] = l15 * 128 + (((4 * u + qq) ^ ((l15 >> 1) & 7)) << 4); });
    bar_vm<2 * PRE, 2>();
    f32x4 y[4];
    u32x4 df[2][4];
    u32 da;
    auto d_ad = [&](int s) { da = dB[s & 1] + (((s >> 2) + nt) & 7) * 16384; };
    auto d_rd = [&](int s, int c) { df[s & 1][c] = lds16(sm, da + ((s >> 1) & 1) * 8192 + c * 2048); };
    d_ad(0);
    sfor<4>([&](auto c) { d_rd(0, c); });
    sfor<32>([&](auto sc_) {
      constexpr int s = sc_;
      constexpr int issued = 2 * (s + PRE) < 69 ? 2 * (s + PRE) : 69;
      SCHED();
      if constexpr (s < 31) d_ad(s + 1);
      wait_a<issued - 1 - 2 * s>(pa[s][0]);
      if constexpr ((s & 3) == 0) FS(80 + (s >> 2));
      sfor<8>([&](auto mc) {
        constexpr int m = mc, c = m & 3, h = m >> 2, n = 2 * (s + PRE) + h;
        if constexpr (m == 4) wait_a<(issued < 69 ? issued + 1 : 69) - 1 - (2 * s + 1)>(pa[s][1]);
        m16_va<s == 0 && h == 0>(y[c], df[s & 1][c], pa[s][h]);
        SCHED();
        if constexpr (h == 0 && s < 31) d_rd(s + 1, c);
        if constexpr (c == 1) {
          if constexpr (n < 64) pa_ld(ic<s + PRE>{}, ic<h>{});
          else if constexpr (n < 68) ld_v1<(n - 64) * 512>(rp[n - 64], ((qq >> 1) * 512 + (qq & 1) * 64 + w * 16 + l15) * 4, rW, WS_RS + tb * 4096);
          else if constexpr (n == 68) ld_v4<0>(vg, (hh * 512 + nt * 64 + 2 * l31) * 8, rW, WS_VG);
        }
        SCHED();
      });
    });
    STAMP(5); FS(35);
    f32x4 ga[4], cbv[4];
    sfor<4>([&](auto c) { const u32 eo = (c * 16 + qq * 4) * 4; ga[c] = ldsf4(sm, lM + 640 * 4 + eo); cbv[c] = ldsf4(sm, lM + 704 * 4 + eo) + 1.f; });
    wait_vm<0>();
    sfor<4>([&](auto p) { tie_v(rp[p]); });
    tie_v(vg); tie_v(xres[0]); tie_v(xres[1]);
    for (u32 sp = 0; sp < SPIN_CAP; ++sp) {
      if (__all(vg[1] == tag32 && vg[3] == tag32)) break;
      __builtin_amdgcn_s_sleep(1);
      ld_v4<0>(vg, (hh * 512 + nt * 64 + 2 * l31) * 8, rW, WS_VG);
      wait_vm<0>();
      tie_v(vg);
    }
    { u32 a = vg[0], b = vg[2]; u32x2 o = {a, b}; *(LAS u32x2*)(sm + lM + (L_RS - L_MODV) + w * 512 + lane * 8) = o; }
    float rre, rim;
    {
      const float r4 = add_x32((__uint_as_float(rp[0]) + __uint_as_float(rp[1])) + (__uint_as_float(rp[2]) + __uint_as_float(rp[3])));
      auto r = __builtin_amdgcn_permlane16_swap(__float_as_uint(r4), __float_as_uint(r4), false, false);
      u32 a = r[0], b = r[1]; rre = __uint_as_float(a); rim = __uint_as_float(b);
    }
    u32 xg[4][2];
    sfor<2>([&](auto p) { u32x4 t = chunk_swap16(xres[p][0], xres[p][1], xres[p][2], xres[p][3]); xg[2 * p][0] = t[0]; xg[2 * p][1] = t[1]; xg[2 * p + 1][0] = t[2]; xg[2 * p + 1][1] = t[3]; });
    m16_fence(y);
    u32 pk[4][2];
    sfor<4>([&](auto c) {
      const u32 eo = (L_RS - L_MODV) + w * 512 + (c * 16 + qq * 4) * 4;
      f32x4 vre = ldsf4(sm, lM + eo), vim = ldsf4(sm, lM + 256 + eo);
      f32x4 xv = {b_lo(xg[c][0]), b_hi(xg[c][0]), b_lo(xg[c][1]), b_hi(xg[c][1])};
      x1[c] = xv + ga[c] * (y[c] + rre * vre - rim * vim);
      f32x4 s = x1[c] * cbv[c];
      pk[c][0] = pack2(s[0], s[1]); pk[c][1] = pack2(s[2], s[3]);
    });
    sfor<2>([&](auto p) {
      pub16<4, p * 2048>(chunk_swap16(pk[2 * p][0], pk[2 * p][1], pk[2 * p + 1][0], pk[2 * p + 1][1]), rW,
                         (qq & 1) * 1024 + (qq >> 1) * 512 + l15 * 16, WS_X1 + ((tb * 2 + wk) * 32 + nt * 4) * 1024 + w1 * 256);
    });
    const f32x4 s4 = (x1[0] + x1[1]) + (x1[2] + x1[3]);
    const float mean = add_x32(add_x16((s4[0] + s4[1]) + (s4[2] + s4[3]))) * (1.f / 64);
    f32x4 q4 = {0.f, 0.f, 0.f, 0.f};
    sfor<4>([&](auto c) { f32x4 d = x1[c] - mean; q4 += d * d; });
    const float m2 = add_x32(add_x16((q4[0] + q4[1]) + (q4[2] + q4[3])));
    u32x2 o = {__float_as_uint(mean), __float_as_uint(m2)};
    pub8<4>(o, rW, l15 * 8, WS_ST + ((tb * 2 + wk) * 8 + nt) * 256 + w1 * 128);
    bar();
  }
  FS(36);
  const rsrc_t rG = mk_rsrc(w < 2 ? Wg : Wu, D * F * 2);
  const rsrc_t rN = mk_rsrc(Wn, F * D * 2);
  u32 vG[4], vN[4];
  sfor<4>([&](auto i) {
    vG[i] = (lane >> 3) * (F * 2) + (((lane & 7) ^ ((qq & 1) << 2)) << 4) + i * (8 * F * 2 - 1024);
    vN[i] = (lane >> 3) * 1024 + (((lane & 7) ^ (((qq & 1) + 2 * (i & 1)) << 1)) << 4) + i * (8 * 1024 - 1024);
  });
  const u32 sG = w1 * 32 * (F * 2) + ft * 256, sN = (wk * 128 + w1 * 32) * 1024 + nt * 128;
  auto g_dma = [&](auto jc, auto ic_) {
    constexpr int j = jc, i = ic_;
    dma<200 + j, i * 1024>(vG[i], rG, sG + ((j + ft) & 7) * (64 * F * 2) + (j >> 3) * 128);
  };
  auto rot7 = [&](int j) { int pr = j + nt; pr = pr >= 7 ? pr - 7 : pr; return pr >= 7 ? pr - 7 : pr; };
  auto n_dma = [&](auto jc, auto ic_) {
    constexpr int j = jc, i = ic_;
    dma<300 + j, i * 1024>(vN[i], rN, sN + rot7(j % 7) * (256 * 1024) + (j / 7) * 65536);
  };
  const bool act4 = loc < 28;
  u32x4 ug[4];
  const u32 vUV = ((tid >> 7) * F + ft * 128 + (tid & 127)) * 16;
  if (act4) {
    sfor<3>([&](auto j) { set_m0n<j * 16384>(wl); sfor<4>([&](auto i) { g_dma(j, i); }); });
  } else {
    sfor<3>([&](auto j) { set_m0n<j * 16384>(wl); sfor<4>([&](auto i) { n_dma(j, i); }); });
  }
  bar_vm<12, 4>();
  if (tid == 0) post_flag(ws + WS_F3 + (tb * 8 + nt) * 128, tag);
  FS(100);
  if (act4) {
    sfor<NG0 - 3>([&](auto j) { set_m0n<(j + 3) * 16384>(wl); sfor<4>([&](auto i) { g_dma(ic<j + 3>{}, i); }); });
  } else {
    sfor<5>([&](auto j) { set_m0n<(j + 3) * 16384>(wl); sfor<4>([&](auto i) { n_dma(ic<j + 3>{}, i); }); });
  }
  FS(101);
  {
    sfor<4>([&](auto i) { tie_v(gvw[i]); });
    tie_v(warm[0]); tie_v(warm[1]);
    float a[16];
    sfor<16>([&](auto j) { a[j] = 0.f; });
    sfor<4>([&](auto i) {
      const float sh = *(LAS float*)(sm + lM + (768 + (tid >> 3) + i * 32) * 4);
      const float cs = 1.f + *(LAS float*)(sm + lM + (896 + (tid >> 3) + i * 32) * 4);
      sfor<4>([&](auto j) { u32 d = gvw[i][+j]; float lo = b_lo(d), hi = b_hi(d);
        a[2 * j] += cs * lo; a[2 * j + 1] += cs * hi; a[8 + 2 * j] += sh * lo; a[8 + 2 * j + 1] += sh * hi; });
    });
    float b8[8], b4[4];
    sfor<8>([&](auto j) { b8[j] = red_x32(a[j], a[8 + j]); });
    sfor<4>([&](auto j) { b4[j] = red_x16(b8[j], b8[4 + j]); });
    f32x2 o = {red_x8(b4[0], b4[2], h0), red_x8(b4[1], b4[3], h0)};
    *(LAS f32x2*)(sm + lM + (L_UVP - L_MODV) + w * 512 + (hh * 64 + (lane & 7) * 8 + ((lane >> 3) & 3) * 2) * 4) = o;
  }
  bar();
  if (w == 0 && bid < 224) {
    float su = 0.f, sv = 0.f;
    sfor<4>([&](auto i) { su += *(LAS float*)(sm + lM + (L_UVP - L_MODV) + i * 512 + lane * 4); sv += *(LAS float*)(sm + lM + (L_UVP - L_MODV) + i * 512 + 256 + lane * 4); });
    u32x4 g = {__float_as_uint(su), tag32, __float_as_uint(sv), tag32};
    pub16<0>(g, rW, lane16, WS_UVG + (ukq * 3584 + (ucb / 28) * F + (ucb % 28) * 64) * 16);
  }
  STAMP(6); FS(37);
  u32x4 gbg[4];
  const GAS u64* pF4A = (const GAS u64*)(ws + WS_F4A + ((tb >> 1) * 16 + (lane < 14 ? lane : 0)) * 128);
  const GAS u64* pF4B = (const GAS u64*)(ws + WS_F4B + ((tb >> 1) * 16 + (lane < 14 ? lane : 0)) * 128);

  if (act4) {
    u32 tB[2][2];
    {
      const u32 q = l15 >> 2, pp = lane & 3;
      sfor<2>([&](auto c) { sfor<2>([&](auto hf) {
        tB[c][hf] = (8 * hh + q) * 128 + ((4 * (c ^ (q >> 1)) + 2 * (qq & 1) + (pp >> 1)) << 4) + 8 * (pp & 1) + hf * 65536; tie_v(tB[c][hf]); }); });
    }
    poll_flags<16>(ws + WS_F3 + rb * 16 * 128, tag, lane);
    STAMP(7); FS(38);
    u32x2 stv[4];
    sfor<4>([&](auto i) { ld_v2<i * 256>(stv[i], hh * 1024 + l31 * 8, rW, WS_ST + (rb * 4 + w) * 2048); });
    u32x4 xa[32];
    const u32 so = WS_X1 + (rb * 4 + w) * 32 * 1024;
    auto xa_ld = [&](auto sc_) { constexpr int s = sc_; ld_a<(s & 3) * 1024>(xa[s], lane16, rW, so + (((s >> 2) + ft) & 7) * 4096); };
    sfor<P4_PRE>([&](auto s) { xa_ld(s); });
    bar_vm<P4_PRE, 200, 200 + NG0 - 1>();
    f32x16 z[2][4];
    u32x4 wf[2][4], uf[4];
    auto w_rd = [&](int t, int j) {
      const u32 o = tB[j & 1][(t >> 4) & 1] + ((t >> 2) & 3) * 16384 + (j >> 1) * 8192 + (t & 3) * 2048;
      wf[t & 1][j] = lds_tr(sm, o, o + 512);
    };
    sfor<4>([&](auto j) { w_rd(0, j); });
    float rr, sd;
    u32x4 xst;
    {
      sfor<4>([&](auto i) { tie_v(stv[i]); });
      float ms = 0.f;
      sfor<4>([&](auto i) { u32 a = stv[i][0]; ms += __uint_as_float(a); });
      const float mean = add_x32(ms) * (1.f / 8);
      float q = 0.f;
      sfor<4>([&](auto i) { u32 a = stv[i][0], b = stv[i][1]; float d = __uint_as_float(a) - mean; q += __uint_as_float(b) + 64.f * d * d; });
      const float var = add_x32(q) * (1.f / D) + 1e-5f;
      rr = __builtin_amdgcn_rsqf(var);
      sd = var * rr;
      const float mh = b_lo(pack2(mean, 0.f)), sh = b_lo(pack2(sd, 0.f));
      xst[0] = hh ? 0u : pack2(-mh, -mh); xst[1] = hh ? 0u : pack2(mh - mean, mh - mean);
      xst[2] = hh ? 0u : pack2(sh, sh); xst[3] = hh ? 0u : pack2(sd - sh, sd - sh);
      tie_v(xst); tie_v(rr); tie_v(sd);
    }
    auto u_rd = [&](int p, int j) { uf[j] = lds16(sm, lM + (L_UV - L_MODV) + ((j >> 1) * 128 + p * 64 + (j & 1) * 32 + l31) * 16); };
    constexpr float C1 = -2.f * 0.7978845608028654f * 1.4426950408889634f, C3 = C1 * 0.044715f;
    float c1v = C1, c3v = C3;
    tie_v(c1v); tie_v(c3v);
    const u32 sAct = WS_ACT + ((rb * 8 + 2 * w) * 56 + ft * 4) * 1024;
    const u32 vAct = (l31 >> 4) * (56 * 1024) + hh * 256 + l15 * 16;
    u32 pk[4];
    Ge ge;
    sfor<64>([&](auto tc) {
      constexpr int t = tc, j = t >> 2, s4 = t & 3, p = j >> 3;
      constexpr bool many = t < 4 * (8 - NG0) + 4;
      SCHED();
      if constexpr (p == 0) wait_a<S4.wait[t] - 1 - S4.posx[t]>(xa[t]);
      sfor<4>([&](auto mc) {
        constexpr int m = mc;
        if constexpr (m == 0 && t < 4 * (8 - NG0)) set_m0<(NG0 + (t >> 2)) * 16384>(wl);
        if constexpr (m == 3 && t >= 3 && (many || s4 == 3)) set_m0<(((t - 3) >> 2) & 7) * 16384>(wl);
        mm_va<(t & 31) == 0>(z[p][m], wf[t & 1][m], xa[t & 31]);
        if constexpr (t == 33) mm_vv<false>(z[0][m], uf[m], xst);
        SCHED();
        if constexpr (s4 != 3) { w_rd(t + 1, m); }
        if constexpr (t == 32) u_rd(0, m);
        if constexpr (m == 0 && t == P4_UR) {
          wait_vm<S4.urw - 1 - S4.posug>();
          sfor<4>([&](auto q) { tie_v(ug[q]); });
          for (u32 sp = 0; sp < SPIN_CAP; ++sp) {
            bool ok = true;
            sfor<4>([&](auto q) { ok = ok && ug[q][1] == tag32 && ug[q][3] == tag32; });
            if (__all(ok)) break;
            __builtin_amdgcn_s_sleep(1);
            sfor<4>([&](auto q) { ld_v4<0>(ug[q], vUV, rW, WS_UVG + q * 3584 * 16); });
            wait_vm<0>();
            sfor<4>([&](auto q) { tie_v(ug[q]); });
          }
          u32 d[2];
          sfor<2>([&](auto e) { u32 a = ug[0][2 * e], b = ug[1][2 * e], c = ug[2][2 * e], dd = ug[3][2 * e];
            const float s = (__uint_as_float(a) + __uint_as_float(b)) + (__uint_as_float(c) + __uint_as_float(dd));
            const float hi = b_lo(pack2(s, 0.f));
            d[e] = pack2(hi, s - hi); });
          u32x4 rec = {d[0], d[0], d[1], d[1]};
          *(LAS u32x4*)(sm + lM + (L_UV - L_MODV) + tid * 16) = rec;
        }
        if constexpr (m == 0 && t < 4 * (8 - NG0)) g_dma(ic<NG0 + (t >> 2)>{}, ic<(t & 3)>{});
        if constexpr (p4_piece(t, m) >= 0) {
          constexpr int e = p4_piece(t, m) / 5, st = p4_piece(t, m) % 5;
          if constexpr (st == 0) ge_a(ge, z[0][e >> 3][2 * (e & 7)], z[0][e >> 3][2 * (e & 7) + 1], rr);
          if constexpr (st == 1) ge_b(ge, c3v, c1v);
          if constexpr (st == 2) ge_c(ge, z[0][2 + (e >> 3)][2 * (e & 7)], z[0][2 + (e >> 3)][2 * (e & 7) + 1]);
          if constexpr (st == 3) ge_d(ge, sd);
          if constexpr (st == 4) {
            pk[e & 3] = ge_e(ge);
            if constexpr ((e & 3) == 3) pub16<5, (e >> 3) * 1024 + ((e >> 2) & 1) * 512>(chunk_swap(pk[0], pk[1], pk[2], pk[3]), rW, vAct, sAct);
          }
        }
        if constexpr (m == 1 && s4 == 3) {
          constexpr int need = j + 1 >= NG0 && j < 15 ? S4.bar[j] - 1 - S4.posg[j + 1] : 63;
          bar_vm<need, (j < 15 ? 201 + j : 0)>();
          FS(40 + j);
          if constexpr (t < 63) { w_rd(t + 1, 0); w_rd(t + 1, 1); } else { u_rd(1, 0); u_rd(1, 1); }
        }
        if constexpr (m == 1) {
          if constexpr (t + P4_PRE < 32) xa_ld(ic<t + P4_PRE>{});
          else if constexpr (t >= P4_UG && t < P4_UG + 4) ld_v4<0>(ug[t - P4_UG], vUV, rW, WS_UVG + (t - P4_UG) * 3584 * 16);
        }
        if constexpr (m >= 2 && s4 == 3) { if constexpr (t < 63) w_rd(t + 1, m); else u_rd(1, m); }
        if constexpr (m == 3 && t >= 3) {
          constexpr int gj = (t - 3) >> 2, gi = (t - 3) & 3;
          if constexpr (gj < 8) g_dma(ic<8 + gj>{}, ic<gi>{}); else n_dma(ic<gj - 8>{}, ic<gi>{});
        }
        SCHED();
      });
    });
    sfor<4>([&](auto m) { mm_vv<false>(z[1][m], uf[m], xst); if constexpr (m < 3) n_dma(ic<7>{}, ic<1 + m>{}); });
    STAMP(8); FS(56);
    mm_fence(z[1][0], z[1][2]); mm_tie(z[1][1], z[1][3]);
    sfor<4>([&](auto qc) {
      constexpr int c = qc >> 1, g0 = (qc & 1) * 2;
      u32 pq[4];
      sfor<2>([&](auto gg) {
        constexpr int g = g0 + gg;
        f32x4 zg = {z[1][c][4 * g], z[1][c][4 * g + 1], z[1][c][4 * g + 2], z[1][c][4 * g + 3]};
        f32x4 zu = {z[1][2 + c][4 * g], z[1][2 + c][4 * g + 1], z[1][2 + c][4 * g + 2], z[1][2 + c][4 * g + 3]};
        f32x4 gt = rr * zg;
        f32x4 ex = gt * (C1 + C3 * gt * gt);
        sfor<4>([&](auto i) { ex[+i] = __builtin_amdgcn_exp2f(ex[+i]); });
        ex = ex * sd + sd;
        sfor<4>([&](auto i) { ex[+i] = __builtin_amdgcn_rcpf(ex[+i]); });
        f32x4 a = gt * zu * ex;
        pq[2 * gg] = pack2(a[0], a[1]); pq[2 * gg + 1] = pack2(a[2], a[3]);
      });
      pub16<6, 2048 + c * 1024 + (qc & 1) * 512>(chunk_swap(pq[0], pq[1], pq[2], pq[3]), rW, vAct, sAct);
      if constexpr (qc == 0) {
        bar_vm<S4.end + 3 + 1 - 1 - S4.possto, 5>();
        if (tid == 0) post_flag(ws + WS_F4A + (rb * 16 + ft) * 128, tag);
      }
    });
  }
  STAMP(9); FS(57);

  {
    bar();
    for (u32 sp = 0; sp < SPIN_CAP; ++sp) {
      u64 fa = __hip_atomic_load(pF4A, RLX_AGENT);
      if (__all(fa == tag)) break;
      __builtin_amdgcn_s_sleep(POLL_SLEEP);
    }
    asm volatile("" ::: "memory");
    FS(102);
    u32x4 aa[56];
    const u32 so = WS_ACT + (tb * 4 + w) * 56 * 1024;
    const u32 vA2 = lane16 + 4096;
    auto aa_ld = [&](auto sc_) {
      constexpr int s = sc_;
      ld_a<(s & 1) * 1024 + (s / 28) * 2048>(aa[s], (s & 2) ? vA2 : lane16, rW, so + rot7((s >> 2) % 7) * 8192);
    };
    sfor<P5_PRE>([&](auto s) { aa_ld(s); });
    bar_vm<P5_PRE, 6, 307>();
    if (act4 && tid == 0) post_flag(ws + WS_F4B + (rb * 16 + ft) * 128, tag);
    STAMP(10); FS(58);
    u32 tN[4][2];
    {
      const u32 q = l15 >> 2, pp = lane & 3, f = (q >> 1) + 2 * (qq & 1);
      sfor<4>([&](auto c) { sfor<2>([&](auto hf) {
        tN[c][hf] = (8 * qq + q) * 128 + ((2 * (c ^ f) + (pp >> 1)) << 4) + 8 * (pp & 1) + hf * 65536; tie_v(tN[c][hf]); }); });
    }
    f32x4 y[4];
    u32x4 nf[2][4];
    auto n_rd = [&](int t, int c) {
      const u32 o = tN[c][(t >> 4) & 1] + ((t >> 2) & 3) * 16384 + (t & 3) * 4096;
      nf[t & 1][c] = lds_tr(sm, o, o + 512);
    };
    sfor<4>([&](auto c) { n_rd(0, c); });
    sfor<4>([&](auto q) { ld_v4<0>(gbg[q], (2560 + nt * 64 + 2 * l31) * 8, rW, WS_MODG + q * 3072 * 8); });
    sfor<56>([&](auto tc) {
      constexpr int t = tc, i = t >> 2, s4 = t & 3;
      SCHED();
      if constexpr (t == 28) {
        for (u32 sp = 0; sp < SPIN_CAP; ++sp) {
          u64 fb = __hip_atomic_load(pF4B, RLX_AGENT);
          if (__all(fb == tag)) break;
          __builtin_amdgcn_s_sleep(POLL_SLEEP);
        }
        asm volatile("" ::: "memory");
        STAMP(11); FS(59);
        sfor<P5_PRE>([&](auto s) { aa_ld(ic<28 + s>{}); });
      }
      wait_a<S5.wait[t] - 1 - S5.posa[t] + (t < P5_PRE ? 4 : 0)>(aa[t]);
      sfor<4>([&](auto mc) {
        constexpr int m = mc;
        if constexpr (m == 2 && s4 == 3 && t < 27) set_m0<(i & 7) * 16384>(wl);
        m16_va<t == 0>(y[m], nf[t & 1][m], aa[t]);
        SCHED();
        if constexpr (s4 != 3) n_rd(t + 1, m);
        if constexpr (m == 1 && s4 == 3 && i < 13) { bar_vm<63, (i >= 7 ? 301 + i : 0)>(); FS(60 + i); n_rd(t + 1, 0); n_rd(t + 1, 1); }
        if constexpr (m >= 2 && s4 == 3 && t < 55) n_rd(t + 1, m);
        if constexpr (m == 1 && (t % 28) + P5_PRE < 28) aa_ld(ic<t + P5_PRE>{});
        if constexpr (m == 3 && t >= 3 && t < 27) n_dma(ic<8 + ((t - 3) >> 2)>{}, ic<((t - 3) & 3)>{});
        SCHED();
      });
    });
    STAMP(12); FS(73);
    sfor<4>([&](auto q) { tie_v(gbg[q]); });
    {
      for (u32 sp = 0; sp < SPIN_CAP; ++sp) {
        bool ok = true;
        sfor<4>([&](auto q) { ok = ok && gbg[q][1] == tag32 && gbg[q][3] == tag32; });
        if (__all(ok)) break;
        sfor<4>([&](auto q) { ld_v4<0>(gbg[q], (2560 + nt * 64 + 2 * l31) * 8, rW, WS_MODG + q * 3072 * 8); });
        wait_vm<0>();
        sfor<4>([&](auto q) { tie_v(gbg[q]); });
      }
      f32x2 mv;
      sfor<2>([&](auto o) {
        u32 a = gbg[0][2 * o], b = gbg[1][2 * o], c = gbg[2][2 * o], d = gbg[3][2 * o];
        mv[+o] = (__uint_as_float(a) + __uint_as_float(b)) + (__uint_as_float(c) + __uint_as_float(d)); });
      *(LAS f32x2*)(sm + lM + (L_RS - L_MODV) + w * 512 + l31 * 8) = mv;
    }
    m16_fence(y);
    u32 pk[4][2];
    sfor<4>([&](auto c) {
      f32x4 m = x1[c] + ldsf4(sm, lM + (L_RS - L_MODV) + w * 512 + (c * 16 + qq * 4) * 4) * y[c];
      pk[c][0] = pack2(m[0], m[1]); pk[c][1] = pack2(m[2], m[3]);
    });
    const rsrc_t rO = mk_rsrc(out, S * D * 2);
    sfor<2>([&](auto p) {
      __builtin_amdgcn_raw_buffer_store_b128(chunk_swap16(pk[2 * p][0], pk[2 * p][1], pk[2 * p + 1][0], pk[2 * p + 1][1]), rO,
                                             l15 * 1024 + (qq & 1) * 32 + (qq >> 1) * 16 + p * 64, (tb * 64 + w * 16) * 1024 + nt * 128, 16);
    });
  }
  STAMP(13); FS(74);
}

extern "C" void kernel_launch(void* const* d_in, const int* in_sizes, int n_in, void* d_out, int out_size, void* d_ws,
                              size_t ws_size, hipStream_t stream) {
  (void)in_sizes; (void)n_in; (void)out_size; (void)ws_size;
  (void)hipFuncSetAttribute((const void*)mk_block, hipFuncAttributeMaxDynamicSharedMemorySize, LDS_BYTES);
  mk_block<<<256, 256, LDS_BYTES, stream>>>((const u16*)d_in[0], (const u16*)d_in[1], (const u16*)d_in[2], (const u16*)d_in[3],
                                            (const u16*)d_in[4], (const u16*)d_in[6], (const u16*)d_in[7], (const u16*)d_in[8],
                                            (const u16*)d_in[9], (const u16*)d_in[10], (u16*)d_out, (char*)d_ws);
}
```

```cpp
#include <hip/hip_runtime.h>
#include <utility>
#pragma clang diagnostic ignored "-Winline-asm"

typedef unsigned short u16;
typedef unsigned u32;
typedef unsigned long long u64;
typedef float f32x2 __attribute__((ext_vector_type(2)));
typedef float f32x4 __attribute__((ext_vector_type(4)));
typedef float f32x16 __attribute__((ext_vector_type(16)));
typedef unsigned u32x2 __attribute__((ext_vector_type(2)));
typedef unsigned u32x4 __attribute__((ext_vector_type(4)));
typedef short s16x4 __attribute__((ext_vector_type(4)));
typedef short s16x8 __attribute__((ext_vector_type(8)));
typedef __bf16 bf16x2 __attribute__((ext_vector_type(2)));
typedef __bf16 bf16x8 __attribute__((ext_vector_type(8)));
typedef __amdgpu_buffer_rsrc_t rsrc_t;
#define LAS __attribute__((address_space(3)))
#define GAS __attribute__((address_space(1)))
#define DI __device__ __forceinline__
#define RLX_AGENT __ATOMIC_RELAXED, __HIP_MEMORY_SCOPE_AGENT

constexpr int S = 2048, D = 512, F = 1792;
constexpr u32 WS_XN = 0;
constexpr u32 WS_PQ = WS_XN + (2u << 20);
constexpr u32 WS_X1 = WS_PQ + (8u << 20);
constexpr u32 WS_ACT = WS_X1 + (2u << 20);
constexpr u32 WS_RS = WS_ACT + (7u << 20);
constexpr u32 WS_ST = WS_RS + (256u << 10);
constexpr u32 WS_MODG = WS_ST + (256u << 10);
constexpr u32 WS_VG = WS_MODG + 4 * 3072 * 8;
constexpr u32 WS_UVG = WS_VG + 1024 * 8;
constexpr u32 WS_F1 = WS_UVG + 4 * 2 * 3584 * 8;
constexpr u32 WS_F2 = WS_F1 + 8 * 256 * 128;
constexpr u32 WS_F3 = WS_F2 + 256 * 128;
constexpr u32 WS_F4A = WS_F3 + 256 * 128;
constexpr u32 WS_F4B = WS_F4A + 256 * 128;
constexpr u32 WS_GEN = WS_F4B + 256 * 128;
constexpr u32 WS_TS = WS_GEN + 256;
constexpr u32 WS_TS2 = WS_TS + 256 * 16 * 8;
constexpr u32 WS_END = WS_TS2 + 256 * 256 * 4;
constexpr u32 L_EX = 128 * 1024;
constexpr u32 L_MODV = 144 * 1024;
constexpr u32 L_UV = L_MODV + 4096;
constexpr u32 L_UVP = L_UV + 4096;
constexpr u32 L_MP = L_UVP + 2048;
constexpr u32 L_RS = L_MP + 1024;
constexpr u32 L_CNT = L_RS + 2048;
constexpr u32 LDS_BYTES = 160 * 1024;
constexpr u32 SPIN_CAP = 200000;
#define POLL_SLEEP 2

template <class Fn, int... Is> DI void sfor_impl(Fn&& f, std::integer_sequence<int, Is...>) { (f(std::integral_constant<int, Is>{}), ...); }
template <int V> using ic = std::integral_constant<int, V>;
template <int N, class Fn> DI void sfor(Fn&& f) { sfor_impl(f, std::make_integer_sequence<int, N>{}); }

DI rsrc_t mk_rsrc(const void* p, u32 bytes) { return __builtin_amdgcn_make_buffer_rsrc((void*)p, 0, bytes, 0x00020000); }
DI float b_lo(u32 v) { return __uint_as_float(v << 16); }
DI float b_hi(u32 v) { return __uint_as_float(v & 0xffff0000u); }
DI u32 pack2(float a, float b) { f32x2 p = {a, b}; bf16x2 r = __builtin_convertvector(p, bf16x2); return __builtin_bit_cast(u32, r); }

template <int IMM> DI void ld_a(u32x4& d, u32 voff, rsrc_t r, u32 soff) {
  asm volatile("buffer_load_dwordx4 %0, %1, %2, %3 offen offset:%c4 sc1" : "=a"(d) : "v"(voff), "s"(r), "s"(soff), "i"(IMM));
}
template <int IMM> DI void ld_v4(u32x4& d, u32 voff, rsrc_t r, u32 soff) {
  asm volatile("buffer_load_dwordx4 %0, %1, %2, %3 offen offset:%c4 sc1" : "=v"(d) : "v"(voff), "s"(r), "s"(soff), "i"(IMM));
}
template <int IMM> DI void ld_v4p(u32x4& d, u32 voff, rsrc_t r, u32 soff) {
  asm volatile("buffer_load_dwordx4 %0, %1, %2, %3 offen offset:%c4" : "=v"(d) : "v"(voff), "s"(r), "s"(soff), "i"(IMM));
}
template <int IMM> DI void ld_v2(u32x2& d, u32 voff, rsrc_t r, u32 soff) {
  asm volatile("buffer_load_dwordx2 %0, %1, %2, %3 offen offset:%c4 sc1" : "=v"(d) : "v"(voff), "s"(r), "s"(soff), "i"(IMM));
}
template <int IMM> DI void ld_v2p(u32x2& d, u32 voff, rsrc_t r, u32 soff) {
  asm volatile("buffer_load_dwordx2 %0, %1, %2, %3 offen offset:%c4" : "=v"(d) : "v"(voff), "s"(r), "s"(soff), "i"(IMM));
}
template <int IMM> DI void ld_v1(u32& d, u32 voff, rsrc_t r, u32 soff) {
  asm volatile("buffer_load_dword %0, %1, %2, %3 offen offset:%c4 sc1" : "=v"(d) : "v"(voff), "s"(r), "s"(soff), "i"(IMM));
}
template <int IMM> DI void ld_u16p(u32& d, u32 voff, rsrc_t r, u32 soff) {
  asm volatile("buffer_load_ushort %0, %1, %2, %3 offen offset:%c4" : "=v"(d) : "v"(voff), "s"(r), "s"(soff), "i"(IMM));
}
template <int IMM> DI void set_m0(u32 base) { asm volatile("s_add_u32 m0, %0, %c1" :: "s"(base), "i"(IMM) : "m0", "scc"); }
template <int IMM> DI void set_m0n(u32 base) { asm volatile("s_add_u32 m0, %0, %c1\n\ts_nop 0" :: "s"(base), "i"(IMM) : "m0", "scc"); }
template <int ID, int OFF> DI void dma(u32 voff, rsrc_t r, u32 soff) {
  asm volatile("buffer_load_dwordx4 %0, %1, %2 offen offset:%c3 lds ; ID %c4" :: "v"(voff), "s"(r), "s"(soff), "i"(OFF), "i"(ID));
}
template <int ID, int OFF = 0> DI void pub16(u32x4 v, rsrc_t r, u32 voff, u32 soff) {
  asm volatile("buffer_store_dwordx4 %0, %1, %2, %3 offen offset:%c5 sc1 ; ID %c4\n\ts_nop 1" :: "v"(v), "v"(voff), "s"(r), "s"(soff), "i"(ID), "i"(OFF));
}
template <int ID> DI void pub8(u32x2 v, rsrc_t r, u32 voff, u32 soff) {
  asm volatile("buffer_store_dwordx2 %0, %1, %2, %3 offen sc1 ; ID %c4" :: "v"(v), "v"(voff), "s"(r), "s"(soff), "i"(ID));
}
template <int ID> DI void pub4(u32 v, rsrc_t r, u32 voff, u32 soff) {
  asm volatile("buffer_store_dword %0, %1, %2, %3 offen sc1 ; ID %c4" :: "v"(v), "v"(voff), "s"(r), "s"(soff), "i"(ID));
}
template <int N, int A = 0> DI void wait_vm() { static_assert(N >= 0 && N < 64, ""); asm volatile("s_waitcnt vmcnt(%c0) ; NEED %c1" :: "i"(N), "i"(A) : "memory"); }
template <int N> DI void wait_a(u32x4& a) { static_assert(N >= 0, ""); asm volatile("s_waitcnt vmcnt(%c1)" : "+a"(a) : "i"(N < 63 ? N : 63)); }
DI void tie_a(u32x4& a) { asm volatile("" : "+a"(a)); }
template <class T> DI void tie_v(T& a) { asm volatile("" : "+v"(a)); }
template <int N, int A = 0, int B = 0> DI void bar_vm() {
  static_assert(N >= 0, "");
  asm volatile("s_waitcnt vmcnt(%c0) lgkmcnt(0) ; NEED %c1 NEED %c2\n\ts_barrier" :: "i"(N < 63 ? N : 63), "i"(A), "i"(B) : "memory");
}
DI void bar() { asm volatile("s_waitcnt lgkmcnt(0)\n\ts_barrier" ::: "memory"); }
#define SCHED() __builtin_amdgcn_sched_barrier(0)

DI void st16(u32x4 v, rsrc_t r, u32 voff, u32 soff) { __builtin_amdgcn_raw_buffer_store_b128(v, r, voff, soff, 16); }
DI void st8(u32x2 v, rsrc_t r, u32 voff, u32 soff) { __builtin_amdgcn_raw_buffer_store_b64(v, r, voff, soff, 16); }
DI void st4(u32 v, rsrc_t r, u32 voff, u32 soff) { __builtin_amdgcn_raw_buffer_store_b32(v, r, voff, soff, 16); }

DI f32x16 mfma(u32x4 a, u32x4 b, f32x16 c) {
  return __builtin_amdgcn_mfma_f32_32x32x16_bf16(__builtin_bit_cast(bf16x8, a), __builtin_bit_cast(bf16x8, b), c, 0, 0, 0);
}
template <bool Z> DI void mm_va(f32x16& c, u32x4 a, u32x4 b) {
  if constexpr (Z) asm volatile("v_mfma_f32_32x32x16_bf16 %0, %1, %2, 0" : "=&v"(c) : "v"(a), "a"(b));
  else asm volatile("v_mfma_f32_32x32x16_bf16 %0, %1, %2, %0" : "+v"(c) : "v"(a), "a"(b));
}
template <bool Z> DI void mm_av(f32x16& c, u32x4 a, u32x4 b) {
  if constexpr (Z) asm volatile("v_mfma_f32_32x32x16_bf16 %0, %1, %2, 0" : "=&v"(c) : "a"(a), "v"(b));
  else asm volatile("v_mfma_f32_32x32x16_bf16 %0, %1, %2, %0" : "+v"(c) : "a"(a), "v"(b));
}
template <bool Z> DI void mm_vv(f32x16& c, u32x4 a, u32x4 b) {
  if constexpr (Z) asm volatile("v_mfma_f32_32x32x16_bf16 %0, %1, %2, 0" : "=&v"(c) : "v"(a), "v"(b));
  else asm volatile("v_mfma_f32_32x32x16_bf16 %0, %1, %2, %0" : "+v"(c) : "v"(a), "v"(b));
}
template <bool Z> DI void m16_va(f32x4& c, u32x4 a, u32x4 b) {
  if constexpr (Z) asm volatile("v_mfma_f32_16x16x32_bf16 %0, %1, %2, 0" : "=&v"(c) : "v"(a), "a"(b));
  else asm volatile("v_mfma_f32_16x16x32_bf16 %0, %1, %2, %0" : "+v"(c) : "v"(a), "a"(b));
}
DI void m16_fence(f32x4 (&y)[4]) { asm volatile("s_nop 11" : "+v"(y[0]), "+v"(y[1]), "+v"(y[2]), "+v"(y[3])); }
DI void mm_fence(f32x16& a, f32x16& b) { asm volatile("s_nop 15" : "+v"(a), "+v"(b)); }
DI void mm_tie(f32x16& a, f32x16& b) { asm volatile("" : "+v"(a), "+v"(b)); }
DI float smul(float a, float b) { float r; asm("v_mul_f32_e32 %0, %1, %2" : "=v"(r) : "v"(a), "v"(b)); return r; }
DI float sfma(float a, float b, float c) { float r; asm("v_fma_f32 %0, %1, %2, %3" : "=v"(r) : "v"(a), "v"(b), "v"(c)); return r; }
DI u32x4 lds16(LAS char* sm, u32 off) { return *(LAS u32x4*)(sm + off); }
DI f32x4 ldsf4(LAS char* sm, u32 off) { return *(LAS f32x4*)(sm + off); }
DI u32x4 lds_tr(LAS char* sm, u32 off0, u32 off1) {
  s16x4 lo = __builtin_amdgcn_ds_read_tr16_b64_v4i16((LAS s16x4*)(sm + off0));
  s16x4 hi = __builtin_amdgcn_ds_read_tr16_b64_v4i16((LAS s16x4*)(sm + off1));
  s16x8 w = __builtin_shufflevector(lo, hi, 0, 1, 2, 3, 4, 5, 6, 7);
  return __builtin_bit_cast(u32x4, w);
}
template <int CTRL> DI float dpp(float v) { return __int_as_float(__builtin_amdgcn_update_dpp(0, __float_as_int(v), CTRL, 0xF, 0xF, true)); }
DI float add_x16(float v) { auto r = __builtin_amdgcn_permlane16_swap(__float_as_uint(v), __float_as_uint(v), false, false); u32 a = r[0], b = r[1]; return __uint_as_float(a) + __uint_as_float(b); }
DI float add_x32(float v) { auto r = __builtin_amdgcn_permlane32_swap(__float_as_uint(v), __float_as_uint(v), false, false); u32 a = r[0], b = r[1]; return __uint_as_float(a) + __uint_as_float(b); }
DI float red_x32(float a, float b) { auto r = __builtin_amdgcn_permlane32_swap(__float_as_uint(a), __float_as_uint(b), false, false); u32 p = r[0], q = r[1]; return __uint_as_float(p) + __uint_as_float(q); }
DI float red_x16(float a, float b) { auto r = __builtin_amdgcn_permlane16_swap(__float_as_uint(a), __float_as_uint(b), false, false); u32 p = r[0], q = r[1]; return __uint_as_float(p) + __uint_as_float(q); }
DI float red_x8(float a, float b, bool hi) { float keep = hi ? b : a, give = hi ? a : b; return keep + dpp<0x128>(give); }
DI float wave_sum(float v) {
  v += dpp<0xB1>(v); v += dpp<0x4E>(v); v += dpp<0x141>(v); v += dpp<0x140>(v);
  return add_x32(add_x16(v));
}
DI u32x4 chunk_swap(u32 a0, u32 a1, u32 b0, u32 b1) {
  auto r0 = __builtin_amdgcn_permlane32_swap(a0, b0, false, false);
  auto r1 = __builtin_amdgcn_permlane32_swap(a1, b1, false, false);
  u32x4 o; o[0] = r0[0]; o[1] = r1[0]; o[2] = r0[1]; o[3] = r1[1];
  return o;
}
DI u32x4 chunk_swap16(u32 a0, u32 a1, u32 b0, u32 b1) {
  auto r0 = __builtin_amdgcn_permlane16_swap(a0, b0, false, false);
  auto r1 = __builtin_amdgcn_permlane16_swap(a1, b1, false, false);
  u32x4 o; o[0] = r0[0]; o[1] = r1[0]; o[2] = r0[1]; o[3] = r1[1];
  return o;
}
template <int NF> DI void poll_flags(const char* p, u64 tag, int lane) {
  const GAS u64* q = (const GAS u64*)p + (lane < NF ? lane : 0) * 16;
  for (u32 sp = 0; sp < SPIN_CAP; ++sp) {
    u64 v = __hip_atomic_load(q, RLX_AGENT);
    if (__all(v == tag)) break;
    __builtin_amdgcn_s_sleep(POLL_SLEEP);
  }
  asm volatile("" ::: "memory");
}
DI void post_flag(char* p, u64 tag) { __hip_atomic_store((GAS u64*)p, tag, RLX_AGENT); }

#define STAMP(i) do {} while (0)
#define FS(i) do {} while (0)

#define NINIT 6
#define NW1 20
#define ND1 4
constexpr int P1_MODC = 1, P1_MODLD = 8, P1_MODUSE = 13, P1_V = 14, P1_XR = 10;
constexpr int p1_extra(int k) { return (k == P1_MODLD || k == P1_MODLD + 1) ? 4 : k == P1_XR ? 2 : 0; }
struct Sched1 { int posx[16][4], posa[24], pose, posm, top[16][4], bar[16], ext[16], pre; };
constexpr Sched1 mk_sched1() {
  Sched1 s{}; int n = 2;
  for (int i = 0; i < NINIT; ++i) s.posa[i] = -1;
  for (int k = 0; k < 3; ++k) {
    for (int i = 0; i < 4; ++i) s.posx[k][i] = n++;
    if (k == 1) { n += 6; s.pose = n - 1; }
  }
  for (int st = NINIT; st < 8; ++st) { n += 4; s.posa[st] = n - 1; }
  s.pre = n;
  for (int k = 0; k < 16; ++k)
    for (int i = 0; i < 4; ++i) {
      s.top[k][i] = n;
      const int g = 4 * k + i - 3;
      if (i == 3) s.bar[k] = n;
      if (i == 3) { s.ext[k] = n; n += p1_extra(k); if (k == P1_MODLD + 1) s.posm = n - 1; }
      if (g >= 0 && (g >> 2) < 8 + ND1) s.posa[8 + (g >> 2)] = n++;
      if (k + 3 < 16) s.posx[k + 3][i] = n++;
    }
  return s;
}
constexpr Sched1 S1 = mk_sched1();
#define NG0 6
constexpr int P4_PRE = 8;
constexpr int P4_E0 = 34;
constexpr int P4_UG = 24, P4_UR = 31;
struct Sched4 { int posx[32], posg[24], posug, possto, wait[64], bar[16], urw, end; };
constexpr int p4_piece(int t, int m) { return (t >= P4_E0 && m < 3 && (t - P4_E0) * 3 + m < 80) ? (t - P4_E0) * 3 + m : -1; }
constexpr Sched4 mk_sched4() {
  Sched4 s{}; int n = 0;
  for (int i = 0; i < P4_PRE; ++i) s.posx[i] = n++;
  for (int t = 0; t < 64; ++t) {
    s.wait[t] = n;
    for (int m = 0; m < 4; ++m) {
      if (m == 0 && t == P4_UR) s.urw = n;
      if (m == 0 && t < 4 * (8 - NG0)) s.posg[NG0 + (t >> 2)] = n++;
      if (p4_piece(t, m) >= 0 && p4_piece(t, m) % 20 == 19) s.possto = n++;
      if (m == 1 && (t & 3) == 3) s.bar[t >> 2] = n;
      if (m == 1) { if (t + P4_PRE < 32) s.posx[t + P4_PRE] = n++; else if (t >= P4_UG && t < P4_UG + 4) s.posug = n++; }
      if (m == 3 && t >= 3) s.posg[8 + ((t - 3) >> 2)] = n++;
    }
  }
  s.end = n;
  return s;
}
constexpr Sched4 S4 = mk_sched4();
constexpr int P5_PRE = 10;
struct Sched5 { int posa[56], wait[56], end; };
constexpr Sched5 mk_sched5() {
  Sched5 s{}; int n = 0;
  for (int i = 0; i < P5_PRE; ++i) s.posa[i] = n++;
  for (int t = 0; t < 56; ++t) {
    if (t == 28) for (int i = 0; i < P5_PRE; ++i) s.posa[28 + i] = n++;
    s.wait[t] = n;
    if ((t % 28) + P5_PRE < 28) s.posa[t + P5_PRE] = n++;
    if (t >= 3 && t < 27) n++;
  }
  s.end = n;
  return s;
}
constexpr Sched5 S5 = mk_sched5();

struct Ge { float g0, g1, a0, a1, m0, m1; };
DI void ge_a(Ge& e, float q0, float q1, float r) {
  asm volatile("v_mul_f32 %0, %4, %5\n\tv_mul_f32 %1, %4, %6\n\tv_mul_f32 %2, %0, %0\n\tv_mul_f32 %3, %1, %1"
               : "=&v"(e.g0), "=&v"(e.g1), "=&v"(e.a0), "=&v"(e.a1) : "v"(r), "v"(q0), "v"(q1));
}
DI void ge_b(Ge& e, float c3, float c1) {
  asm volatile("v_fma_f32 %0, %0, %4, %5\n\tv_fma_f32 %1, %1, %4, %5\n\tv_mul_f32 %0, %0, %2\n\tv_mul_f32 %1, %1, %3"
               : "+v"(e.a0), "+v"(e.a1) : "v"(e.g0), "v"(e.g1), "v"(c3), "v"(c1));
}
DI void ge_c(Ge& e, float u0, float u1) {
  asm volatile("v_exp_f32 %0, %0\n\tv_exp_f32 %1, %1\n\tv_mul_f32 %2, %4, %6\n\tv_mul_f32 %3, %5, %7"
               : "+v"(e.a0), "+v"(e.a1), "=&v"(e.m0), "=&v"(e.m1) : "v"(e.g0), "v"(e.g1), "v"(u0), "v"(u1));
}
DI void ge_d(Ge& e, float sd) {
  asm volatile("v_fma_f32 %0, %0, %2, %2\n\tv_fma_f32 %1, %1, %2, %2\n\tv_rcp_f32 %0, %0\n\tv_rcp_f32 %1, %1\n\ts_nop 0"
               : "+v"(e.a0), "+v"(e.a1) : "v"(sd));
}
DI u32 ge_e(Ge& e) {
  u32 o;
  asm volatile("v_mul_f32 %1, %1, %3\n\tv_mul_f32 %2, %2, %4\n\tv_cvt_pk_bf16_f32 %0, %1, %2"
               : "=v"(o), "+v"(e.m0), "+v"(e.m1) : "v"(e.a0), "v"(e.a1));
  return o;
}

__global__ void __launch_bounds__(256, 1)
mk_block(const u16* __restrict__ x, const u16* __restrict__ Sre, const u16* __restrict__ Sim, const u16* __restrict__ Dre,
         const u16* __restrict__ Dim, const u16* __restrict__ Wg, const u16* __restrict__ Wu, const u16* __restrict__ Wn,
         const u16* __restrict__ cvec, const u16* __restrict__ wmod, u16* __restrict__ out, char* __restrict__ ws) {
  extern __shared__ __attribute__((aligned(16))) char smem_[];
  LAS char* sm = (LAS char*)smem_;
  const int tid = threadIdx.x, lane = tid & 63, w = __builtin_amdgcn_readfirstlane(tid >> 6);
  const int l31 = lane & 31, hh = lane >> 5, l15 = lane & 15, qq = lane >> 4;
  const int bid = blockIdx.x, xcd = bid & 7, loc = bid >> 3;
  const int tb = xcd * 4 + (loc >> 3), nt = loc & 7;
  const int cb = nt & 3, kh = nt >> 2;
  const int rb = (xcd >> 1) * 4 + loc / 7, ft = (xcd & 1) * 7 + loc % 7;
  const int w1 = w & 1, wk = w >> 1;
  const u32 lane16 = lane * 16, wl = w * 4096;
  const rsrc_t rX = mk_rsrc(x, S * D * 2);
  u32x4 xr[2];
  ld_v4p<0>(xr[0], lane16, rX, (bid * 8 + w * 2) * 1024);
  ld_v4p<1024>(xr[1], lane16, rX, (bid * 8 + w * 2) * 1024);
  STAMP(0); FS(90);
  const rsrc_t rW = mk_rsrc(ws, WS_END);
  u32 vA[4];
  sfor<4>([&](auto i) { vA[i] = (lane >> 3) * 4096 + (((lane & 7) ^ ((i & 1) * 4 + qq)) << 4) + i * (8 * 4096 - 1024); });
  auto kblk = [&](int st) { return kh * 16 + ((cb * 4 + st) & 15); };
  u64 tag;

  {
    const rsrc_t r0 = mk_rsrc(w1 == 0 ? Sre : Sim, S * S * 2);
    auto fill = [&](auto qc) {
      constexpr int q = qc, st = q >> 3, hf = (q >> 2) & 1, p = q & 3;
      if constexpr (p == 0) set_m0n<st * 16384 + hf * 4096>(w1 * 8192);
      dma<100 + st, p * 1024>(vA[p], r0, (tb * 64 + hf * 32) * 4096 + kblk(st) * 128);
    };
    asm volatile("s_load_dwordx2 %0, %1, 0x0" : "=s"(tag) : "s"(ws + WS_GEN));
    if (w >= 2) { sfor<NW1>([&](auto q) { fill(q); }); wait_vm<NW1>(); } else { wait_vm<0>(); }
    asm volatile("s_waitcnt lgkmcnt(0)" : "+s"(tag));
    tag += 1;
    *(LAS u32*)(sm + L_CNT) = 0;
    tie_v(xr[0]); tie_v(xr[1]);
    FS(91);
    float v[2][8], mu[2], rs[2];
    sfor<2>([&](auto i) {
      sfor<4>([&](auto j) { u32 d = xr[i][+j]; v[i][2 * j] = b_lo(d); v[i][2 * j + 1] = b_hi(d); });
      mu[i] = ((v[i][0] + v[i][1]) + (v[i][2] + v[i][3])) + ((v[i][4] + v[i][5]) + (v[i][6] + v[i][7]));
    });
    sfor<2>([&](auto i) { mu[i] = wave_sum(mu[i]) * (1.f / D); });
    sfor<2>([&](auto i) {
      float s = 0.f;
      sfor<8>([&](auto j) { v[i][j] -= mu[i]; s += v[i][j] * v[i][j]; });
      rs[i] = s;
    });
    sfor<2>([&](auto i) { rs[i] = __builtin_amdgcn_rsqf(wave_sum(rs[i]) * (1.f / D) + 1e-5f); });
    sfor<2>([&](auto i) {
      u32x4 o;
      sfor<4>([&](auto j) { o[+j] = pack2(v[i][2 * j] * rs[i], v[i][2 * j + 1] * rs[i]); });
      *(LAS u32x4*)(sm + L_EX + (w * 2 + i) * 1088 + lane16) = o;
    });
    bar();
    FS(92);
    if (w < 2) {
      const u32 q = (lane & 15) >> 2, pp = lane & 3;
      const u32 ta = L_EX + q * 1088 + (w * 256 + qq * 16 + pp * 4) * 2;
      sfor<4>([&](auto m) {
        u32x4 o = lds_tr(sm, ta + m * 128, ta + m * 128 + 4 * 1088);
        pub16<1>(o, rW, hh * 1024 + l31 * 16, WS_XN + (bid >> 1) * 16384 + (w * 8 + m * 2) * 1024 + (bid & 1) * 512);
      });
      bar();
      wait_vm<0, 1>();
      FS(93);
      u32 old = 0;
      if (lane == 0) old = __hip_atomic_fetch_add((LAS u32*)(sm + L_CNT), 1u, __ATOMIC_RELAXED, __HIP_MEMORY_SCOPE_WORKGROUP);
      old = __builtin_amdgcn_readfirstlane(old);
      if (old == 1 && lane < 8) post_flag(ws + WS_F1 + (lane * 256 + bid) * 128, tag);
      STAMP(14);
    } else {
      bar();
      sfor<NINIT * 8 - NW1>([&](auto q) { fill(ic<NW1 + q>{}); });
    }
  }
  const u32 tag32 = (u32)tag;
  const u32 sF = WS_F1 + (xcd * 256 + kh * 128) * 128;
  const u32 vF0 = ((cb * 32 + l31) & 127) * 128, vF1 = ((cb * 32 + 32 + lane) & 127) * 128, vF2 = ((cb * 32 + 96 + l31) & 127) * 128;
  {
    const GAS u64* q = (const GAS u64*)(ws + sF + vF0);
    for (u32 sp = 0; sp < SPIN_CAP; ++sp) {
      u64 a = __hip_atomic_load(q, RLX_AGENT);
      if (__all(a == tag)) break;
      __builtin_amdgcn_s_sleep(POLL_SLEEP);
    }
    asm volatile("" ::: "memory");
  }
  u32x2 fl[2];
  ld_v2<0>(fl[0], vF1, rW, sF);
  ld_v2<0>(fl[1], vF2, rW, sF);
  STAMP(1); FS(94);

  const rsrc_t rS = mk_rsrc(w < 2 ? Sre : Sim, S * S * 2);
  const rsrc_t rD = mk_rsrc(w < 2 ? Dre : Dim, D * D * 2);
  u32 vD[4], vX[4];
  sfor<4>([&](auto i) { vD[i] = (lane >> 3) * 1024 + (((lane & 7) ^ ((i & 1) * 4 + qq)) << 4) + i * (8 * 1024 - 1024); vX[i] = lane16 + i * 16384; });
  const u32 sA = (tb * 64 + w1 * 32) * 4096, sD = (nt * 64 + w1 * 32) * 1024;
  const u32 sXN = WS_XN + (cb * 4 + w) * 1024;
  auto a_dma = [&](auto stc, auto pc) { constexpr int st = stc, p = pc; dma<100 + st, p * 1024>(vA[p], rS, sA + kblk(st) * 128); };
  auto d_dma = [&](auto sc_, auto pc) { constexpr int s = sc_, p = pc; dma<2, p * 1024>(vD[p], rD, sD + s * 128); };
  u32x4 xw[4][4];
  auto xn_ld = [&](auto stc, auto ic_) { constexpr int st = stc, i = ic_; ld_a<0>(xw[st & 3][i], vX[i], rW, sXN + kblk(st) * 65536); };
  u32x4 mg[8], gvd, wm[4], xres[2];
  u32x2 cr;
  const int ukq = bid < 224 ? bid / 56 : 3, ucb = bid % 56;
  const int mkq = bid < 192 ? bid / 48 : 3, mcb = bid % 48;
  sfor<3>([&](auto kc) {
    sfor<4>([&](auto i) { xn_ld(kc, i); });
    if constexpr (kc == 1) {
      const rsrc_t rM = mk_rsrc(wmod, D * 6 * D * 2);
      const rsrc_t rC = mk_rsrc(cvec, D * 2);
      sfor<4>([&](auto r) { ld_v4p<0>(wm[r], (lane >> 3) * 4 * 6144 + (lane & 7) * 16, rM, (mkq * 128 + w * 32 + r) * 6144 + mcb * 128); });
      ld_v2p<0>(cr, (lane >> 3) * 8, rC, (mkq * 128 + w * 32) * 2);
      const rsrc_t rV = mk_rsrc(bid < 128 ? Dre : Dim, D * D * 2);
      ld_v4p<0>(gvd, lane16, rV, ((bid & 127) * 4 + w) * 1024);
    }
  });
  sfor<8 - NINIT>([&](auto s) { set_m0n<(NINIT + s) * 16384>(wl); sfor<4>([&](auto p) { a_dma(ic<NINIT + s>{}, p); }); });

  u32 aB[4][2];
  sfor<4>([&](auto i) { sfor<2>([&](auto hf) { aB[i][hf] = l31 * 128 + (((2 * i + hh) ^ ((l31 >> 1) & 7)) << 4) + hf * 65536; tie_v(aB[i][hf]); }); });
  u32x4 af[2][4], rf;
  auto a_rd = [&](int t, int j) { af[t & 1][j] = lds16(sm, aB[t & 3][(t >> 4) & 1] + ((t >> 2) & 3) * 16384 + j * 4096); };
  f32x16 acc[4], rsa;
  u32x4 ones = {0x3f803f80u, 0x3f803f80u, 0x3f803f80u, 0x3f803f80u};
  tie_v(ones);
  const bool h0 = (lane >> 3) & 1;
  u32 lM = L_MODV; tie_v(lM);
  const u32 vM2 = (w == 0 ? 512 + cb * 128 + 2 * lane : w == 1 ? 1024 + hh * 1024 + nt * 64 + 2 * l31 : (w == 2 ? 1536 : 2048) + ukq * 128 + 2 * lane) * 8;
  float ma[8];
  bar_vm<S1.pre - 1 - S1.posx[0][0], 100>();
  sfor<4>([&](auto j) { a_rd(0, j); });
  rf = lds16(sm, aB[0][0] + wl);
  tie_v(fl[0]); tie_v(fl[1]);
  for (u32 sp = 0; sp < SPIN_CAP; ++sp) {
    const u32 th = (u32)(tag >> 32);
    if (__all(fl[0][0] == tag32 && fl[0][1] == th && fl[1][0] == tag32 && fl[1][1] == th)) break;
    __builtin_amdgcn_s_sleep(POLL_SLEEP);
    ld_v2<0>(fl[0], vF1, rW, sF);
    ld_v2<0>(fl[1], vF2, rW, sF);
    wait_vm<0>();
    tie_v(fl[0]); tie_v(fl[1]);
  }
  sfor<64>([&](auto tc) {
    constexpr int t = tc, k = t >> 2, i = t & 3, g = t - 3;
    SCHED();
    wait_a<S1.top[k][i] - 1 - S1.posx[k][i]>(xw[k & 3][i]);
    sfor<4>([&](auto mc) {
      constexpr int m = mc;
      if constexpr (m == 2 && i == 3 && k < 8 + ND1) set_m0<(k & 7) * 16384>(wl);
      mm_av<t == 0>(acc[m], xw[k & 3][i], af[t & 1][m]);
      if constexpr (t < 16 && m == 0) mm_vv<t == 0>(rsa, ones, rf);
      SCHED();
      if constexpr (i != 3) a_rd(t + 1, m);
      if constexpr (m == 1 && i == 3) {
        constexpr int na = k < 15 ? S1.bar[k] - 1 - S1.posa[k + 1] : 63;
        constexpr int ne = k == P1_MODC ? S1.bar[k] - 1 - S1.pose : k == P1_MODUSE ? S1.bar[k] - 1 - S1.posm : 63;
        bar_vm<(na < ne ? na : ne), (k < 15 ? 101 + k : 0)>();
        FS(k);
        if constexpr (t < 63) { a_rd(t + 1, 0); a_rd(t + 1, 1); }
      }
      if constexpr (m >= 2 && i == 3 && t < 63) a_rd(t + 1, m);
      if constexpr (m == 2 && t < 15) rf = lds16(sm, aB[(t + 1) & 3][0] + ((t + 1) >> 2) * 16384 + wl);
      if constexpr (m == 2 && i == 3) {
        if constexpr (k == P1_MODC || k == P1_MODC + 1) {
          if constexpr (k == P1_MODC) { sfor<4>([&](auto r) { tie_v(wm[r]); }); tie_v(cr); sfor<8>([&](auto q) { ma[q] = 0.f; }); }
          sfor<2>([&](auto rr) {
            constexpr int r = 2 * (k - P1_MODC) + rr;
            u32 cd = cr[r >> 1];
            float cv = (r & 1) ? b_hi(cd) : b_lo(cd);
            float sc = cv * __builtin_amdgcn_rcpf(1.f + __builtin_amdgcn_exp2f(-1.4426950408889634f * cv));
            sfor<4>([&](auto jj) { u32 d = wm[r][+jj]; ma[2 * jj] += sc * b_lo(d); ma[2 * jj + 1] += sc * b_hi(d); });
          });
          if constexpr (k == P1_MODC + 1) {
            float b4[4], b2[2];
            sfor<4>([&](auto jj) { b4[jj] = red_x32(ma[jj], ma[4 + jj]); });
            sfor<2>([&](auto jj) { b2[jj] = red_x16(b4[jj], b4[2 + jj]); });
            *(LAS float*)(sm + lM + (L_MP - L_MODV) + w * 256 + ((lane & 7) * 8 + (lane >> 3)) * 4) = red_x8(b2[0], b2[1], h0);
          } else {
            sfor<8>([&](auto q) { tie_v(ma[q]); });
          }
        }
        if constexpr (k == P1_MODC + 2) {
          if (w == 0 && bid < 192) {
            float s = 0.f;
            sfor<4>([&](auto r) { s += *(LAS float*)(sm + lM + (L_MP - L_MODV) + r * 256 + lane * 4); });
            u32x2 gr = {__float_as_uint(s), tag32};
            pub8<0>(gr, rW, lane * 8, WS_MODG + (mkq * 3072 + mcb * 64) * 8);
          }
        }
        if constexpr (k == P1_MODLD || k == P1_MODLD + 1) {
          sfor<2>([&](auto qc) { constexpr int q = 2 * (k - P1_MODLD) + qc;
            ld_v4<0>(mg[2 * q], tid * 16, rW, WS_MODG + q * 3072 * 8); ld_v4<0>(mg[2 * q + 1], vM2, rW, WS_MODG + q * 3072 * 8); });
        }
        if constexpr (k == P1_XR)
          sfor<2>([&](auto p) { ld_v4p<p * 64>(xres[p], l15 * 1024 + (qq & 1) * 32 + (qq >> 1) * 16, rX, (tb * 64 + w * 16) * 1024 + nt * 128); });
        if constexpr (k == P1_MODUSE) {
          sfor<8>([&](auto q) { tie_v(mg[q]); });
          for (u32 sp = 0; sp < SPIN_CAP; ++sp) {
            bool ok = true;
            sfor<8>([&](auto q) { ok = ok && mg[q][1] == tag32 && mg[q][3] == tag32; });
            if (__all(ok)) break;
            __builtin_amdgcn_s_sleep(1);
            sfor<4>([&](auto q) { ld_v4<0>(mg[2 * q], tid * 16, rW, WS_MODG + q * 3072 * 8); ld_v4<0>(mg[2 * q + 1], vM2, rW, WS_MODG + q * 3072 * 8); });
            wait_vm<0>();
            sfor<8>([&](auto q) { tie_v(mg[q]); });
          }
          sfor<2>([&](auto e) {
            f32x2 mv;
            sfor<2>([&](auto o) {
              u32 a = mg[e][2 * o], b = mg[2 + e][2 * o], c = mg[4 + e][2 * o], d = mg[6 + e][2 * o];
              mv[+o] = (__uint_as_float(a) + __uint_as_float(b)) + (__uint_as_float(c) + __uint_as_float(d)); });
            *(LAS f32x2*)(sm + lM + e * 2048 + tid * 8) = mv;
          });
        }
        if constexpr (k == P1_V) {
          tie_v(gvd);
          f32x4 s0 = ldsf4(sm, lM + lane * 32), s1 = ldsf4(sm, lM + lane * 32 + 16);
          u32 d0 = gvd[0], d1 = gvd[1], d2 = gvd[2], d3 = gvd[3];
          float v = s0[0] * b_lo(d0) + s0[1] * b_hi(d0) + s0[2] * b_lo(d1) + s0[3] * b_hi(d1) + s1[0] * b_lo(d2) + s1[1] * b_hi(d2) + s1[2] * b_lo(d3) + s1[3] * b_hi(d3);
          v = wave_sum(v);
          if (lane == 0) { u32x2 gr = {__float_as_uint(v), tag32}; pub8<0>(gr, rW, 0, WS_VG + (bid * 4 + w) * 8); }
        }
      }
      if constexpr (m == 2 && g >= 0 && (g >> 2) < 8 + ND1) {
        if constexpr ((g >> 2) < 8) a_dma(ic<8 + (g >> 2)>{}, ic<(g & 3)>{}); else d_dma(ic<(g >> 2) - 8>{}, ic<(g & 3)>{});
      }
      if constexpr (m == 3 && k + 3 < 16) xn_ld(ic<k + 3>{}, ic<i>{});
      SCHED();
    });
  });
  STAMP(2); FS(32);
  u32x4 gvw[4], warm[2];
  {
    f32x4 sc[4];
    sfor<4>([&](auto g) { sc[g] = ldsf4(sm, lM + (512 + w * 32 + g * 8 + hh * 4) * 4) + 1.f; });
    mm_fence(acc[0], acc[1]); mm_tie(acc[2], acc[3]); mm_tie(rsa, rsa);
    const u32 vP = (l31 >> 4) * 65536 + hh * 256 + l15 * 16;
    sfor<4>([&](auto jc) {
      constexpr int j = jc;
      u32 pk[4][2];
      sfor<4>([&](auto g) {
        f32x4 m = {acc[j][4 * g], acc[j][4 * g + 1], acc[j][4 * g + 2], acc[j][4 * g + 3]};
        m = j < 2 ? m * sc[g] : -m * sc[g];
        pk[g][0] = pack2(m[0], m[1]); pk[g][1] = pack2(m[2], m[3]);
      });
      sfor<2>([&](auto q) {
        pub16<3, q * 512>(chunk_swap(pk[2 * q][0], pk[2 * q][1], pk[2 * q + 1][0], pk[2 * q + 1][1]), rW, vP,
                          WS_PQ + ((tb * 4 + (j & 1) * 2) * 64 + kh * 32 + (j >> 1) * 16 + cb * 4 + w) * 1024);
      });
    });
    pub4<3>(__float_as_uint(rsa[0]), rW, l31 * 4, WS_RS + ((tb * 8 + nt) * 128 + w * 32) * 4);
    bar();
    FS(39);
    sfor<2>([&](auto s) { set_m0n<(ND1 + s) * 16384>(wl); sfor<4>([&](auto p) { d_dma(ic<ND1 + s>{}, p); }); });
    bar_vm<8, 3>();
    if (tid == 0) post_flag(ws + WS_F2 + (tb * 8 + nt) * 128, tag);
    sfor<6 - ND1>([&](auto s) { set_m0n<(ND1 + 2 + s) * 16384>(wl); sfor<4>([&](auto p) { d_dma(ic<ND1 + 2 + s>{}, p); }); });
    const rsrc_t rU = mk_rsrc(ucb < 28 ? Wg : Wu, D * F * 2);
    const u32 vu = (tid >> 3) * (F * 2) + (lane & 7) * 16;
    sfor<4>([&](auto r) { ld_v4p<0>(gvw[r], vu, rU, (ukq * 128 + r * 32) * (F * 2) + (ucb % 28) * 128); });
    const rsrc_t rN = mk_rsrc(Wn, F * D * 2);
    ld_v4p<0>(warm[0], tid * 16, rN, bid * 7168);
    ld_v4p<0>(warm[1], tid * 16, rN, bid * 7168 + 4096);
  }
  STAMP(3); FS(33);
  poll_flags<8>(ws + WS_F2 + tb * 8 * 128, tag, lane);
  if (bid == 0 && tid == 0) post_flag(ws + WS_GEN, tag);
  STAMP(4); FS(34);

  f32x4 x1[4];
  {
    constexpr int PRE = 5;
    u32x4 pa[32][2];
    const u32 so = WS_PQ + (tb * 4 + w) * 65536;
    const u32 vP1 = lane16 + 32768;
    auto pa_ld = [&](auto sc_, auto hc) {
      constexpr int s = sc_, h = hc;
      ld_a<(s & 1) * 1024>(pa[s][h], h ? vP1 : lane16, rW, so + (((s >> 2) + nt) & 7) * 2048 + ((s >> 1) & 1) * 16384);
    };
    sfor<PRE>([&](auto s) { pa_ld(s, ic<0>{}); pa_ld(s, ic<1>{}); });
    u32 rp[4];
    u32x4 vg;
    u32 dB[2];
    sfor<2>([&](auto u) { dB[u] = l15 * 128 + (((4 * u + qq) ^ ((l15 >> 1) & 7)) << 4); });
    bar_vm<2 * PRE, 2>();
    f32x4 y[4];
    u32x4 df[2][4];
    u32 da;
    auto d_ad = [&](int s) { da = dB[s & 1] + (((s >> 2) + nt) & 7) * 16384; };
    auto d_rd = [&](int s, int c) { df[s & 1][c] = lds16(sm, da + ((s >> 1) & 1) * 8192 + c * 2048); };
    d_ad(0);
    sfor<4>([&](auto c) { d_rd(0, c); });
    sfor<32>([&](auto sc_) {
      constexpr int s = sc_;
      constexpr int issued = 2 * (s + PRE) < 69 ? 2 * (s + PRE) : 69;
      SCHED();
      if constexpr (s < 31) d_ad(s + 1);
      wait_a<issued - 1 - 2 * s>(pa[s][0]);
      if constexpr ((s & 3) == 0) FS(80 + (s >> 2));
      sfor<8>([&](auto mc) {
        constexpr int m = mc, c = m & 3, h = m >> 2, n = 2 * (s + PRE) + h;
        if constexpr (m == 4) wait_a<(issued < 69 ? issued + 1 : 69) - 1 - (2 * s + 1)>(pa[s][1]);
        m16_va<s == 0 && h == 0>(y[c], df[s & 1][c], pa[s][h]);
        SCHED();
        if constexpr (h == 0 && s < 31) d_rd(s + 1, c);
        if constexpr (c == 1) {
          if constexpr (n < 64) pa_ld(ic<s + PRE>{}, ic<h>{});
          else if constexpr (n < 68) ld_v1<(n - 64) * 512>(rp[n - 64], ((qq >> 1) * 512 + (qq & 1) * 64 + w * 16 + l15) * 4, rW, WS_RS + tb * 4096);
          else if constexpr (n == 68) ld_v4<0>(vg, (hh * 512 + nt * 64 + 2 * l31) * 8, rW, WS_VG);
        }
        SCHED();
      });
    });
    STAMP(5); FS(35);
    f32x4 ga[4], cbv[4];
    sfor<4>([&](auto c) { const u32 eo = (c * 16 + qq * 4) * 4; ga[c] = ldsf4(sm, lM + 640 * 4 + eo); cbv[c] = ldsf4(sm, lM + 704 * 4 + eo) + 1.f; });
    wait_vm<0>();
    sfor<4>([&](auto p) { tie_v(rp[p]); });
    tie_v(vg); tie_v(xres[0]); tie_v(xres[1]);
    for (u32 sp = 0; sp < SPIN_CAP; ++sp) {
      if (__all(vg[1] == tag32 && vg[3] == tag32)) break;
      __builtin_amdgcn_s_sleep(1);
      ld_v4<0>(vg, (hh * 512 + nt * 64 + 2 * l31) * 8, rW, WS_VG);
      wait_vm<0>();
      tie_v(vg);
    }
    { u32 a = vg[0], b = vg[2]; u32x2 o = {a, b}; *(LAS u32x2*)(sm + lM + (L_RS - L_MODV) + w * 512 + lane * 8) = o; }
    float rre, rim;
    {
      const float r4 = add_x32((__uint_as_float(rp[0]) + __uint_as_float(rp[1])) + (__uint_as_float(rp[2]) + __uint_as_float(rp[3])));
      auto r = __builtin_amdgcn_permlane16_swap(__float_as_uint(r4), __float_as_uint(r4), false, false);
      u32 a = r[0], b = r[1]; rre = __uint_as_float(a); rim = __uint_as_float(b);
    }
    u32 xg[4][2];
    sfor<2>([&](auto p) { u32x4 t = chunk_swap16(xres[p][0], xres[p][1], xres[p][2], xres[p][3]); xg[2 * p][0] = t[0]; xg[2 * p][1] = t[1]; xg[2 * p + 1][0] = t[2]; xg[2 * p + 1][1] = t[3]; });
    m16_fence(y);
    u32 pk[4][2];
    sfor<4>([&](auto c) {
      const u32 eo = (L_RS - L_MODV) + w * 512 + (c * 16 + qq * 4) * 4;
      f32x4 vre = ldsf4(sm, lM + eo), vim = ldsf4(sm, lM + 256 + eo);
      f32x4 xv = {b_lo(xg[c][0]), b_hi(xg[c][0]), b_lo(xg[c][1]), b_hi(xg[c][1])};
      x1[c] = xv + ga[c] * (y[c] + rre * vre - rim * vim);
      f32x4 s = x1[c] * cbv[c];
      pk[c][0] = pack2(s[0], s[1]); pk[c][1] = pack2(s[2], s[3]);
    });
    sfor<2>([&](auto p) {
      pub16<4, p * 2048>(chunk_swap16(pk[2 * p][0], pk[2 * p][1], pk[2 * p + 1][0], pk[2 * p + 1][1]), rW,
                         (qq & 1) * 1024 + (qq >> 1) * 512 + l15 * 16, WS_X1 + ((tb * 2 + wk) * 32 + nt * 4) * 1024 + w1 * 256);
    });
    const f32x4 s4 = (x1[0] + x1[1]) + (x1[2] + x1[3]);
    const float mean = add_x32(add_x16((s4[0] + s4[1]) + (s4[2] + s4[3]))) * (1.f / 64);
    f32x4 q4 = {0.f, 0.f, 0.f, 0.f};
    sfor<4>([&](auto c) { f32x4 d = x1[c] - mean; q4 += d * d; });
    const float m2 = add_x32(add_x16((q4[0] + q4[1]) + (q4[2] + q4[3])));
    u32x2 o = {__float_as_uint(mean), __float_as_uint(m2)};
    pub8<4>(o, rW, l15 * 8, WS_ST + ((tb * 2 + wk) * 8 + nt) * 256 + w1 * 128);
    bar();
  }
  FS(36);
  const rsrc_t rG = mk_rsrc(w < 2 ? Wg : Wu, D * F * 2);
  const rsrc_t rN = mk_rsrc(Wn, F * D * 2);
  u32 vG[4], vN[4];
  sfor<4>([&](auto i) {
    vG[i] = (lane >> 3) * (F * 2) + (((lane & 7) ^ ((qq & 1) << 2)) << 4) + i * (8 * F * 2 - 1024);
    vN[i] = (lane >> 3) * 1024 + (((lane & 7) ^ (((qq & 1) + 2 * (i & 1)) << 1)) << 4) + i * (8 * 1024 - 1024);
  });
  const u32 sG = w1 * 32 * (F * 2) + ft * 256, sN = (wk * 128 + w1 * 32) * 1024 + nt * 128;
  auto g_dma = [&](auto jc, auto ic_) {
    constexpr int j = jc, i = ic_;
    dma<200 + j, i * 1024>(vG[i], rG, sG + ((j + ft) & 7) * (64 * F * 2) + (j >> 3) * 128);
  };
  auto rot7 = [&](int j) { int pr = j + nt; pr = pr >= 7 ? pr - 7 : pr; return pr >= 7 ? pr - 7 : pr; };
  auto n_dma = [&](auto jc, auto ic_) {
    constexpr int j = jc, i = ic_;
    dma<300 + j, i * 1024>(vN[i], rN, sN + rot7(j % 7) * (256 * 1024) + (j / 7) * 65536);
  };
  const bool act4 = loc < 28;
  u32x4 ug[4];
  const u32 vUV = ((tid >> 7) * F + ft * 128 + (tid & 127)) * 16;
  if (act4) {
    sfor<3>([&](auto j) { set_m0n<j * 16384>(wl); sfor<4>([&](auto i) { g_dma(j, i); }); });
  } else {
    sfor<3>([&](auto j) { set_m0n<j * 16384>(wl); sfor<4>([&](auto i) { n_dma(j, i); }); });
  }
  bar_vm<12, 4>();
  if (tid == 0) post_flag(ws + WS_F3 + (tb * 8 + nt) * 128, tag);
  FS(100);
  if (act4) {
    sfor<NG0 - 3>([&](auto j) { set_m0n<(j + 3) * 16384>(wl); sfor<4>([&](auto i) { g_dma(ic<j + 3>{}, i); }); });
  } else {
    sfor<5>([&](auto j) { set_m0n<(j + 3) * 16384>(wl); sfor<4>([&](auto i) { n_dma(ic<j + 3>{}, i); }); });
  }
  FS(101);
  {
    sfor<4>([&](auto i) { tie_v(gvw[i]); });
    tie_v(warm[0]); tie_v(warm[1]);
    float a[16];
    sfor<16>([&](auto j) { a[j] = 0.f; });
    sfor<4>([&](auto i) {
      const float sh = *(LAS float*)(sm + lM + (768 + (tid >> 3) + i * 32) * 4);
      const float cs = 1.f + *(LAS float*)(sm + lM + (896 + (tid >> 3) + i * 32) * 4);
      sfor<4>([&](auto j) { u32 d = gvw[i][+j]; float lo = b_lo(d), hi = b_hi(d);
        a[2 * j] += cs * lo; a[2 * j + 1] += cs * hi; a[8 + 2 * j] += sh * lo; a[8 + 2 * j + 1] += sh * hi; });
    });
    float b8[8], b4[4];
    sfor<8>([&](auto j) { b8[j] = red_x32(a[j], a[8 + j]); });
    sfor<4>([&](auto j) { b4[j] = red_x16(b8[j], b8[4 + j]); });
    f32x2 o = {red_x8(b4[0], b4[2], h0), red_x8(b4[1], b4[3], h0)};
    *(LAS f32x2*)(sm + lM + (L_UVP - L_MODV) + w * 512 + (hh * 64 + (lane & 7) * 8 + ((lane >> 3) & 3) * 2) * 4) = o;
  }
  bar();
  if (w == 0 && bid < 224) {
    float su = 0.f, sv = 0.f;
    sfor<4>([&](auto i) { su += *(LAS float*)(sm + lM + (L_UVP - L_MODV) + i * 512 + lane * 4); sv += *(LAS float*)(sm + lM + (L_UVP - L_MODV) + i * 512 + 256 + lane * 4); });
    u32x4 g = {__float_as_uint(su), tag32, __float_as_uint(sv), tag32};
    pub16<0>(g, rW, lane16, WS_UVG + (ukq * 3584 + (ucb / 28) * F + (ucb % 28) * 64) * 16);
  }
  STAMP(6); FS(37);
  u32x4 gbg[4];
  const GAS u64* pF4A = (const GAS u64*)(ws + WS_F4A + ((tb >> 1) * 16 + (lane < 14 ? lane : 0)) * 128);
  const GAS u64* pF4B = (const GAS u64*)(ws + WS_F4B + ((tb >> 1) * 16 + (lane < 14 ? lane : 0)) * 128);

  if (act4) {
    u32 tB[2][2];
    {
      const u32 q = l15 >> 2, pp = lane & 3;
      sfor<2>([&](auto c) { sfor<2>([&](auto hf) {
        tB[c][hf] = (8 * hh + q) * 128 + ((4 * (c ^ (q >> 1)) + 2 * (qq & 1) + (pp >> 1)) << 4) + 8 * (pp & 1) + hf * 65536; tie_v(tB[c][hf]); }); });
    }
    poll_flags<16>(ws + WS_F3 + rb * 16 * 128, tag, lane);
    STAMP(7); FS(38);
    u32x2 stv[4];
    sfor<4>([&](auto i) { ld_v2<i * 256>(stv[i], hh * 1024 + l31 * 8, rW, WS_ST + (rb * 4 + w) * 2048); });
    u32x4 xa[32];
    const u32 so = WS_X1 + (rb * 4 + w) * 32 * 1024;
    auto xa_ld = [&](auto sc_) { constexpr int s = sc_; ld_a<(s & 3) * 1024>(xa[s], lane16, rW, so + (((s >> 2) + ft) & 7) * 4096); };
    sfor<P4_PRE>([&](auto s) { xa_ld(s); });
    bar_vm<P4_PRE, 200, 200 + NG0 - 1>();
    f32x16 z[2][4];
    u32x4 wf[2][4], uf[4];
    auto w_rd = [&](int t, int j) {
      const u32 o = tB[j & 1][(t >> 4) & 1] + ((t >> 2) & 3) * 16384 + (j >> 1) * 8192 + (t & 3) * 2048;
      wf[t & 1][j] = lds_tr(sm, o, o + 512);
    };
    sfor<4>([&](auto j) { w_rd(0, j); });
    float rr, sd;
    u32x4 xst;
    {
      sfor<4>([&](auto i) { tie_v(stv[i]); });
      float ms = 0.f;
      sfor<4>([&](auto i) { u32 a = stv[i][0]; ms += __uint_as_float(a); });
      const float mean = add_x32(ms) * (1.f / 8);
      float q = 0.f;
      sfor<4>([&](auto i) { u32 a = stv[i][0], b = stv[i][1]; float d = __uint_as_float(a) - mean; q += __uint_as_float(b) + 64.f * d * d; });
      const float var = add_x32(q) * (1.f / D) + 1e-5f;
      rr = __builtin_amdgcn_rsqf(var);
      sd = var * rr;
      const float mh = b_lo(pack2(mean, 0.f)), sh = b_lo(pack2(sd, 0.f));
      xst[0] = hh ? 0u : pack2(-mh, -mh); xst[1] = hh ? 0u : pack2(mh - mean, mh - mean);
      xst[2] = hh ? 0u : pack2(sh, sh); xst[3] = hh ? 0u : pack2(sd - sh, sd - sh);
      tie_v(xst); tie_v(rr); tie_v(sd);
    }
    auto u_rd = [&](int p, int j) { uf[j] = lds16(sm, lM + (L_UV - L_MODV) + ((j >> 1) * 128 + p * 64 + (j & 1) * 32 + l31) * 16); };
    constexpr float C1 = -2.f * 0.7978845608028654f * 1.4426950408889634f, C3 = C1 * 0.044715f;
    float c1v = C1, c3v = C3;
    tie_v(c1v); tie_v(c3v);
    const u32 sAct = WS_ACT + ((rb * 8 + 2 * w) * 56 + ft * 4) * 1024;
    const u32 vAct = (l31 >> 4) * (56 * 1024) + hh * 256 + l15 * 16;
    u32 pk[4];
    Ge ge;
    sfor<64>([&](auto tc) {
      constexpr int t = tc, j = t >> 2, s4 = t & 3, p = j >> 3;
      constexpr bool many = t < 4 * (8 - NG0) + 4;
      SCHED();
      if constexpr (p == 0) wait_a<S4.wait[t] - 1 - S4.posx[t]>(xa[t]);
      sfor<4>([&](auto mc) {
        constexpr int m = mc;
        if constexpr (m == 0 && t < 4 * (8 - NG0)) set_m0<(NG0 + (t >> 2)) * 16384>(wl);
        if constexpr (m == 3 && t >= 3 && (many || s4 == 3)) set_m0<(((t - 3) >> 2) & 7) * 16384>(wl);
        mm_va<(t & 31) == 0>(z[p][m], wf[t & 1][m], xa[t & 31]);
        if constexpr (t == 33) mm_vv<false>(z[0][m], uf[m], xst);
        SCHED();
        if constexpr (s4 != 3) { w_rd(t + 1, m); }
        if constexpr (t == 32) u_rd(0, m);
        if constexpr (m == 0 && t == P4_UR) {
          wait_vm<S4.urw - 1 - S4.posug>();
          sfor<4>([&](auto q) { tie_v(ug[q]); });
          for (u32 sp = 0; sp < SPIN_CAP; ++sp) {
            bool ok = true;
            sfor<4>([&](auto q) { ok = ok && ug[q][1] == tag32 && ug[q][3] == tag32; });
            if (__all(ok)) break;
            __builtin_amdgcn_s_sleep(1);
            sfor<4>([&](auto q) { ld_v4<0>(ug[q], vUV, rW, WS_UVG + q * 3584 * 16); });
            wait_vm<0>();
            sfor<4>([&](auto q) { tie_v(ug[q]); });
          }
          u32 d[2];
          sfor<2>([&](auto e) { u32 a = ug[0][2 * e], b = ug[1][2 * e], c = ug[2][2 * e], dd = ug[3][2 * e];
            const float s = (__uint_as_float(a) + __uint_as_float(b)) + (__uint_as_float(c) + __uint_as_float(dd));
            const float hi = b_lo(pack2(s, 0.f));
            d[e] = pack2(hi, s - hi); });
          u32x4 rec = {d[0], d[0], d[1], d[1]};
          *(LAS u32x4*)(sm + lM + (L_UV - L_MODV) + tid * 16) = rec;
        }
        if constexpr (m == 0 && t < 4 * (8 - NG0)) g_dma(ic<NG0 + (t >> 2)>{}, ic<(t & 3)>{});
        if constexpr (p4_piece(t, m) >= 0) {
          constexpr int e = p4_piece(t, m) / 5, st = p4_piece(t, m) % 5;
          if constexpr (st == 0) ge_a(ge, z[0][e >> 3][2 * (e & 7)], z[0][e >> 3][2 * (e & 7) + 1], rr);
          if constexpr (st == 1) ge_b(ge, c3v, c1v);
          if constexpr (st == 2) ge_c(ge, z[0][2 + (e >> 3)][2 * (e & 7)], z[0][2 + (e >> 3)][2 * (e & 7) + 1]);
          if constexpr (st == 3) ge_d(ge, sd);
          if constexpr (st == 4) {
            pk[e & 3] = ge_e(ge);
            if constexpr ((e & 3) == 3) pub16<5, (e >> 3) * 1024 + ((e >> 2) & 1) * 512>(chunk_swap(pk[0], pk[1], pk[2], pk[3]), rW, vAct, sAct);
          }
        }
        if constexpr (m == 1 && s4 == 3) {
          constexpr int need = j + 1 >= NG0 && j < 15 ? S4.bar[j] - 1 - S4.posg[j + 1] : 63;
          bar_vm<need, (j < 15 ? 201 + j : 0)>();
          FS(40 + j);
          if constexpr (t < 63) { w_rd(t + 1, 0); w_rd(t + 1, 1); } else { u_rd(1, 0); u_rd(1, 1); }
        }
        if constexpr (m == 1) {
          if constexpr (t + P4_PRE < 32) xa_ld(ic<t + P4_PRE>{});
          else if constexpr (t >= P4_UG && t < P4_UG + 4) ld_v4<0>(ug[t - P4_UG], vUV, rW, WS_UVG + (t - P4_UG) * 3584 * 16);
        }
        if constexpr (m >= 2 && s4 == 3) { if constexpr (t < 63) w_rd(t + 1, m); else u_rd(1, m); }
        if constexpr (m == 3 && t >= 3) {
          constexpr int gj = (t - 3) >> 2, gi = (t - 3) & 3;
          if constexpr (gj < 8) g_dma(ic<8 + gj>{}, ic<gi>{}); else n_dma(ic<gj - 8>{}, ic<gi>{});
        }
        SCHED();
      });
    });
    sfor<4>([&](auto m) { mm_vv<false>(z[1][m], uf[m], xst); if constexpr (m < 3) n_dma(ic<7>{}, ic<1 + m>{}); });
    STAMP(8); FS(56);
    mm_fence(z[1][0], z[1][2]); mm_tie(z[1][1], z[1][3]);
    sfor<4>([&](auto qc) {
      constexpr int c = qc >> 1, g0 = (qc & 1) * 2;
      u32 pq[4];
      sfor<2>([&](auto gg) {
        constexpr int g = g0 + gg;
        f32x4 zg = {z[1][c][4 * g], z[1][c][4 * g + 1], z[1][c][4 * g + 2], z[1][c][4 * g + 3]};
        f32x4 zu = {z[1][2 + c][4 * g], z[1][2 + c][4 * g + 1], z[1][2 + c][4 * g + 2], z[1][2 + c][4 * g + 3]};
        f32x4 gt = rr * zg;
        f32x4 ex = gt * (C1 + C3 * gt * gt);
        sfor<4>([&](auto i) { ex[+i] = __builtin_amdgcn_exp2f(ex[+i]); });
        ex = ex * sd + sd;
        sfor<4>([&](auto i) { ex[+i] = __builtin_amdgcn_rcpf(ex[+i]); });
        f32x4 a = gt * zu * ex;
        pq[2 * gg] = pack2(a[0], a[1]); pq[2 * gg + 1] = pack2(a[2], a[3]);
      });
      pub16<6, 2048 + c * 1024 + (qc & 1) * 512>(chunk_swap(pq[0], pq[1], pq[2], pq[3]), rW, vAct, sAct);
      if constexpr (qc == 0) {
        bar_vm<S4.end + 3 + 1 - 1 - S4.possto, 5>();
        if (tid == 0) post_flag(ws + WS_F4A + (rb * 16 + ft) * 128, tag);
      }
    });
  }
  STAMP(9); FS(57);

  {
    bar();
    for (u32 sp = 0; sp < SPIN_CAP; ++sp) {
      u64 fa = __hip_atomic_load(pF4A, RLX_AGENT);
      if (__all(fa == tag)) break;
      __builtin_amdgcn_s_sleep(POLL_SLEEP);
    }
    asm volatile("" ::: "memory");
    FS(102);
    u32x4 aa[56];
    const u32 so = WS_ACT + (tb * 4 + w) * 56 * 1024;
    const u32 vA2 = lane16 + 4096;
    auto aa_ld = [&](auto sc_) {
      constexpr int s = sc_;
      ld_a<(s & 1) * 1024 + (s / 28) * 2048>(aa[s], (s & 2) ? vA2 : lane16, rW, so + rot7((s >> 2) % 7) * 8192);
    };
    sfor<P5_PRE>([&](auto s) { aa_ld(s); });
    bar_vm<P5_PRE, 6, 307>();
    if (act4 && tid == 0) post_flag(ws + WS_F4B + (rb * 16 + ft) * 128, tag);
    STAMP(10); FS(58);
    u32 tN[4][2];
    {
      const u32 q = l15 >> 2, pp = lane & 3, f = (q >> 1) + 2 * (qq & 1);
      sfor<4>([&](auto c) { sfor<2>([&](auto hf) {
        tN[c][hf] = (8 * qq + q) * 128 + ((2 * (c ^ f) + (pp >> 1)) << 4) + 8 * (pp & 1) + hf * 65536; tie_v(tN[c][hf]); }); });
    }
    f32x4 y[4];
    u32x4 nf[2][4];
    auto n_rd = [&](int t, int c) {
      const u32 o = tN[c][(t >> 4) & 1] + ((t >> 2) & 3) * 16384 + (t & 3) * 4096;
      nf[t & 1][c] = lds_tr(sm, o, o + 512);
    };
    sfor<4>([&](auto c) { n_rd(0, c); });
    sfor<4>([&](auto q) { ld_v4<0>(gbg[q], (2560 + nt * 64 + 2 * l31) * 8, rW, WS_MODG + q * 3072 * 8); });
    sfor<56>([&](auto tc) {
      constexpr int t = tc, i = t >> 2, s4 = t & 3;
      SCHED();
      if constexpr (t == 28) {
        for (u32 sp = 0; sp < SPIN_CAP; ++sp) {
          u64 fb = __hip_atomic_load(pF4B, RLX_AGENT);
          if (__all(fb == tag)) break;
          __builtin_amdgcn_s_sleep(POLL_SLEEP);
        }
        asm volatile("" ::: "memory");
        STAMP(11); FS(59);
        sfor<P5_PRE>([&](auto s) { aa_ld(ic<28 + s>{}); });
      }
      wait_a<S5.wait[t] - 1 - S5.posa[t] + (t < P5_PRE ? 4 : 0)>(aa[t]);
      sfor<4>([&](auto mc) {
        constexpr int m = mc;
        if constexpr (m == 2 && s4 == 3 && t < 27) set_m0<(i & 7) * 16384>(wl);
        m16_va<t == 0>(y[m], nf[t & 1][m], aa[t]);
        SCHED();
        if constexpr (s4 != 3) n_rd(t + 1, m);
        if constexpr (m == 1 && s4 == 3 && i < 13) { bar_vm<63, (i >= 7 ? 301 + i : 0)>(); FS(60 + i); n_rd(t + 1, 0); n_rd(t + 1, 1); }
        if constexpr (m >= 2 && s4 == 3 && t < 55) n_rd(t + 1, m);
        if constexpr (m == 1 && (t % 28) + P5_PRE < 28) aa_ld(ic<t + P5_PRE>{});
        if constexpr (m == 3 && t >= 3 && t < 27) n_dma(ic<8 + ((t - 3) >> 2)>{}, ic<((t - 3) & 3)>{});
        SCHED();
      });
    });
    STAMP(12); FS(73);
    sfor<4>([&](auto q) { tie_v(gbg[q]); });
    {
      for (u32 sp = 0; sp < SPIN_CAP; ++sp) {
        bool ok = true;
        sfor<4>([&](auto q) { ok = ok && gbg[q][1] == tag32 && gbg[q][3] == tag32; });
        if (__all(ok)) break;
        sfor<4>([&](auto q) { ld_v4<0>(gbg[q], (2560 + nt * 64 + 2 * l31) * 8, rW, WS_MODG + q * 3072 * 8); });
        wait_vm<0>();
        sfor<4>([&](auto q) { tie_v(gbg[q]); });
      }
      f32x2 mv;
      sfor<2>([&](auto o) {
        u32 a = gbg[0][2 * o], b = gbg[1][2 * o], c = gbg[2][2 * o], d = gbg[3][2 * o];
        mv[+o] = (__uint_as_float(a) + __uint_as_float(b)) + (__uint_as_float(c) + __uint_as_float(d)); });
      *(LAS f32x2*)(sm + lM + (L_RS - L_MODV) + w * 512 + l31 * 8) = mv;
    }
    m16_fence(y);
    u32 pk[4][2];
    sfor<4>([&](auto c) {
      f32x4 m = x1[c] + ldsf4(sm, lM + (L_RS - L_MODV) + w * 512 + (c * 16 + qq * 4) * 4) * y[c];
      pk[c][0] = pack2(m[0], m[1]); pk[c][1] = pack2(m[2], m[3]);
    });
    const rsrc_t rO = mk_rsrc(out, S * D * 2);
    sfor<2>([&](auto p) {
      __builtin_amdgcn_raw_buffer_store_b128(chunk_swap16(pk[2 * p][0], pk[2 * p][1], pk[2 * p + 1][0], pk[2 * p + 1][1]), rO,
                                             l15 * 1024 + (qq & 1) * 32 + (qq >> 1) * 16 + p * 64, (tb * 64 + w * 16) * 1024 + nt * 128, 0);
    });
  }
  if (tid == 0) ((u64*)(ws + (100u << 20)))[bid] = __builtin_amdgcn_s_memrealtime();
}

__global__ void k_post(char* p) { u64 t = __builtin_amdgcn_s_memrealtime(); u64 m = 0; for (int i = 0; i < 256; ++i) { u64 v = ((u64*)(p + (100u << 20)))[i]; m = v > m ? v : m; } u64 d = (t - m) * 20; while (__builtin_amdgcn_s_memrealtime() - t < d) __builtin_amdgcn_s_sleep(4); }
extern "C" void kernel_launch(void* const* d_in, const int* in_sizes, int n_in, void* d_out, int out_size, void* d_ws,
                              size_t ws_size, hipStream_t stream) {
  (void)in_sizes; (void)n_in; (void)out_size; (void)ws_size;
  (void)hipFuncSetAttribute((const void*)mk_block, hipFuncAttributeMaxDynamicSharedMemorySize, LDS_BYTES);
  mk_block<<<256, 256, LDS_BYTES, stream>>>((const u16*)d_in[0], (const u16*)d_in[1], (const u16*)d_in[2], (const u16*)d_in[3],
                                            (const u16*)d_in[4], (const u16*)d_in[6], (const u16*)d_in[7], (const u16*)d_in[8],
                                            (const u16*)d_in[9], (const u16*)d_in[10], (u16*)d_out, (char*)d_ws);
  k_post<<<1, 64, 0, stream>>>((char*)d_ws);
}
```

```cpp
#include <hip/hip_runtime.h>
#include <utility>
#pragma clang diagnostic ignored "-Winline-asm"

typedef unsigned short u16;
typedef unsigned u32;
typedef unsigned long long u64;
typedef float f32x2 __attribute__((ext_vector_type(2)));
typedef float f32x4 __attribute__((ext_vector_type(4)));
typedef float f32x16 __attribute__((ext_vector_type(16)));
typedef unsigned u32x2 __attribute__((ext_vector_type(2)));
typedef unsigned u32x4 __attribute__((ext_vector_type(4)));
typedef short s16x4 __attribute__((ext_vector_type(4)));
typedef short s16x8 __attribute__((ext_vector_type(8)));
typedef __bf16 bf16x2 __attribute__((ext_vector_type(2)));
typedef __bf16 bf16x8 __attribute__((ext_vector_type(8)));
typedef __amdgpu_buffer_rsrc_t rsrc_t;
#define LAS __attribute__((address_space(3)))
#define GAS __attribute__((address_space(1)))
#define DI __device__ __forceinline__
#define RLX_AGENT __ATOMIC_RELAXED, __HIP_MEMORY_SCOPE_AGENT

constexpr int S = 2048, D = 512, F = 1792;
constexpr u32 WS_XN = 0;
constexpr u32 WS_PQ = WS_XN + (2u << 20);
constexpr u32 WS_X1 = WS_PQ + (8u << 20);
constexpr u32 WS_ACT = WS_X1 + (2u << 20);
constexpr u32 WS_RS = WS_ACT + (7u << 20);
constexpr u32 WS_ST = WS_RS + (256u << 10);
constexpr u32 WS_MODP = WS_ST + (256u << 10);
constexpr u32 WS_VG = WS_MODP + 4 * 3072 * 4;
constexpr u32 WS_UVG = WS_VG + 1024 * 8;
constexpr u32 WS_F1 = WS_UVG + 4 * 2 * 3584 * 8;
constexpr u32 WS_F2 = WS_F1 + 8 * 256 * 128;
constexpr u32 WS_F3 = WS_F2 + 256 * 128;
constexpr u32 WS_F4A = WS_F3 + 256 * 128;
constexpr u32 WS_F4B = WS_F4A + 256 * 128;
constexpr u32 WS_FM = WS_F4B + 256 * 128;
constexpr u32 WS_GEN = WS_FM + 256 * 128;
constexpr u32 WS_TS = WS_GEN + 256;
constexpr u32 WS_TS2 = WS_TS + 256 * 16 * 8;
constexpr u32 WS_END = WS_TS2 + 256 * 256 * 4;
constexpr u32 L_EX = 128 * 1024;
constexpr u32 L_MODV = 144 * 1024;
constexpr u32 L_UV = L_MODV + 5120;
constexpr u32 L_UVP = L_UV + 4096;
constexpr u32 L_MP = L_UVP + 2048;
constexpr u32 L_RS = L_MP + 1024;
constexpr u32 L_CNT = L_RS + 2048;
constexpr u32 LDS_BYTES = 160 * 1024;
constexpr u32 SPIN_CAP = 200000;
#define POLL_ATOMIC 0
#define POLL_SLEEP 2

template <class Fn, int... Is> DI void sfor_impl(Fn&& f, std::integer_sequence<int, Is...>) { (f(std::integral_constant<int, Is>{}), ...); }
template <int V> using ic = std::integral_constant<int, V>;
template <int N, class Fn> DI void sfor(Fn&& f) { sfor_impl(f, std::make_integer_sequence<int, N>{}); }

DI rsrc_t mk_rsrc(const void* p, u32 bytes) { return __builtin_amdgcn_make_buffer_rsrc((void*)p, 0, bytes, 0x00020000); }
DI float b_lo(u32 v) { return __uint_as_float(v << 16); }
DI float b_hi(u32 v) { return __uint_as_float(v & 0xffff0000u); }
DI u32 pack2(float a, float b) { f32x2 p = {a, b}; bf16x2 r = __builtin_convertvector(p, bf16x2); return __builtin_bit_cast(u32, r); }

template <int IMM> DI void ld_a(u32x4& d, u32 voff, rsrc_t r, u32 soff) {
  asm volatile("buffer_load_dwordx4 %0, %1, %2, %3 offen offset:%c4 sc1" : "=a"(d) : "v"(voff), "s"(r), "s"(soff), "i"(IMM));
}
template <int IMM> DI void ld_v4(u32x4& d, u32 voff, rsrc_t r, u32 soff) {
  asm volatile("buffer_load_dwordx4 %0, %1, %2, %3 offen offset:%c4 sc1" : "=v"(d) : "v"(voff), "s"(r), "s"(soff), "i"(IMM));
}
template <int IMM> DI void ld_v4p(u32x4& d, u32 voff, rsrc_t r, u32 soff) {
  asm volatile("buffer_load_dwordx4 %0, %1, %2, %3 offen offset:%c4" : "=v"(d) : "v"(voff), "s"(r), "s"(soff), "i"(IMM));
}
template <int IMM> DI void ld_v2(u32x2& d, u32 voff, rsrc_t r, u32 soff) {
  asm volatile("buffer_load_dwordx2 %0, %1, %2, %3 offen offset:%c4 sc1" : "=v"(d) : "v"(voff), "s"(r), "s"(soff), "i"(IMM));
}
DI void ld_v2t(u32x2& d, u32 voff, rsrc_t r, u32 soff) {
  asm volatile("buffer_load_dwordx2 %0, %1, %2, %3 offen sc1" : "+v"(d) : "v"(voff), "s"(r), "s"(soff));
}
template <int IMM> DI void ld_v2p(u32x2& d, u32 voff, rsrc_t r, u32 soff) {
  asm volatile("buffer_load_dwordx2 %0, %1, %2, %3 offen offset:%c4" : "=v"(d) : "v"(voff), "s"(r), "s"(soff), "i"(IMM));
}
template <int IMM> DI void ld_v1(u32& d, u32 voff, rsrc_t r, u32 soff) {
  asm volatile("buffer_load_dword %0, %1, %2, %3 offen offset:%c4 sc1" : "=v"(d) : "v"(voff), "s"(r), "s"(soff), "i"(IMM));
}
template <int IMM> DI void ld_v1p(u32& d, u32 voff, rsrc_t r, u32 soff) {
  asm volatile("buffer_load_dword %0, %1, %2, %3 offen offset:%c4" : "+v"(d) : "v"(voff), "s"(r), "s"(soff), "i"(IMM));
}
template <int IMM> DI void ld_u16p(u32& d, u32 voff, rsrc_t r, u32 soff) {
  asm volatile("buffer_load_ushort %0, %1, %2, %3 offen offset:%c4" : "=v"(d) : "v"(voff), "s"(r), "s"(soff), "i"(IMM));
}
template <int IMM> DI void set_m0(u32 base) { asm volatile("s_add_u32 m0, %0, %c1" :: "s"(base), "i"(IMM) : "m0", "scc"); }
template <int IMM> DI void set_m0n(u32 base) { asm volatile("s_add_u32 m0, %0, %c1\n\ts_nop 0" :: "s"(base), "i"(IMM) : "m0", "scc"); }
template <int ID, int OFF> DI void dma(u32 voff, rsrc_t r, u32 soff) {
  asm volatile("buffer_load_dwordx4 %0, %1, %2 offen offset:%c3 lds ; ID %c4" :: "v"(voff), "s"(r), "s"(soff), "i"(OFF), "i"(ID));
}
template <int ID, int OFF = 0> DI void pub16(u32x4 v, rsrc_t r, u32 voff, u32 soff) {
  asm volatile("buffer_store_dwordx4 %0, %1, %2, %3 offen offset:%c5 sc1 ; ID %c4\n\ts_nop 1" :: "v"(v), "v"(voff), "s"(r), "s"(soff), "i"(ID), "i"(OFF));
}
template <int ID> DI void pub8(u32x2 v, rsrc_t r, u32 voff, u32 soff) {
  asm volatile("buffer_store_dwordx2 %0, %1, %2, %3 offen sc1 ; ID %c4" :: "v"(v), "v"(voff), "s"(r), "s"(soff), "i"(ID));
}
template <int ID> DI void pub4(u32 v, rsrc_t r, u32 voff, u32 soff) {
  asm volatile("buffer_store_dword %0, %1, %2, %3 offen sc1 ; ID %c4" :: "v"(v), "v"(voff), "s"(r), "s"(soff), "i"(ID));
}
template <int N, int A = 0> DI void wait_vm() { static_assert(N >= 0 && N < 64, ""); asm volatile("s_waitcnt vmcnt(%c0) ; NEED %c1" :: "i"(N), "i"(A) : "memory"); }
template <int N> DI void wait_a(u32x4&) { static_assert(N >= 0, ""); asm volatile("s_waitcnt vmcnt(%c0)" :: "i"(N < 63 ? N : 63)); }
DI void tie_a(u32x4& a) { asm volatile("" : "+a"(a)); }
template <class T> DI void tie_v(T& a) { asm volatile("" : "+v"(a)); }
template <int N, int A = 0, int B = 0> DI void bar_vm() {
  static_assert(N >= 0, "");
  asm volatile("s_waitcnt vmcnt(%c0) lgkmcnt(0) ; NEED %c1 NEED %c2\n\ts_barrier" :: "i"(N < 63 ? N : 63), "i"(A), "i"(B) : "memory");
}
DI void bar() { asm volatile("s_waitcnt lgkmcnt(0)\n\ts_barrier" ::: "memory"); }
#define SCHED() __builtin_amdgcn_sched_barrier(0)

DI void st16(u32x4 v, rsrc_t r, u32 voff, u32 soff) { __builtin_amdgcn_raw_buffer_store_b128(v, r, voff, soff, 16); }
DI void st8(u32x2 v, rsrc_t r, u32 voff, u32 soff) { __builtin_amdgcn_raw_buffer_store_b64(v, r, voff, soff, 16); }
DI void st4(u32 v, rsrc_t r, u32 voff, u32 soff) { __builtin_amdgcn_raw_buffer_store_b32(v, r, voff, soff, 16); }

DI f32x16 mfma(u32x4 a, u32x4 b, f32x16 c) {
  return __builtin_amdgcn_mfma_f32_32x32x16_bf16(__builtin_bit_cast(bf16x8, a), __builtin_bit_cast(bf16x8, b), c, 0, 0, 0);
}
template <bool Z> DI void mm_va(f32x16& c, u32x4 a, u32x4 b) {
  if constexpr (Z) asm volatile("v_mfma_f32_32x32x16_bf16 %0, %1, %2, 0" : "=&v"(c) : "v"(a), "a"(b));
  else asm volatile("v_mfma_f32_32x32x16_bf16 %0, %1, %2, %0" : "+v"(c) : "v"(a), "a"(b));
}
template <bool Z> DI void mm_av(f32x16& c, u32x4 a, u32x4 b) {
  if constexpr (Z) asm volatile("v_mfma_f32_32x32x16_bf16 %0, %1, %2, 0" : "=&v"(c) : "a"(a), "v"(b));
  else asm volatile("v_mfma_f32_32x32x16_bf16 %0, %1, %2, %0" : "+v"(c) : "a"(a), "v"(b));
}
template <bool Z> DI void mm_vv(f32x16& c, u32x4 a, u32x4 b) {
  if constexpr (Z) asm volatile("v_mfma_f32_32x32x16_bf16 %0, %1, %2, 0" : "=&v"(c) : "v"(a), "v"(b));
  else asm volatile("v_mfma_f32_32x32x16_bf16 %0, %1, %2, %0" : "+v"(c) : "v"(a), "v"(b));
}
template <bool Z> DI void m16_va(f32x4& c, u32x4 a, u32x4 b) {
  if constexpr (Z) asm volatile("v_mfma_f32_16x16x32_bf16 %0, %1, %2, 0" : "=&v"(c) : "v"(a), "a"(b));
  else asm volatile("v_mfma_f32_16x16x32_bf16 %0, %1, %2, %0" : "+v"(c) : "v"(a), "a"(b));
}
DI void m16_fence(f32x4 (&y)[4]) { asm volatile("s_nop 11" : "+v"(y[0]), "+v"(y[1]), "+v"(y[2]), "+v"(y[3])); }
DI void mm_fence(f32x16& a, f32x16& b) { asm volatile("s_nop 15" : "+v"(a), "+v"(b)); }
DI void mm_tie(f32x16& a, f32x16& b) { asm volatile("" : "+v"(a), "+v"(b)); }
DI float smul(float a, float b) { float r; asm("v_mul_f32_e32 %0, %1, %2" : "=v"(r) : "v"(a), "v"(b)); return r; }
DI float sfma(float a, float b, float c) { float r; asm("v_fma_f32 %0, %1, %2, %3" : "=v"(r) : "v"(a), "v"(b), "v"(c)); return r; }
DI u32x4 lds16(LAS char* sm, u32 off) { return *(LAS u32x4*)(sm + off); }
DI f32x4 ldsf4(LAS char* sm, u32 off) { return *(LAS f32x4*)(sm + off); }
DI u32x4 lds_tr(LAS char* sm, u32 off0, u32 off1) {
  s16x4 lo = __builtin_amdgcn_ds_read_tr16_b64_v4i16((LAS s16x4*)(sm + off0));
  s16x4 hi = __builtin_amdgcn_ds_read_tr16_b64_v4i16((LAS s16x4*)(sm + off1));
  s16x8 w = __builtin_shufflevector(lo, hi, 0, 1, 2, 3, 4, 5, 6, 7);
  return __builtin_bit_cast(u32x4, w);
}
template <int OFF> DI u32x4 tr_asm(u32 a) {
  u32x2 lo, hi;
  asm volatile("ds_read_b64_tr_b16 %0, %2 offset:%c3\n\tds_read_b64_tr_b16 %1, %2 offset:%c4" : "=&v"(lo), "=&v"(hi) : "v"(a), "i"(OFF), "i"(OFF + 512));
  return __builtin_shufflevector(lo, hi, 0, 1, 2, 3);
}
template <int OFF> DI u32x4 rd_asm(u32 a) { u32x4 r; asm volatile("ds_read_b128 %0, %1 offset:%c2" : "=v"(r) : "v"(a), "i"(OFF)); return r; }
template <int N> DI void wait_l() { asm volatile("s_waitcnt lgkmcnt(%c0)" :: "i"(N)); }
DI void lds_spin(LAS char* sm, u32 off, u32 want) {
  for (u32 sp = 0; sp < SPIN_CAP; ++sp)
    if (__builtin_amdgcn_readfirstlane(*(volatile LAS u32*)(sm + off)) == want) break;
  asm volatile("" ::: "memory");
}
template <int CTRL> DI float dpp(float v) { return __int_as_float(__builtin_amdgcn_update_dpp(0, __float_as_int(v), CTRL, 0xF, 0xF, true)); }
DI float add_x16(float v) { auto r = __builtin_amdgcn_permlane16_swap(__float_as_uint(v), __float_as_uint(v), false, false); u32 a = r[0], b = r[1]; return __uint_as_float(a) + __uint_as_float(b); }
DI float add_x32(float v) { auto r = __builtin_amdgcn_permlane32_swap(__float_as_uint(v), __float_as_uint(v), false, false); u32 a = r[0], b = r[1]; return __uint_as_float(a) + __uint_as_float(b); }
DI float red_x32(float a, float b) { auto r = __builtin_amdgcn_permlane32_swap(__float_as_uint(a), __float_as_uint(b), false, false); u32 p = r[0], q = r[1]; return __uint_as_float(p) + __uint_as_float(q); }
DI float red_x16(float a, float b) { auto r = __builtin_amdgcn_permlane16_swap(__float_as_uint(a), __float_as_uint(b), false, false); u32 p = r[0], q = r[1]; return __uint_as_float(p) + __uint_as_float(q); }
DI float red_x8(float a, float b, bool hi) { float keep = hi ? b : a, give = hi ? a : b; return keep + dpp<0x128>(give); }
DI float wave_sum(float v) {
  v += dpp<0xB1>(v); v += dpp<0x4E>(v); v += dpp<0x141>(v); v += dpp<0x140>(v);
  return add_x32(add_x16(v));
}
DI float wave_sum4(float v0, float v1, float v2, float v3) {
  float v = red_x16(red_x32(v0, v1), red_x32(v2, v3));
  v += dpp<0xB1>(v); v += dpp<0x4E>(v); v += dpp<0x141>(v); v += dpp<0x140>(v);
  return v;
}
DI float lane_of(float v, int row) { return __int_as_float(__builtin_amdgcn_readlane(__float_as_int(v), (row & 1) * 32 + (row >> 1) * 16)); }
template <bool NEG> DI u32 scale_pack(float a, float b, float sa, float sb) {
  u32 o; float t0, t1;
  if constexpr (NEG) asm volatile("v_mul_f32 %1, -%3, %5\n\tv_mul_f32 %2, -%4, %6\n\tv_cvt_pk_bf16_f32 %0, %1, %2" : "=v"(o), "=&v"(t0), "=&v"(t1) : "v"(a), "v"(b), "v"(sa), "v"(sb));
  else asm volatile("v_mul_f32 %1, %3, %5\n\tv_mul_f32 %2, %4, %6\n\tv_cvt_pk_bf16_f32 %0, %1, %2" : "=v"(o), "=&v"(t0), "=&v"(t1) : "v"(a), "v"(b), "v"(sa), "v"(sb));
  return o;
}
DI u32x4 chunk_swap(u32 a0, u32 a1, u32 b0, u32 b1) {
  auto r0 = __builtin_amdgcn_permlane32_swap(a0, b0, false, false);
  auto r1 = __builtin_amdgcn_permlane32_swap(a1, b1, false, false);
  u32x4 o; o[0] = r0[0]; o[1] = r1[0]; o[2] = r0[1]; o[3] = r1[1];
  return o;
}
DI u32x4 chunk_swap16(u32 a0, u32 a1, u32 b0, u32 b1) {
  auto r0 = __builtin_amdgcn_permlane16_swap(a0, b0, false, false);
  auto r1 = __builtin_amdgcn_permlane16_swap(a1, b1, false, false);
  u32x4 o; o[0] = r0[0]; o[1] = r1[0]; o[2] = r0[1]; o[3] = r1[1];
  return o;
}
struct Poll {
  u32x2 v[4];
  DI void init() { sfor<4>([&](auto i) { v[i] = u32x2{0u, 0u}; }); }
  DI void run(u32 voff, rsrc_t r, u32 soff, u64 tag, u64 lanes) {
    u32 cnt; u64 ex = 0; asm volatile("" : "+s"(ex));
#define PL_LD(n) "buffer_load_dwordx2 %" #n ", %5, %6, %7 offen sc1\n\t"
#define PL_STEP(n, c) PL_LD(n) "s_waitcnt vmcnt(3)\n\tv_cmp_ne_u64 vcc, %8, %" #c "\n\ts_cbranch_vccz .Lpd%=\n\ts_sleep %c9\n\t"
    asm volatile("s_mov_b64 %11, exec\n\ts_mov_b64 exec, %12\n\t" PL_LD(0) "s_sleep %c9\n\t" PL_LD(1) "s_sleep %c9\n\t" PL_LD(2) "s_sleep %c9\n\ts_mov_b32 %4, %c10\n"
                 ".Lpl%=:\n\t" PL_STEP(3, 0) PL_STEP(0, 1) PL_STEP(1, 2) PL_STEP(2, 3)
                 "s_sub_u32 %4, %4, 1\n\ts_cbranch_scc0 .Lpl%=\n"
                 ".Lpd%=:\n\ts_mov_b64 exec, %11"
                 : "+v"(v[0]), "+v"(v[1]), "+v"(v[2]), "+v"(v[3]), "=&s"(cnt)
                 : "v"(voff), "s"(r), "s"(soff), "s"(tag), "i"(POLL_SLEEP), "i"(SPIN_CAP / 4), "s"(ex), "s"(lanes)
                 : "vcc", "scc", "memory");
  }
  DI void done() { sfor<4>([&](auto i) { tie_v(v[i]); }); }
};
DI void post_flag(char* p, u64 tag) { __hip_atomic_store((GAS u64*)p, tag, RLX_AGENT); }

#define STAMP(i) do {} while (0)
#define FS(i) do {} while (0)

#define PRO_LEFT 0
#define PRE_WAIT 1
#define FA_AT 0
#define NH1 2
#define LATIN 0
#define ROT2 0
#define ROT4 0
#define ROT5 0
#define NINIT 3
#define NTHR 8
#define ND1 4
constexpr int P1_MODC = 2, P1_MODFL = 8, P1_MODLD = 10, P1_MODUSE = 12, P1_XR = 11, P1_RS = 6, P1_GV = 13;
constexpr int p1_extra(int k) { return k == P1_MODFL ? 1 : k == P1_MODLD ? 5 : k == P1_XR ? 2 : 0; }
constexpr int P1_NE = 14 + 4 * (7 - NINIT);
constexpr int p1_e(int t, int m) { int e = t < 7 ? (m < 2 ? 2 * t + m : -1) : (m == 0 ? 7 + t : -1); return e < P1_NE ? e : -1; }
constexpr int p1_est(int e) { return e >= 4 && e < 8 ? NINIT : e >= 14 ? NINIT + 1 + ((e - 14) >> 2) : -1; }
struct Sched1 { int posx[16][4], posa[24], pose, posext[16], top[16][4], bar[16], pre, end; };
constexpr Sched1 mk_sched1() {
  Sched1 s{}; int n = 0;
  for (int i = 0; i < NINIT; ++i) s.posa[i] = -1;
  for (int k = 0; k < 2; ++k) {
    for (int i = 0; i < 4; ++i) s.posx[k][i] = n++;
  }
  s.pre = n;
  for (int k = 0; k < 16; ++k)
    for (int i = 0; i < 4; ++i) {
      const int t = 4 * k + i, g = t - 3;
      s.top[k][i] = n;
      for (int m = 0; m < 2; ++m) {
        if (m == 1 && i == 3) s.bar[k] = n;
        const int e = p1_e(t, m);
        if (e < 0) continue;
        if (e < 4) s.posx[2][e] = n; else if (p1_est(e) >= 0) s.posa[p1_est(e)] = n; else s.pose = n;
        n++;
      }
      if (i == 3) { n += p1_extra(k); s.posext[k] = n - 1; }
      if (g >= 0 && (g >> 2) < 8 + ND1) s.posa[8 + (g >> 2)] = n++;
      if (k + 3 < 16) s.posx[k + 3][i] = n++;
      if (t == 56 || t == 57) n += 3;
      if (t >= 58) n++;
    }
  s.end = n;
  return s;
}
constexpr Sched1 S1 = mk_sched1();
#define NG0 6
constexpr int P4_PRE = 8;
constexpr int P4_E0 = 34;
constexpr int P4_ST = 6, P4_FA = 59;
constexpr int p4_u0(int t, int m) { int u = (t - P4_E0) * 12 + (m == 0 ? 0 : m == 1 ? 2 : m == 2 ? 6 : m == 3 ? 8 : 12); return u < 0 ? 0 : u > 192 ? 192 : u; }
struct Sched4 { int posx[32], posg[24], posst, posfa, possto, wait[64], bar[16], end; };
constexpr Sched4 mk_sched4() {
  Sched4 s{}; int n = 0;
  for (int i = 0; i < P4_PRE; ++i) s.posx[i] = n++;
  n += 4; s.posst = n - 1;
  for (int t = 0; t < 64; ++t) {
    s.wait[t] = n;
    for (int m = 0; m < 4; ++m) {
      if (m == 0 && t < 4 * (8 - NG0)) s.posg[NG0 + (t >> 2)] = n++;
      if (m == 1 && (t & 3) == 3) s.bar[t >> 2] = n;
      for (int u = p4_u0(t, m); u < p4_u0(t, m + 1); ++u) if (u % 48 == 47) s.possto = n++;
      if (m == 1) { if (t + P4_PRE < 32) s.posx[t + P4_PRE] = n++; }
      if (m == 3 && t >= 3) s.posg[8 + ((t - 3) >> 2)] = n++;
    }
  }
  s.end = n;
  return s;
}
constexpr Sched4 S4 = mk_sched4();
constexpr int P5_PRE = 10, P5_FB = 13;
struct Sched5 { int posa[56], posn[16], posfb, wait[56], bar[16], end; };
constexpr Sched5 mk_sched5() {
  Sched5 s{}; int n = P5_PRE;
  for (int i = 0; i < P5_PRE; ++i) s.posa[i] = i;
  for (int t = 0; t < 56; ++t) {
    s.wait[t] = n;
    if ((t & 3) == 3) s.bar[t >> 2] = n;
    if (t + P5_PRE < 56) s.posa[t + P5_PRE] = n++;
    if (t == P5_FB) s.posfb = n++;
    if (t >= 3 && t < 27) s.posn[8 + ((t - 3) >> 2)] = n++;
  }
  s.end = n;
  return s;
}
constexpr Sched5 S5 = mk_sched5();

struct Ge { float g0, g1, a0, a1, m0, m1; };
template <int U> DI void ge_unit(Ge& e, float q0, float q1, float u0, float u1, float r, float sd, float c3, float c1, u32& o) {
  if constexpr (U == 0) asm volatile("v_mul_f32 %0, %2, %3\n\tv_mul_f32 %1, %2, %4" : "=&v"(e.g0), "=&v"(e.g1) : "v"(r), "v"(q0), "v"(q1));
  if constexpr (U == 1) asm volatile("v_mul_f32 %0, %2, %2\n\tv_mul_f32 %1, %3, %3" : "=&v"(e.a0), "=&v"(e.a1) : "v"(e.g0), "v"(e.g1));
  if constexpr (U == 2) asm volatile("v_fma_f32 %0, %0, %2, %3\n\tv_fma_f32 %1, %1, %2, %3" : "+v"(e.a0), "+v"(e.a1) : "v"(c3), "v"(c1));
  if constexpr (U == 3) asm volatile("v_mul_f32 %0, %0, %2\n\tv_mul_f32 %1, %1, %3" : "+v"(e.a0), "+v"(e.a1) : "v"(e.g0), "v"(e.g1));
  if constexpr (U == 4) asm volatile("v_exp_f32 %0, %0" : "+v"(e.a0));
  if constexpr (U == 5) asm volatile("v_exp_f32 %0, %0" : "+v"(e.a1));
  if constexpr (U == 6) asm volatile("v_mul_f32 %0, %2, %4\n\tv_mul_f32 %1, %3, %5" : "=&v"(e.m0), "=&v"(e.m1) : "v"(e.g0), "v"(e.g1), "v"(u0), "v"(u1));
  if constexpr (U == 7) asm volatile("v_fma_f32 %0, %0, %2, %2\n\tv_fma_f32 %1, %1, %2, %2" : "+v"(e.a0), "+v"(e.a1) : "v"(sd));
  if constexpr (U == 8) asm volatile("v_rcp_f32 %0, %0" : "+v"(e.a0));
  if constexpr (U == 9) asm volatile("v_rcp_f32 %0, %0" : "+v"(e.a1));
  if constexpr (U == 10) asm volatile("v_mul_f32 %0, %0, %2\n\tv_mul_f32 %1, %1, %3" : "+v"(e.m0), "+v"(e.m1) : "v"(e.a0), "v"(e.a1));
  if constexpr (U == 11) asm volatile("v_cvt_pk_bf16_f32 %0, %1, %2" : "=v"(o) : "v"(e.m0), "v"(e.m1));
}
DI void mac2(float& a0, float& a1, u32 d, float s) {
  float t0, t1;
  asm volatile("v_lshlrev_b32 %2, 16, %4\n\tv_and_b32 %3, 0xffff0000, %4\n\tv_fmac_f32 %0, %5, %2\n\tv_fmac_f32 %1, %5, %3"
               : "+v"(a0), "+v"(a1), "=&v"(t0), "=&v"(t1) : "v"(d), "v"(s));
}

__global__ void __launch_bounds__(256, 1)
mk_block(const u16* __restrict__ x, const u16* __restrict__ Sre, const u16* __restrict__ Sim, const u16* __restrict__ Dre,
         const u16* __restrict__ Dim, const u16* __restrict__ Wg, const u16* __restrict__ Wu, const u16* __restrict__ Wn,
         const u16* __restrict__ cvec, const u16* __restrict__ wmod, u16* __restrict__ out, char* __restrict__ ws) {
  extern __shared__ __attribute__((aligned(16))) char smem_[];
  LAS char* sm = (LAS char*)smem_;
  const int tid = threadIdx.x, lane = tid & 63, w = __builtin_amdgcn_readfirstlane(tid >> 6);
  const int l31 = lane & 31, hh = lane >> 5, l15 = lane & 15, qq = lane >> 4;
  const int bid = blockIdx.x, xcd = bid & 7, loc = bid >> 3;
  const int tb = xcd * 4 + (loc >> 3), nt = loc & 7;
  const int cb = nt & 3, kh = nt >> 2;
  const int rb = (xcd >> 1) * 4 + loc / 7, ft = (xcd & 1) * 7 + loc % 7;
  const int w1 = w & 1, wk = w >> 1;
  const u32 lane16 = lane * 16, wl = w * 4096;
  const rsrc_t rX = mk_rsrc(x, S * D * 2);
  u32x4 xr[4];
  if (w < 2) sfor<4>([&](auto i) { ld_v4p<i * 1024>(xr[i], lane16, rX, (bid * 8 + w * 4) * 1024); });
  STAMP(0); FS(90);
  const rsrc_t rW = mk_rsrc(ws, WS_END);
  u32 vA[4];
  sfor<4>([&](auto i) { vA[i] = (lane >> 3) * 4096 + (((lane & 7) ^ ((i & 1) * 4 + qq)) << 4) + i * (8 * 4096 - 1024); });
  const int ro = (cb + LATIN * tb) & 3;
  auto kblk = [&](int st) { return kh * 16 + ((ro * 4 + st) & 15); };
  u64 tag;

  Poll pl;
  pl.init();
  if (tid < 4) *(LAS u32*)(sm + L_CNT + tid * 4) = 0;
  u64 pc; u32 clen, cof2, cof4, cof5, cpf = 0;
  asm volatile("s_getpc_b64 %0\n.Lmk_a:\n\ts_mov_b32 %1, .Lmk_z-.Lmk_a\n\ts_mov_b32 %2, .Lmk_2-.Lmk_a\n\ts_mov_b32 %3, .Lmk_4-.Lmk_a\n\ts_mov_b32 %4, .Lmk_5-.Lmk_a"
               : "=s"(pc), "=s"(clen), "=s"(cof2), "=s"(cof4), "=s"(cof5));
  const rsrc_t rP = mk_rsrc((const void*)pc, clen);
  bar();
  asm volatile("s_load_dwordx2 %0, %1, 0x0" : "=s"(tag) : "s"(ws + WS_GEN));
  const u32 sF = WS_F1 + (xcd * 256 + kh * 128) * 128;
  if (w < 2) {
    wait_vm<0>();
    sfor<4>([&](auto i) { tie_v(xr[i]); });
    FS(91);
    f32x2 v[4][4];
    float r4[4];
    sfor<4>([&](auto i) {
      sfor<4>([&](auto j) { u32 d = xr[i][+j]; v[i][j] = f32x2{b_lo(d), b_hi(d)}; });
      const f32x2 t = (v[i][0] + v[i][1]) + (v[i][2] + v[i][3]);
      r4[i] = t[0] + t[1];
    });
    const float mu = wave_sum4(r4[0], r4[1], r4[2], r4[3]) * (1.f / D);
    sfor<4>([&](auto i) {
      const float m = lane_of(mu, i);
      sfor<4>([&](auto j) { v[i][j] -= m; });
      const f32x2 t = (v[i][0] * v[i][0] + v[i][1] * v[i][1]) + (v[i][2] * v[i][2] + v[i][3] * v[i][3]);
      r4[i] = t[0] + t[1];
    });
    const float rs = __builtin_amdgcn_rsqf(wave_sum4(r4[0], r4[1], r4[2], r4[3]) * (1.f / D) + 1e-5f);
    sfor<4>([&](auto i) {
      const float r = lane_of(rs, i);
      u32x4 o;
      sfor<4>([&](auto j) { const f32x2 t = v[i][j] * r; o[+j] = pack2(t[0], t[1]); });
      *(LAS u32x4*)(sm + L_EX + (w * 4 + i) * 1088 + lane16) = o;
    });
    FS(110);
    asm volatile("s_waitcnt lgkmcnt(0)" : "+s"(tag) :: "memory");
    tag += 1;
    *(volatile LAS u32*)(sm + L_CNT + w * 4) = 1;
    lds_spin(sm, L_CNT + (w ^ 1) * 4, 1);
    FS(92);
    const u32 q = (lane & 15) >> 2, pp = lane & 3;
    const u32 ta = L_EX + q * 1088 + (w * 256 + qq * 16 + pp * 4) * 2;
    sfor<4>([&](auto m) {
      u32x4 o = lds_tr(sm, ta + m * 128, ta + m * 128 + 4 * 1088);
      pub16<1>(o, rW, hh * 1024 + l31 * 16, WS_XN + (bid >> 1) * 16384 + (w * 8 + m * 2) * 1024 + (bid & 1) * 512);
    });
    wait_vm<0, 1>();
    FS(93);
    if (w == 1) {
      *(volatile LAS u32*)(sm + L_CNT + 8) = 1;
    } else {
      lds_spin(sm, L_CNT + 8, 1);
      if (lane < 8) post_flag(ws + WS_F1 + (lane * 256 + bid) * 128, tag);
      STAMP(14);
      FS(111);
    }
  } else {
    ld_v1p<0>(cpf, lane * 128, rP, w1 == 0 ? 0 : (1 + loc % 5) * 8192);
    const rsrc_t r0 = mk_rsrc(w1 == 0 ? Sre : Sim, S * S * 2);
    sfor<NINIT * 8>([&](auto qc) {
      constexpr int q = qc, st = q >> 3, hf = (q >> 2) & 1, p = q & 3;
      if constexpr (p == 0) set_m0n<st * 16384 + hf * 4096>(w1 * 8192);
      if constexpr (q >= NTHR) wait_vm<NTHR - 1>();
      dma<100 + st, p * 1024>(vA[p], r0, (tb * 64 + hf * 32) * 4096 + kblk(st) * 128);
    });
    asm volatile("s_waitcnt lgkmcnt(0)" : "+s"(tag));
    tag += 1;
    wait_vm<0, 100>();
  }
  const rsrc_t rS = mk_rsrc(w < 2 ? Sre : Sim, S * S * 2);
  const rsrc_t rD = mk_rsrc(w < 2 ? Dre : Dim, D * D * 2);
  u32 vD[4], vX[4];
  sfor<4>([&](auto i) { vD[i] = (lane >> 3) * 1024 + (((lane & 7) ^ ((i & 1) * 4 + qq)) << 4) + i * (8 * 1024 - 1024); vX[i] = lane16 + i * 16384; tie_v(vD[i]); tie_v(vX[i]); });
  const u32 sA = (tb * 64 + w1 * 32) * 4096, sD = (nt * 64 + w1 * 32) * 1024;
  const u32 sXN = WS_XN + (cb * 4 + w) * 1024;
  auto a_dma = [&](auto stc, auto pc) { constexpr int st = stc, p = pc; dma<100 + st, p * 1024>(vA[p], rS, sA + kblk(st) * 128); };
  auto d_dma = [&](auto sc_, auto pc) { constexpr int s = sc_, p = pc; dma<2, p * 1024>(vD[p], rD, sD + s * 128); };
  u32x4 xw[4][4];
  auto xn_ld = [&](auto stc, auto ic_) { constexpr int st = stc, i = ic_; ld_a<0>(xw[st & 3][i], vX[i], rW, sXN + kblk(st) * 65536); };
  u32x4 mg[5], gvd, wm[4], xres[2];
  u32x2 cr, fm;
  const int ukq = bid < 224 ? bid / 56 : 3, ucb = bid % 56;
  const int mkq = bid < 192 ? bid / 48 : 3, mcb = bid % 48;
  u32 aB[4][2];
  sfor<4>([&](auto i) { sfor<2>([&](auto hf) { aB[i][hf] = l31 * 128 + (((2 * i + hh) ^ ((l31 >> 1) & 7)) << 4) + hf * 65536; tie_v(aB[i][hf]); }); });
  u32x4 af[2][4], rf[2], gvw[4], warm[2];
  auto a_rd = [&](auto tc, auto jc) {
    constexpr int t = tc, j = jc;
    if constexpr (t < 56) af[t & 1][j] = rd_asm<((t >> 2) & 3) * 16384 + j * 4096>(aB[t & 3][(t >> 4) & 1]);
    else af[t & 1][j] = rd_asm<(2 + (t & 1)) * 16384 + ((t - 56) >> 1) * 4096>(aB[j][1]);
  };
  f32x16 acc[4], rsa;
  u32x4 ones = {0x3f803f80u, 0x3f803f80u, 0x3f803f80u, 0x3f803f80u};
  tie_v(ones);
  const bool h0 = (lane >> 3) & 1;
  u32 lM = L_MODV; tie_v(lM);
  u32 mI = w < 2 ? tid * 4 : w == 2 ? (lane < 32 ? 512 + cb * 128 + lane * 4 : 1024 + (qq & 1) * 1024 + nt * 64 + l15 * 4) : 1536 + hh * 512 + ukq * 128 + l31 * 4;
  u32 vFM = ((qq * 48 + (w == 0 && l15 == 15 ? 40 + nt : __builtin_amdgcn_ds_bpermute(l15 * 16, (int)mI) / 64)) * 128);
  float ma[8], sc4[4];
  tie_v(mI); tie_v(vFM);
  f32x4 sc[4];
  u32 pk[8];
  u32 vP = (l31 >> 4) * 65536 + hh * 256 + l15 * 16;
  tie_v(vP);
  const u32 sP = WS_PQ + (tb * 4 * 64 + kh * 32 + cb * 4 + w) * 1024;
  auto p_unit = [&](auto jc, auto uc) {
    constexpr int j = jc, u = uc;
    pk[u] = scale_pack<(j >= 2)>(acc[j][2 * u], acc[j][2 * u + 1], sc[u >> 1][2 * (u & 1)], sc[u >> 1][2 * (u & 1) + 1]);
    if constexpr ((u & 3) == 3) {
      asm volatile("s_nop 1" : "+v"(pk[u]));
      pub16<3, (u >> 2) * 512>(chunk_swap(pk[u - 3], pk[u - 2], pk[u - 1], pk[u]), rW, vP, sP + ((j & 1) * 128 + (j >> 1) * 16) * 1024);
    }
  };
  u32 vF1 = ((ro * 32 + 32 + lane) & 127) * 128, vF2 = ((ro * 32 + 96 + l31) & 127) * 128;
  u32x2 fl[2];
  tie_v(vF1); tie_v(vF2);
  bar();
  FS(112);
  fl[0] = fl[1] = u32x2{0u, 0u};
  if (w == 0) {
    pl.run(((ro * 32 + l31) & 127) * 128, rW, sF, tag, 0xffffffffull);
    ld_v2t(fl[0], vF1, rW, sF); ld_v2t(fl[1], vF2, rW, sF);
  }
  bar();
  const u32 tag32 = (u32)tag;
  STAMP(1); FS(94);

  sfor<2>([&](auto kc) { sfor<4>([&](auto i) { xn_ld(kc, i); }); });

  FS(95);
  sfor<4>([&](auto j) { a_rd(ic<0>{}, j); });
  sfor<64>([&](auto tc) {
    constexpr int t = tc, k = t >> 2, i = t & 3, g = t - 3;
    constexpr bool many = t < P1_NE - 7 + 4;
    SCHED();
    if constexpr (t < 56) wait_a<S1.top[k][i] - 1 - S1.posx[k][i]>(xw[k & 3][i]);
    if constexpr (t == 56 || t == 57) wait_a<S1.top[k][i] - 1 - S1.posx[t - 42][3]>(xw[0][0]);
    if constexpr (t < 3) FS(97 + t);
    sfor<4>([&](auto mc) {
      constexpr int m = mc, e = p1_e(t, m);
      constexpr bool rs_next = t + 1 >= 4 * P1_RS && t + 1 < 4 * P1_RS + 16, rs_now = k >= P1_RS && k < P1_RS + 4;
      if constexpr (e >= 0 && p1_est(e) >= 0) set_m0<p1_est(e) * 16384>(wl);
      if constexpr (m == 2 && g >= 0 && (g >> 2) < 8 + ND1 && (many || i == 3)) set_m0<((g >> 2) & 7) * 16384>(wl);
      if constexpr (m == 0) wait_l<(rs_now ? 3 : 2)>();
      if constexpr (m == 2) wait_l<(t < 63 ? 2 : 0)>();
      if constexpr (t < 56) mm_av<t == 0>(acc[m], xw[k & 3][i], af[t & 1][m]);
      else mm_av<false>(acc[(t - 56) >> 1], xw[2 + (t & 1)][m], af[t & 1][m]);
      if constexpr (rs_now && m == 2) mm_vv<t == 4 * P1_RS>(rsa, ones, rf[t & 1]);
      SCHED();
      if constexpr (m == 0 && (i != 3 || k >= 14) && t < 63) { a_rd(ic<t + 1>{}, ic<0>{}); a_rd(ic<t + 1>{}, ic<1>{}); }
      if constexpr (m == 0 && t == 2) {
        pl.done(); tie_v(cpf);
        if (w == 0) {
  tie_v(fl[0]); tie_v(fl[1]);
  for (u32 sp = 0; sp < SPIN_CAP; ++sp) {
    const u32 th = (u32)(tag >> 32);
    if (__all(fl[0][0] == tag32 && fl[0][1] == th && fl[1][0] == tag32 && fl[1][1] == th)) break;
    __builtin_amdgcn_s_sleep(POLL_SLEEP);
    ld_v2t(fl[0], vF1, rW, sF);
    ld_v2t(fl[1], vF2, rW, sF);
    wait_vm<0>();
    tie_v(fl[0]); tie_v(fl[1]);
  }
        }
      }
      if constexpr (m == 1 && i == 3 && k < 14) {
        constexpr int na = S1.bar[k] - 1 - S1.posa[k == 13 ? 15 : k + 1];
        constexpr int ne = k == P1_MODC ? S1.bar[k] - 1 - S1.pose : k == P1_MODLD ? S1.bar[k] - 1 - S1.posext[P1_MODFL] : k == P1_MODUSE ? S1.bar[k] - 1 - S1.posext[P1_MODLD] : 63;
        bar_vm<(na < ne ? na : ne), 101 + k, (k == 13 ? 115 : 0)>();
        FS(k);
        a_rd(ic<t + 1>{}, ic<0>{}); a_rd(ic<t + 1>{}, ic<1>{});
      }
      if constexpr (e >= 0) {
        if constexpr (e < 4) xn_ld(ic<2>{}, ic<e>{});
        else if constexpr (p1_est(e) >= 0) a_dma(ic<p1_est(e)>{}, ic<((e < 8 ? e : e - 14) & 3)>{});
        else if constexpr (e < 12) ld_v4p<0>(wm[e - 8], (lane >> 3) * 4 * 6144 + (lane & 7) * 16, mk_rsrc(wmod, D * 6 * D * 2), (mkq * 128 + w * 32 + e - 8) * 6144 + mcb * 128);
        else if constexpr (e == 12) ld_v2p<0>(cr, (lane >> 3) * 8, mk_rsrc(cvec, D * 2), (mkq * 128 + w * 32) * 2);
        else ld_v4p<0>(gvd, lane16, mk_rsrc(bid < 128 ? Dre : Dim, D * D * 2), ((bid & 127) * 4 + w) * 1024);
      }
      if constexpr (m == 2 && t < 63) {
        a_rd(ic<t + 1>{}, ic<2>{}); a_rd(ic<t + 1>{}, ic<3>{});
        if constexpr (rs_next) rf[(t + 1) & 1] = rd_asm<(((t + 1) >> 2) & 3) * 16384>(aB[(t + 1) & 3][((t + 1) >> 4) & 1] + wl);
      }
      if constexpr (t == 56) sc[m] = ldsf4(sm, lM + (512 + w * 32 + m * 8 + hh * 4) * 4) + 1.f;
      if constexpr ((t == 56 || t == 57) && m >= 1) {
        constexpr int r = 3 * (t - 56) + m - 1;
        if constexpr (r < 4) ld_v4p<0>(gvw[r], (tid >> 3) * (F * 2) + (lane & 7) * 16, mk_rsrc(ucb < 28 ? Wg : Wu, D * F * 2), (ukq * 128 + r * 32) * (F * 2) + (ucb % 28) * 128);
        else ld_v4p<0>(warm[r - 4], tid * 16, mk_rsrc(Wn, F * D * 2), bid * 7168 + (r - 4) * 4096);
      }
      if constexpr (t >= 58) {
        if constexpr (m == 1 && (t & 1) == 0) asm volatile("s_nop 7" : "+v"(acc[((t - 56) >> 1) - 1]));
        if constexpr (m >= 1) p_unit(ic<((t - 56) >> 1) - 1>{}, ic<(t & 1) * 4 + m - 1>{});
        if constexpr (m == 3) p_unit(ic<((t - 56) >> 1) - 1>{}, ic<(t & 1) * 4 + 3>{});
      }
      if constexpr (m == 2 && i == 3) {
        if constexpr (k == P1_MODC) {
          sfor<4>([&](auto r) { tie_v(wm[r]); }); tie_v(cr);
          sfor<8>([&](auto q) { ma[q] = 0.f; tie_v(ma[q]); });
          sfor<4>([&](auto r) {
            u32 cd = cr[r >> 1];
            float cv = (r & 1) ? b_hi(cd) : b_lo(cd);
            sc4[r] = cv * __builtin_amdgcn_rcpf(1.f + __builtin_amdgcn_exp2f(-1.4426950408889634f * cv));
            tie_v(sc4[r]);
          });
        }
        if constexpr (k == P1_MODC + 1) {
          float b4[4], b2[2];
          sfor<4>([&](auto jj) { b4[jj] = red_x32(ma[jj], ma[4 + jj]); });
          sfor<2>([&](auto jj) { b2[jj] = red_x16(b4[jj], b4[2 + jj]); });
          *(LAS float*)(sm + lM + (L_MP - L_MODV) + w * 256 + ((lane & 7) * 8 + (lane >> 3)) * 4) = red_x8(b2[0], b2[1], h0);
        }
        if constexpr (k == P1_MODC + 2) {
          if (w == 0 && bid < 192) {
            float s = 0.f;
            sfor<4>([&](auto r) { s += *(LAS float*)(sm + lM + (L_MP - L_MODV) + r * 256 + lane * 4); });
            pub4<7>(__float_as_uint(s), rW, lane * 4, WS_MODP + (mkq * 3072 + mcb * 64) * 4);
          }
        }
        if constexpr (k == P1_MODC + 3) {
          if (w == 0 && bid < 192) {
            wait_vm<S1.bar[k] - S1.bar[k - 1], 7>();
            if (lane == 0) post_flag(ws + WS_FM + bid * 128, tag);
          }
        }
        if constexpr (k == P1_MODFL) ld_v2<0>(fm, vFM, rW, WS_FM);
        if constexpr (k == P1_MODLD) {
          tie_v(fm);
          for (u32 sp = 0; sp < SPIN_CAP; ++sp) {
            if (__all(fm[0] == tag32 && fm[1] == (u32)(tag >> 32))) break;
            __builtin_amdgcn_s_sleep(1);
            ld_v2<0>(fm, vFM, rW, WS_FM);
            wait_vm<0>();
            tie_v(fm);
          }
          sfor<4>([&](auto q) { ld_v4<0>(mg[q], mI * 4, rW, WS_MODP + q * 3072 * 4); });
          ld_v4<0>(mg[4], (qq * 3072 + 2560 + nt * 64 + l15 * 4) * 4, rW, WS_MODP);
        }
        if constexpr (k == P1_XR)
          sfor<2>([&](auto p) { ld_v4p<p * 64>(xres[p], l15 * 1024 + (qq & 1) * 32 + (qq >> 1) * 16, rX, (tb * 64 + w * 16) * 1024 + nt * 128); });
        if constexpr (k == P1_MODUSE) {
          sfor<5>([&](auto q) { tie_v(mg[q]); });
          f32x4 p0 = __builtin_bit_cast(f32x4, mg[0]), p1 = __builtin_bit_cast(f32x4, mg[1]), p2 = __builtin_bit_cast(f32x4, mg[2]), p3 = __builtin_bit_cast(f32x4, mg[3]);
          *(LAS f32x4*)(sm + lM + tid * 16) = (p0 + p1) + (p2 + p3);
        }
      }
      {
        constexpr int u0 = 4 * (t - 4 * P1_MODC - 4) + m + 1;
        if constexpr (u0 >= 0 && u0 < 15) sfor<(u0 == 14 ? 2 : 1)>([&](auto d) { constexpr int u = u0 + d; mac2(ma[2 * (u & 3)], ma[2 * (u & 3) + 1], wm[u >> 2][u & 3], sc4[u >> 2]); });
      }
      if constexpr (m == 2 && g >= 0 && (g >> 2) < 8 + ND1) {
        if constexpr ((g >> 2) < 8) a_dma(ic<8 + (g >> 2)>{}, ic<(g & 3)>{}); else d_dma(ic<(g >> 2) - 8>{}, ic<(g & 3)>{});
      }
      if constexpr (m == 3 && k + 3 < 16) xn_ld(ic<k + 3>{}, ic<i>{});
      SCHED();
    });
  });
  STAMP(2); FS(32);
  {
    mm_fence(acc[3], rsa);
    sfor<8>([&](auto u) { p_unit(ic<3>{}, u); });
    pub4<3>(__float_as_uint(rsa[0]), rW, l31 * 4, WS_RS + ((tb * 8 + nt) * 128 + w * 32) * 4);
    bar();
    FS(39);
    {
      tie_v(gvd);
      f32x4 s0 = ldsf4(sm, lM + lane * 32), s1 = ldsf4(sm, lM + lane * 32 + 16);
      u32 d0 = gvd[0], d1 = gvd[1], d2 = gvd[2], d3 = gvd[3];
      float v = s0[0] * b_lo(d0) + s0[1] * b_hi(d0) + s0[2] * b_lo(d1) + s0[3] * b_hi(d1) + s1[0] * b_lo(d2) + s1[1] * b_hi(d2) + s1[2] * b_lo(d3) + s1[3] * b_hi(d3);
      v = wave_sum(v);
      u32x2 gr = {__float_as_uint(v), tag32};
      pub8<0>(gr, rW, 0, WS_VG + (bid * 4 + w) * 8);
    }
    sfor<NH1>([&](auto s) { set_m0n<(ND1 + s) * 16384>(wl); sfor<4>([&](auto p) { d_dma(ic<ND1 + s>{}, p); }); });
    FS(108);
    bar_vm<4 * NH1 + 1, 3>();
    FS(109);
    if (tid == 0) post_flag(ws + WS_F2 + (tb * 8 + nt) * 128, tag);
    sfor<8 - ND1 - NH1>([&](auto s) { set_m0n<(ND1 + NH1 + s) * 16384>(wl); sfor<4>([&](auto p) { d_dma(ic<ND1 + NH1 + s>{}, p); }); });
    if (w == 1) ld_v1p<0>(cpf, lane * 128, rP, cof2 + (loc & 3) * 8192);
          if (w == 0) {
            f32x4 gq = __builtin_bit_cast(f32x4, mg[4]);
            sfor<4>([&](auto e) { gq[+e] = add_x32(add_x16(gq[+e])); });
            *(LAS f32x4*)(sm + lM + 4096 + l15 * 16) = gq;
          }
  }
  {
    sfor<4>([&](auto i) { tie_v(gvw[i]); });
    tie_v(warm[0]); tie_v(warm[1]);
    float a[16];
    sfor<16>([&](auto j) { a[j] = 0.f; });
    sfor<4>([&](auto i) {
      const float sh = *(LAS float*)(sm + lM + (768 + (tid >> 3) + i * 32) * 4);
      const float cs = 1.f + *(LAS float*)(sm + lM + (896 + (tid >> 3) + i * 32) * 4);
      sfor<4>([&](auto j) { u32 d = gvw[i][+j]; float lo = b_lo(d), hi = b_hi(d);
        a[2 * j] += cs * lo; a[2 * j + 1] += cs * hi; a[8 + 2 * j] += sh * lo; a[8 + 2 * j + 1] += sh * hi; });
    });
    float b8[8], b4[4];
    sfor<8>([&](auto j) { b8[j] = red_x32(a[j], a[8 + j]); });
    sfor<4>([&](auto j) { b4[j] = red_x16(b8[j], b8[4 + j]); });
    f32x2 o = {red_x8(b4[0], b4[2], h0), red_x8(b4[1], b4[3], h0)};
    *(LAS f32x2*)(sm + lM + (L_UVP - L_MODV) + w * 512 + (hh * 64 + (lane & 7) * 8 + ((lane >> 3) & 3) * 2) * 4) = o;
  }
  u32 dB[2][2];
  sfor<2>([&](auto u) { sfor<2>([&](auto hf) { dB[u][hf] = l15 * 128 + (((4 * u + qq) ^ ((l15 >> 1) & 7)) << 4) + hf * 65536; tie_v(dB[u][hf]); }); });
  STAMP(3); FS(33);
  if (w == 0) {
    if (PRE_WAIT) wait_vm<0>();
    pl.run(lane * 128, rW, WS_F2 + tb * 1024, tag, 0xffull);
    wait_vm<3, 2>();
    if (bid == 0 && lane == 0) post_flag(ws + WS_GEN, tag);
  } else {
    wait_vm<0, 2>();
  }
  bar();
  asm volatile(".Lmk_2:");
  STAMP(4); FS(34);

  f32x4 x1[4];
  {
    constexpr int PRE = 6;
    constexpr int SM = 16;
    u32x4 pa[32][2];
    const u32 so = WS_PQ + (tb * 4 + w) * 65536;
    const u32 vP1 = lane16 + 32768;
    auto pa_ld = [&](auto sc_, auto hc) {
      constexpr int s = sc_, h = hc;
      ld_a<(s & 1) * 1024>(pa[s][h], h ? vP1 : lane16, rW, so + (s >> 2) * 2048 + ((s >> 1) & 1) * 16384);
    };
    sfor<PRE>([&](auto s) { pa_ld(s, ic<0>{}); pa_ld(s, ic<1>{}); });
  if (w == 1 && bid < 224) {
    float su = 0.f, sv = 0.f;
    sfor<4>([&](auto i) { su += *(LAS float*)(sm + lM + (L_UVP - L_MODV) + i * 512 + lane * 4); sv += *(LAS float*)(sm + lM + (L_UVP - L_MODV) + i * 512 + 256 + lane * 4); });
    u32x4 g = {__float_as_uint(su), tag32, __float_as_uint(sv), tag32};
    pub16<0>(g, rW, lane16, WS_UVG + (ukq * 3584 + (ucb / 28) * F + (ucb % 28) * 64) * 16);
  }
    u32 rp[4];
    u32x4 vg, xt;
    float rre, rim;
    f32x4 ga[4], cbv[4];
    f32x4 y[4];
    u32x4 df[2][4];
    auto d_rd = [&](auto sc_, auto cc) { constexpr int s = sc_, c = cc; df[s & 1][c] = rd_asm<((s >> 2) & 3) * 16384 + ((s >> 1) & 1) * 8192 + c * 2048>(dB[s & 1][s >> 4]); };
    sfor<4>([&](auto c) { d_rd(ic<0>{}, c); });
    sfor<32>([&](auto sc_) {
      constexpr int s = sc_;
      constexpr int issued = 2 * (s + PRE) < 64 ? 2 * (s + PRE) : 64;
      constexpr int small = (s > SM && s <= SM + PRE) ? 5 : 0;
      SCHED();
      wait_l<0>();
      wait_a<issued - 1 - 2 * s + small>(pa[s][0]);
      if constexpr (s == 0) { pl.done(); tie_v(cpf); }
      if constexpr ((s & 3) == 0) FS(80 + (s >> 2));
      sfor<8>([&](auto mc) {
        constexpr int m = mc, c = m & 3, h = m >> 2, n = 2 * (s + PRE) + h;
        if constexpr (m == 4) wait_a<(issued < 64 ? issued + 1 : 64) - 1 - (2 * s + 1) + small>(pa[s][1]);
        m16_va<s == 0 && h == 0>(y[c], df[s & 1][c], pa[s][h]);
        SCHED();
        if constexpr (h == 0 && s < 31) d_rd(ic<s + 1>{}, ic<c>{});
        if constexpr (c == 1 && n < 64) pa_ld(ic<s + PRE>{}, ic<h>{});
        if constexpr (s == SM && m == 7) {
          sfor<4>([&](auto p) { ld_v1<p * 512>(rp[p], ((qq >> 1) * 512 + (qq & 1) * 64 + w * 16 + l15) * 4, rW, WS_RS + tb * 4096); });
          ld_v4<0>(vg, (hh * 512 + nt * 64 + 2 * l31) * 8, rW, WS_VG);
        }
        if constexpr (s == SM + PRE + 2 && m == 7) {
          sfor<4>([&](auto p) { tie_v(rp[p]); });
          tie_v(vg); tie_v(xres[0]); tie_v(xres[1]);
          for (u32 sp = 0; sp < SPIN_CAP; ++sp) {
            if (__all(vg[1] == tag32 && vg[3] == tag32)) break;
            __builtin_amdgcn_s_sleep(1);
            ld_v4<0>(vg, (hh * 512 + nt * 64 + 2 * l31) * 8, rW, WS_VG);
            wait_vm<0>();
            tie_v(vg);
          }
          { u32 a = vg[0], b = vg[2]; u32x2 o = {a, b}; *(LAS u32x2*)(sm + lM + (L_RS - L_MODV) + w * 512 + lane * 8) = o; }
          const float r4 = add_x32((__uint_as_float(rp[0]) + __uint_as_float(rp[1])) + (__uint_as_float(rp[2]) + __uint_as_float(rp[3])));
          auto r = __builtin_amdgcn_permlane16_swap(__float_as_uint(r4), __float_as_uint(r4), false, false);
          u32 a = r[0], b = r[1]; rre = __uint_as_float(a); rim = __uint_as_float(b);
          tie_v(rre); tie_v(rim);
        }
        if constexpr (s > SM + PRE + 2 && s <= SM + PRE + 6 && m == 7) {
          constexpr int cc = s - (SM + PRE + 3);
          if constexpr ((cc & 1) == 0) xt = chunk_swap16(xres[cc >> 1][0], xres[cc >> 1][1], xres[cc >> 1][2], xres[cc >> 1][3]);
          const u32 eo = (cc * 16 + qq * 4) * 4;
          f32x4 vre = ldsf4(sm, lM + (L_RS - L_MODV) + w * 512 + eo), vim = ldsf4(sm, lM + (L_RS - L_MODV) + w * 512 + 256 + eo);
          ga[cc] = ldsf4(sm, lM + 640 * 4 + eo); cbv[cc] = ldsf4(sm, lM + 704 * 4 + eo) + 1.f;
          const u32 d0 = xt[2 * (cc & 1)], d1 = xt[2 * (cc & 1) + 1];
          f32x4 xv = {b_lo(d0), b_hi(d0), b_lo(d1), b_hi(d1)};
          x1[cc] = xv + ga[cc] * (rre * vre - rim * vim);
          tie_v(x1[cc]); tie_v(ga[cc]); tie_v(cbv[cc]);
        }
        SCHED();
      });
    });
    STAMP(5); FS(35);
    m16_fence(y);
    u32 pk[4][2];
    sfor<4>([&](auto c) {
      x1[c] += ga[c] * y[c];
      f32x4 s = x1[c] * cbv[c];
      pk[c][0] = pack2(s[0], s[1]); pk[c][1] = pack2(s[2], s[3]);
    });
    sfor<2>([&](auto p) {
      pub16<4, p * 2048>(chunk_swap16(pk[2 * p][0], pk[2 * p][1], pk[2 * p + 1][0], pk[2 * p + 1][1]), rW,
                         (qq & 1) * 1024 + (qq >> 1) * 512 + l15 * 16, WS_X1 + ((tb * 2 + wk) * 32 + nt * 4) * 1024 + w1 * 256);
    });
    const f32x4 s4 = (x1[0] + x1[1]) + (x1[2] + x1[3]);
    const float mean = add_x32(add_x16((s4[0] + s4[1]) + (s4[2] + s4[3]))) * (1.f / 64);
    f32x4 q4 = {0.f, 0.f, 0.f, 0.f};
    sfor<4>([&](auto c) { f32x4 d = x1[c] - mean; q4 += d * d; });
    const float m2 = add_x32(add_x16((q4[0] + q4[1]) + (q4[2] + q4[3])));
    u32x2 o = {__float_as_uint(mean), __float_as_uint(m2)};
    pub8<4>(o, rW, l15 * 8, WS_ST + ((tb * 2 + wk) * 8 + nt) * 256 + w1 * 128);
    bar();
  }
  FS(36);
  const rsrc_t rG = mk_rsrc(w < 2 ? Wg : Wu, D * F * 2);
  const rsrc_t rN = mk_rsrc(Wn, F * D * 2);
  u32 vG[4], vN[4];
  sfor<4>([&](auto i) {
    vG[i] = (lane >> 3) * (F * 2) + (((lane & 7) ^ ((qq & 1) << 2)) << 4) + i * (8 * F * 2 - 1024);
    vN[i] = (lane >> 3) * 1024 + (((lane & 7) ^ (((qq & 1) + 2 * (i & 1)) << 1)) << 4) + i * (8 * 1024 - 1024);
  });
  const u32 sG = w1 * 32 * (F * 2) + ft * 256, sN = (wk * 128 + w1 * 32) * 1024 + nt * 128;
  auto g_dma = [&](auto jc, auto ic_) {
    constexpr int j = jc, i = ic_;
    dma<200 + j, i * 1024>(vG[i], rG, sG + ((j + ROT4) & 7) * (64 * F * 2) + (j >> 3) * 128);
  };
  auto rot7 = [&](int j) { int pr = j + ROT5; pr = pr >= 7 ? pr - 7 : pr; return pr >= 7 ? pr - 7 : pr; };
  auto n_dma = [&](auto jc, auto ic_) {
    constexpr int j = jc, i = ic_;
    dma<300 + j, i * 1024>(vN[i], rN, sN + rot7(j % 7) * (256 * 1024) + (j / 7) * 65536);
  };
  const bool act4 = loc < 28;
  u32x4 ug[4];
  const u32 vUV = ((tid >> 7) * F + ft * 128 + (tid & 127)) * 16;
  if (act4) {
    sfor<3>([&](auto j) { set_m0n<j * 16384>(wl); sfor<4>([&](auto i) { g_dma(j, i); }); });
  } else {
    sfor<3>([&](auto j) { set_m0n<j * 16384>(wl); sfor<4>([&](auto i) { n_dma(j, i); }); });
  }
  bar_vm<12, 4>();
  if (tid == 0) post_flag(ws + WS_F3 + (tb * 8 + nt) * 128, tag);
  FS(100);
  if (w == 2) ld_v1p<0>(cpf, lane * 128, rP, cof4 + (loc & 3) * 8192);
  if (!act4) sfor<5>([&](auto j) { set_m0n<(j + 3) * 16384>(wl); sfor<4>([&](auto i) { n_dma(ic<j + 3>{}, i); }); });
  STAMP(6); FS(37);

  const u32 vF4 = (lane < 14 ? lane : 0) * 128;
    u32x4 aa[56];
    const u32 sAa = WS_ACT + (tb * 4 + w) * 56 * 1024;
    const u32 vA2 = lane16 + 4096;
    auto aa_ld = [&](auto sc_) {
      constexpr int s = sc_;
      ld_a<(s & 1) * 1024 + (s / 28) * 2048>(aa[s], (s & 2) ? vA2 : lane16, rW, sAa + rot7((s >> 2) % 7) * 8192);
    };
  auto fl_wait = [&](u32 so) {
    tie_v(fl[0]);
    for (u32 sp = 0; sp < SPIN_CAP; ++sp) {
      if (__all(fl[0][0] == tag32 && fl[0][1] == (u32)(tag >> 32))) break;
      ld_v2<0>(fl[0], vF4, rW, so);
      wait_vm<0>();
      tie_v(fl[0]);
    }
  };

  if (act4) {
    u32 tB[2][2];
    {
      const u32 q = l15 >> 2, pp = lane & 3;
      sfor<2>([&](auto c) { sfor<2>([&](auto hf) {
        tB[c][hf] = (8 * hh + q) * 128 + ((4 * (c ^ (q >> 1)) + 2 * (qq & 1) + (pp >> 1)) << 4) + 8 * (pp & 1) + hf * 65536; tie_v(tB[c][hf]); }); });
    }
    sfor<4>([&](auto q) { ld_v4<0>(ug[q], vUV, rW, WS_UVG + q * 3584 * 16); });
    sfor<NG0 - 3>([&](auto j) { set_m0n<(j + 3) * 16384>(wl); sfor<4>([&](auto i) { g_dma(ic<j + 3>{}, i); }); });
    {
          wait_vm<4 * (NG0 - 3)>();
          sfor<4>([&](auto q) { tie_v(ug[q]); });
          for (u32 sp = 0; sp < SPIN_CAP; ++sp) {
            bool ok = true;
            sfor<4>([&](auto q) { ok = ok && ug[q][1] == tag32 && ug[q][3] == tag32; });
            if (__all(ok)) break;
            __builtin_amdgcn_s_sleep(1);
            sfor<4>([&](auto q) { ld_v4<0>(ug[q], vUV, rW, WS_UVG + q * 3584 * 16); });
            wait_vm<0>();
            sfor<4>([&](auto q) { tie_v(ug[q]); });
          }
          u32 d[2];
          sfor<2>([&](auto e) { u32 a = ug[0][2 * e], b = ug[1][2 * e], c = ug[2][2 * e], dd = ug[3][2 * e];
            const float s = (__uint_as_float(a) + __uint_as_float(b)) + (__uint_as_float(c) + __uint_as_float(dd));
            const float hi = b_lo(pack2(s, 0.f));
            d[e] = pack2(hi, s - hi); });
          u32x4 rec = {d[0], d[0], d[1], d[1]};
          *(LAS u32x4*)(sm + lM + (L_UV - L_MODV) + tid * 16) = rec;
        }
    FS(101);
    if (w == 0) { if (PRE_WAIT) wait_vm<0>(); pl.run(lane * 128, rW, WS_F3 + rb * 2048, tag, 0xffffull); wait_vm<3, 200 + NG0 - 1>(); } else { wait_vm<0, 200 + NG0 - 1>(); }
    bar();
    asm volatile(".Lmk_4:");
    STAMP(7); FS(38);
    u32x4 xa[32];
    const u32 so = WS_X1 + (rb * 4 + w) * 32 * 1024;
    auto xa_ld = [&](auto sc_) { constexpr int s = sc_; ld_a<(s & 3) * 1024>(xa[s], lane16, rW, so + (((s >> 2) + ROT4) & 7) * 4096); };
    sfor<P4_PRE>([&](auto s) { xa_ld(s); });
    u32x2 stv[4];
    sfor<4>([&](auto i) { ld_v2<i * 256>(stv[i], hh * 1024 + l31 * 8, rW, WS_ST + (rb * 4 + w) * 2048); });
    f32x16 z[2][4];
    u32x4 wf[2][4], uf[4];
    auto w_rd = [&](auto tc, auto jc) {
      constexpr int t = tc, j = jc;
      wf[t & 1][j] = tr_asm<((t >> 2) & 3) * 16384 + (j >> 1) * 8192 + (t & 3) * 2048>(tB[j & 1][(t >> 4) & 1]);
    };
    FS(103);
    sfor<4>([&](auto j) { w_rd(ic<0>{}, j); });
    float rr, sd;
    u32x4 xst;
    auto u_rd = [&](int p, int j) { uf[j] = lds16(sm, lM + (L_UV - L_MODV) + ((j >> 1) * 128 + p * 64 + (j & 1) * 32 + l31) * 16); };
    constexpr float C1 = -2.f * 0.7978845608028654f * 1.4426950408889634f, C3 = C1 * 0.044715f;
    float c1v = C1, c3v = C3;
    tie_v(c1v); tie_v(c3v);
    const u32 sAct = WS_ACT + ((rb * 8 + 2 * w) * 56 + ft * 4) * 1024;
    const u32 vAct = (l31 >> 4) * (56 * 1024) + hh * 256 + l15 * 16;
    u32 pk[4];
    Ge ge;
    sfor<64>([&](auto tc) {
      constexpr int t = tc, j = t >> 2, s4 = t & 3, p = j >> 3;
      constexpr bool many = t < 4 * (8 - NG0) + 4;
      SCHED();
      if constexpr (p == 0) wait_a<S4.wait[t] - 1 - S4.posx[t]>(xa[t]);
      if constexpr (t == 0) pl.done();
      if constexpr (t < 4) FS(104 + t);
      sfor<4>([&](auto mc) {
        constexpr int m = mc;
        if constexpr (m == 0 && t < 4 * (8 - NG0)) set_m0<(NG0 + (t >> 2)) * 16384>(wl);
        if constexpr (m == 3 && t >= 3 && (many || s4 == 3)) set_m0<(((t - 3) >> 2) & 7) * 16384>(wl);
        if constexpr (m == 0) wait_l<4>();
        if constexpr (m == 2) wait_l<(t < 63 ? 4 : 2)>();
        mm_va<(t & 31) == 0>(z[p][m], wf[t & 1][m], xa[t & 31]);
        if constexpr (t == 33) mm_vv<false>(z[0][m], uf[m], xst);
        SCHED();
        if constexpr (m == 0 && s4 != 3) { w_rd(ic<t + 1>{}, ic<0>{}); w_rd(ic<t + 1>{}, ic<1>{}); }
        if constexpr (m == 2) { if constexpr (t < 63) { w_rd(ic<t + 1>{}, ic<2>{}); w_rd(ic<t + 1>{}, ic<3>{}); } else { u_rd(1, 2); u_rd(1, 3); } }
        if constexpr (t == 32 && m == 3) sfor<4>([&](auto q) { u_rd(0, q); });
        if constexpr (m == 1 && t == P4_ST) {
          wait_vm<S4.wait[t] - 1 - S4.posst + (t < 4 * (8 - NG0) ? 1 : 0)>();
          sfor<4>([&](auto i) { tie_v(stv[i]); });
          float ms = 0.f;
          sfor<4>([&](auto i) { u32 a = stv[i][0]; ms += __uint_as_float(a); });
          const float mean = add_x32(ms) * (1.f / 8);
          float q = 0.f;
          sfor<4>([&](auto i) { u32 a = stv[i][0], b = stv[i][1]; float d = __uint_as_float(a) - mean; q += __uint_as_float(b) + 64.f * d * d; });
          const float var = add_x32(q) * (1.f / D) + 1e-5f;
          rr = __builtin_amdgcn_rsqf(var);
          sd = var * rr;
          const float mh = b_lo(pack2(mean, 0.f)), sh = b_lo(pack2(sd, 0.f));
          xst[0] = hh ? 0u : pack2(-mh, -mh); xst[1] = hh ? 0u : pack2(mh - mean, mh - mean);
          xst[2] = hh ? 0u : pack2(sh, sh); xst[3] = hh ? 0u : pack2(sd - sh, sd - sh);
          tie_v(xst); tie_v(rr); tie_v(sd);
        }
        if constexpr (m == 0 && t < 4 * (8 - NG0)) g_dma(ic<NG0 + (t >> 2)>{}, ic<(t & 3)>{});
        if constexpr (m == 1 && s4 == 3) {
          constexpr int need = j + 1 >= NG0 && j < 15 ? S4.bar[j] - 1 - S4.posg[j + 1] : 63;
          constexpr int nsto = j == 13 ? S4.bar[j] - 1 - S4.possto : 63;
          bar_vm<(need < nsto ? need : nsto), (j < 15 ? 201 + j : 0), (j == 13 ? 5 : 0)>();
          if constexpr (j == 13) { if (tid == 0) post_flag(ws + WS_F4A + (rb * 16 + ft) * 128, tag); }
          FS(40 + j);
          if constexpr (t < 63) { w_rd(ic<t + 1>{}, ic<0>{}); w_rd(ic<t + 1>{}, ic<1>{}); } else { u_rd(1, 0); u_rd(1, 1); }
        }
        sfor<p4_u0(t, m + 1) - p4_u0(t, m)>([&](auto uc) {
          constexpr int u = p4_u0(t, m) + uc, e = u / 12;
          ge_unit<u % 12>(ge, z[0][e >> 3][2 * (e & 7)], z[0][e >> 3][2 * (e & 7) + 1], z[0][2 + (e >> 3)][2 * (e & 7)],
                          z[0][2 + (e >> 3)][2 * (e & 7) + 1], rr, sd, c3v, c1v, pk[e & 3]);
          if constexpr (u % 48 == 47) asm volatile("s_nop 1" : "+v"(pk[3]));
          if constexpr (u % 48 == 47) pub16<5, (e >> 3) * 1024 + ((e >> 2) & 1) * 512>(chunk_swap(pk[0], pk[1], pk[2], pk[3]), rW, vAct, sAct);
        });
        if constexpr (m == 1) {
          if constexpr (t + P4_PRE < 32) xa_ld(ic<t + P4_PRE>{});
        }
        if constexpr (m == 3 && t >= 3) {
          constexpr int gj = (t - 3) >> 2, gi = (t - 3) & 3;
          if constexpr (gj < 8) g_dma(ic<8 + gj>{}, ic<gi>{}); else n_dma(ic<gj - 8>{}, ic<gi>{});
        }
        SCHED();
      });
    });
    sfor<4>([&](auto m) { mm_vv<false>(z[1][m], uf[m], xst); if constexpr (m < 3) n_dma(ic<7>{}, ic<1 + m>{}); });
    STAMP(8); FS(56);
    mm_fence(z[1][0], z[1][2]); mm_tie(z[1][1], z[1][3]);
    sfor<4>([&](auto qc) {
      constexpr int c = qc >> 1, g0 = (qc & 1) * 2;
      u32 pq[4];
      sfor<2>([&](auto gg) {
        constexpr int g = g0 + gg;
        f32x4 zg = {z[1][c][4 * g], z[1][c][4 * g + 1], z[1][c][4 * g + 2], z[1][c][4 * g + 3]};
        f32x4 zu = {z[1][2 + c][4 * g], z[1][2 + c][4 * g + 1], z[1][2 + c][4 * g + 2], z[1][2 + c][4 * g + 3]};
        f32x4 gt = rr * zg;
        f32x4 ex = gt * (C1 + C3 * gt * gt);
        sfor<4>([&](auto i) { ex[+i] = __builtin_amdgcn_exp2f(ex[+i]); });
        ex = ex * sd + sd;
        sfor<4>([&](auto i) { ex[+i] = __builtin_amdgcn_rcpf(ex[+i]); });
        f32x4 a = gt * zu * ex;
        pq[2 * gg] = pack2(a[0], a[1]); pq[2 * gg + 1] = pack2(a[2], a[3]);
      });
      pub16<6, 2048 + c * 1024 + (qc & 1) * 512>(chunk_swap(pq[0], pq[1], pq[2], pq[3]), rW, vAct, sAct);
      if constexpr (qc == FA_AT) ld_v2<0>(fl[0], vF4, rW, WS_F4A + (tb >> 1) * 2048);
      if constexpr (qc == FA_AT + 1) {
        wait_vm<1>();
        fl_wait(WS_F4A + (tb >> 1) * 2048);
        FS(102);
        sfor<P5_PRE>([&](auto s) { aa_ld(s); });
      }
      SCHED();
    });
  } else {
    fl[0] = u32x2{0u, 0u};
    fl_wait(WS_F4A + (tb >> 1) * 2048);
    sfor<P5_PRE>([&](auto s) { aa_ld(s); });
  }
  STAMP(9); FS(57);

  {
    asm volatile(".Lmk_5:");
    bar_vm<0, 6, 307>();
    if (act4 && tid == 0) post_flag(ws + WS_F4B + (rb * 16 + ft) * 128, tag);
    STAMP(10); FS(58);
    u32 tN[4][2];
    {
      const u32 q = l15 >> 2, pp = lane & 3, f = (q >> 1) + 2 * (qq & 1);
      sfor<4>([&](auto c) { sfor<2>([&](auto hf) {
        tN[c][hf] = (8 * qq + q) * 128 + ((2 * (c ^ f) + (pp >> 1)) << 4) + 8 * (pp & 1) + hf * 65536; tie_v(tN[c][hf]); }); });
    }
    f32x4 y[4];
    u32x4 nf[2][4];
    auto n_rd = [&](int t, int c) {
      const u32 o = tN[c][(t >> 4) & 1] + ((t >> 2) & 3) * 16384 + (t & 3) * 4096;
      nf[t & 1][c] = lds_tr(sm, o, o + 512);
    };
    sfor<4>([&](auto c) { n_rd(0, c); });
    tie_v(cpf);
    sfor<56>([&](auto tc) {
      constexpr int t = tc, i = t >> 2, s4 = t & 3;
      SCHED();
      if constexpr (t == 28 - P5_PRE) {
        wait_vm<S5.wait[t] - 1 - S5.posfb>();
        fl_wait(WS_F4B + (tb >> 1) * 2048);
        STAMP(11); FS(59);
      }
      if constexpr (t >= P5_PRE) wait_a<S5.wait[t] - 1 - S5.posa[t]>(aa[t]);
      sfor<4>([&](auto mc) {
        constexpr int m = mc;
        if constexpr (m == 2 && s4 == 3 && t < 27) set_m0<(i & 7) * 16384>(wl);
        m16_va<t == 0>(y[m], nf[t & 1][m], aa[t]);
        SCHED();
        if constexpr (s4 != 3) n_rd(t + 1, m);
        if constexpr (m == 1 && s4 == 3 && i < 13) { bar_vm<(i >= 7 ? S5.bar[i] - 1 - S5.posn[i + 1] : 63), (i >= 7 ? 301 + i : 0)>(); FS(60 + i); n_rd(t + 1, 0); n_rd(t + 1, 1); }
        if constexpr (m >= 2 && s4 == 3 && t < 55) n_rd(t + 1, m);
        if constexpr (m == 1 && t + P5_PRE < 56) aa_ld(ic<t + P5_PRE>{});
        if constexpr (m == 2 && t == P5_FB) ld_v2<0>(fl[0], vF4, rW, WS_F4B + (tb >> 1) * 2048);
        if constexpr (m == 3 && t >= 3 && t < 27) n_dma(ic<8 + ((t - 3) >> 2)>{}, ic<((t - 3) & 3)>{});
        SCHED();
      });
    });
    STAMP(12); FS(73);
    f32x4 gb[4];
    sfor<4>([&](auto c) { gb[c] = ldsf4(sm, lM + 4096 + (c * 16 + qq * 4) * 4); });
    m16_fence(y);
    u32 pk[4][2];
    sfor<4>([&](auto c) {
      f32x4 m = x1[c] + gb[c] * y[c];
      pk[c][0] = pack2(m[0], m[1]); pk[c][1] = pack2(m[2], m[3]);
    });
    const rsrc_t rO = mk_rsrc(out, S * D * 2);
    sfor<2>([&](auto p) {
      __builtin_amdgcn_raw_buffer_store_b128(chunk_swap16(pk[2 * p][0], pk[2 * p][1], pk[2 * p + 1][0], pk[2 * p + 1][1]), rO,
                                             l15 * 1024 + (qq & 1) * 32 + (qq >> 1) * 16 + p * 64, (tb * 64 + w * 16) * 1024 + nt * 128, 0);
    });
  }
  STAMP(13); FS(74);
  asm volatile(".Lmk_z:");
}

extern "C" void kernel_launch(void* const* d_in, const int* in_sizes, int n_in, void* d_out, int out_size, void* d_ws,
                              size_t ws_size, hipStream_t stream) {
  (void)in_sizes; (void)n_in; (void)out_size; (void)ws_size;
  (void)hipFuncSetAttribute((const void*)mk_block, hipFuncAttributeMaxDynamicSharedMemorySize, LDS_BYTES);
  mk_block<<<256, 256, LDS_BYTES, stream>>>((const u16*)d_in[0], (const u16*)d_in[1], (const u16*)d_in[2], (const u16*)d_in[3],
                                            (const u16*)d_in[4], (const u16*)d_in[6], (const u16*)d_in[7], (const u16*)d_in[8],
                                            (const u16*)d_in[9], (const u16*)d_in[10], (u16*)d_out, (char*)d_ws);
}
```

```cpp
#include <hip/hip_runtime.h>
#include <utility>
#pragma clang diagnostic ignored "-Winline-asm"

typedef unsigned short u16;
typedef unsigned u32;
typedef unsigned long long u64;
typedef float f32x2 __attribute__((ext_vector_type(2)));
typedef float f32x4 __attribute__((ext_vector_type(4)));
typedef float f32x16 __attribute__((ext_vector_type(16)));
typedef unsigned u32x2 __attribute__((ext_vector_type(2)));
typedef unsigned u32x4 __attribute__((ext_vector_type(4)));
typedef short s16x4 __attribute__((ext_vector_type(4)));
typedef short s16x8 __attribute__((ext_vector_type(8)));
typedef __bf16 bf16x2 __attribute__((ext_vector_type(2)));
typedef __bf16 bf16x8 __attribute__((ext_vector_type(8)));
typedef __amdgpu_buffer_rsrc_t rsrc_t;
#define LAS __attribute__((address_space(3)))
#define GAS __attribute__((address_space(1)))
#define DI __device__ __forceinline__
#define RLX_AGENT __ATOMIC_RELAXED, __HIP_MEMORY_SCOPE_AGENT

constexpr int S = 2048, D = 512, F = 1792;
constexpr u32 WS_XN = 0;
constexpr u32 WS_PQ = WS_XN + (2u << 20);
constexpr u32 WS_X1 = WS_PQ + (8u << 20);
constexpr u32 WS_ACT = WS_X1 + (2u << 20);
constexpr u32 WS_RS = WS_ACT + (7u << 20);
constexpr u32 WS_ST = WS_RS + (256u << 10);
constexpr u32 WS_MODP = WS_ST + (256u << 10);
constexpr u32 WS_VG = WS_MODP + 4 * 3072 * 4;
constexpr u32 WS_UVG = WS_VG + 1024 * 8;
constexpr u32 WS_F1 = WS_UVG + 4 * 2 * 3584 * 8;
#define NREP 8
constexpr u32 FG = NREP * 128;
constexpr u32 WS_F2 = WS_F1 + 8 * 256 * 8;
constexpr u32 WS_F3 = WS_F2 + 32 * FG;
constexpr u32 WS_F4A = WS_F3 + 16 * FG;
constexpr u32 WS_F4B = WS_F4A + 16 * FG;
constexpr u32 WS_FM = WS_F4B + 16 * FG;
constexpr u32 WS_GEN = WS_FM + 256 * 128;
constexpr u32 WS_TS = WS_GEN + 256;
constexpr u32 WS_TS2 = WS_TS + 256 * 16 * 8;
constexpr u32 WS_END = WS_TS2 + 256 * 256 * 4;
constexpr u32 L_EX = 128 * 1024;
constexpr u32 L_MODV = 144 * 1024;
constexpr u32 L_UV = L_MODV + 5120;
constexpr u32 L_UVP = L_UV + 4096;
constexpr u32 L_MP = L_UVP + 2048;
constexpr u32 L_RS = L_MP + 1024;
constexpr u32 L_CNT = L_RS + 2048;
constexpr u32 LDS_BYTES = 160 * 1024;
constexpr u32 SPIN_CAP = 200000;
#define POLL_ATOMIC 0
#define POLL_SLEEP 2

template <class Fn, int... Is> DI void sfor_impl(Fn&& f, std::integer_sequence<int, Is...>) { (f(std::integral_constant<int, Is>{}), ...); }
template <int V> using ic = std::integral_constant<int, V>;
template <int N, class Fn> DI void sfor(Fn&& f) { sfor_impl(f, std::make_integer_sequence<int, N>{}); }

DI rsrc_t mk_rsrc(const void* p, u32 bytes) { return __builtin_amdgcn_make_buffer_rsrc((void*)p, 0, bytes, 0x00020000); }
DI const void* argp(u32 i) {
  u64 p;
  asm volatile("s_load_dwordx2 %0, %1, %2\n\ts_waitcnt lgkmcnt(0)" : "=s"(p) : "s"(__builtin_amdgcn_kernarg_segment_ptr()), "s"(i * 8));
  return (const void*)p;
}
DI void argp4(u64 (&p)[4], u32 a, u32 b, u32 c, u32 d) {
  asm volatile("s_load_dwordx2 %0, %4, %5\n\ts_load_dwordx2 %1, %4, %6\n\ts_load_dwordx2 %2, %4, %7\n\ts_load_dwordx2 %3, %4, %8"
               : "=&s"(p[0]), "=&s"(p[1]), "=&s"(p[2]), "=&s"(p[3]) : "s"(__builtin_amdgcn_kernarg_segment_ptr()), "s"(a * 8), "s"(b * 8), "s"(c * 8), "s"(d * 8));
}
DI void argp4_land(u64 (&p)[4]) { asm volatile("" : "+s"(p[0]), "+s"(p[1]), "+s"(p[2]), "+s"(p[3])); }
DI float b_lo(u32 v) { return __uint_as_float(v << 16); }
DI float b_hi(u32 v) { return __uint_as_float(v & 0xffff0000u); }
DI u32 pack2(float a, float b) { f32x2 p = {a, b}; bf16x2 r = __builtin_convertvector(p, bf16x2); return __builtin_bit_cast(u32, r); }

template <int IMM> DI void ld_a(u32x4& d, u32 voff, rsrc_t r, u32 soff) {
  asm volatile("buffer_load_dwordx4 %0, %1, %2, %3 offen offset:%c4 sc1" : "=a"(d) : "v"(voff), "s"(r), "s"(soff), "i"(IMM));
}
template <int IMM> DI void ld_v4(u32x4& d, u32 voff, rsrc_t r, u32 soff) {
  asm volatile("buffer_load_dwordx4 %0, %1, %2, %3 offen offset:%c4 sc1" : "=v"(d) : "v"(voff), "s"(r), "s"(soff), "i"(IMM));
}
template <int IMM> DI void ld_v4p(u32x4& d, u32 voff, rsrc_t r, u32 soff) {
  asm volatile("buffer_load_dwordx4 %0, %1, %2, %3 offen offset:%c4" : "=v"(d) : "v"(voff), "s"(r), "s"(soff), "i"(IMM));
}
template <int IMM> DI void ld_v2(u32x2& d, u32 voff, rsrc_t r, u32 soff) {
  asm volatile("buffer_load_dwordx2 %0, %1, %2, %3 offen offset:%c4 sc1" : "=v"(d) : "v"(voff), "s"(r), "s"(soff), "i"(IMM));
}
DI void ld_v2t(u32x2& d, u32 voff, rsrc_t r, u32 soff) {
  asm volatile("buffer_load_dwordx2 %0, %1, %2, %3 offen sc1" : "+v"(d) : "v"(voff), "s"(r), "s"(soff));
}
DI void ld_v4t(u32x4& d, u32 voff, rsrc_t r, u32 soff) {
  asm volatile("buffer_load_dwordx4 %0, %1, %2, %3 offen" : "+v"(d) : "v"(voff), "s"(r), "s"(soff));
}
template <int IMM> DI void ld_v2p(u32x2& d, u32 voff, rsrc_t r, u32 soff) {
  asm volatile("buffer_load_dwordx2 %0, %1, %2, %3 offen offset:%c4" : "=v"(d) : "v"(voff), "s"(r), "s"(soff), "i"(IMM));
}
template <int IMM> DI void ld_v1(u32& d, u32 voff, rsrc_t r, u32 soff) {
  asm volatile("buffer_load_dword %0, %1, %2, %3 offen offset:%c4 sc1" : "=v"(d) : "v"(voff), "s"(r), "s"(soff), "i"(IMM));
}
template <int IMM> DI void ld_v1p(u32& d, u32 voff, rsrc_t r, u32 soff) {
  asm volatile("buffer_load_dword %0, %1, %2, %3 offen offset:%c4" : "+v"(d) : "v"(voff), "s"(r), "s"(soff), "i"(IMM));
}
template <int IMM> DI void ld_u16p(u32& d, u32 voff, rsrc_t r, u32 soff) {
  asm volatile("buffer_load_ushort %0, %1, %2, %3 offen offset:%c4" : "=v"(d) : "v"(voff), "s"(r), "s"(soff), "i"(IMM));
}
template <int IMM> DI void set_m0(u32 base) { asm volatile("s_add_u32 m0, %0, %c1" :: "s"(base), "i"(IMM) : "m0", "scc"); }
template <int IMM> DI void set_m0n(u32 base) { asm volatile("s_add_u32 m0, %0, %c1\n\ts_nop 0" :: "s"(base), "i"(IMM) : "m0", "scc"); }
template <int ID, int OFF> DI void dma(u32 voff, rsrc_t r, u32 soff) {
  asm volatile("buffer_load_dwordx4 %0, %1, %2 offen offset:%c3 lds ; ID %c4" :: "v"(voff), "s"(r), "s"(soff), "i"(OFF), "i"(ID));
}
template <int ID, int OFF = 0> DI void pub16(u32x4 v, rsrc_t r, u32 voff, u32 soff) {
  asm volatile("buffer_store_dwordx4 %0, %1, %2, %3 offen offset:%c5 sc1 ; ID %c4\n\ts_nop 1" :: "v"(v), "v"(voff), "s"(r), "s"(soff), "i"(ID), "i"(OFF));
}
template <int ID> DI void pub8(u32x2 v, rsrc_t r, u32 voff, u32 soff) {
  asm volatile("buffer_store_dwordx2 %0, %1, %2, %3 offen sc1 ; ID %c4" :: "v"(v), "v"(voff), "s"(r), "s"(soff), "i"(ID));
}
template <int ID> DI void pub4(u32 v, rsrc_t r, u32 voff, u32 soff) {
  asm volatile("buffer_store_dword %0, %1, %2, %3 offen sc1 ; ID %c4" :: "v"(v), "v"(voff), "s"(r), "s"(soff), "i"(ID));
}
template <int N, int A = 0> DI void wait_vm() { static_assert(N >= 0 && N < 64, ""); asm volatile("s_waitcnt vmcnt(%c0) ; NEED %c1" :: "i"(N), "i"(A) : "memory"); }
template <int N> DI void wait_a(u32x4&) { static_assert(N >= 0, ""); asm volatile("s_waitcnt vmcnt(%c0)" :: "i"(N < 63 ? N : 63)); }
DI void tie_a(u32x4& a) { asm volatile("" : "+a"(a)); }
template <class T> DI void tie_v(T& a) { asm volatile("" : "+v"(a)); }
template <int N, int A = 0, int B = 0> DI void bar_vm() {
  static_assert(N >= 0, "");
  asm volatile("s_waitcnt vmcnt(%c0) lgkmcnt(0) ; NEED %c1 NEED %c2\n\ts_barrier" :: "i"(N < 63 ? N : 63), "i"(A), "i"(B) : "memory");
}
DI void bar() { asm volatile("s_waitcnt lgkmcnt(0)\n\ts_barrier" ::: "memory"); }
#define SCHED() __builtin_amdgcn_sched_barrier(0)

DI void st16(u32x4 v, rsrc_t r, u32 voff, u32 soff) { __builtin_amdgcn_raw_buffer_store_b128(v, r, voff, soff, 16); }
DI void st8(u32x2 v, rsrc_t r, u32 voff, u32 soff) { __builtin_amdgcn_raw_buffer_store_b64(v, r, voff, soff, 16); }
DI void st4(u32 v, rsrc_t r, u32 voff, u32 soff) { __builtin_amdgcn_raw_buffer_store_b32(v, r, voff, soff, 16); }

DI f32x16 mfma(u32x4 a, u32x4 b, f32x16 c) {
  return __builtin_amdgcn_mfma_f32_32x32x16_bf16(__builtin_bit_cast(bf16x8, a), __builtin_bit_cast(bf16x8, b), c, 0, 0, 0);
}
template <bool Z> DI void mm_va(f32x16& c, u32x4 a, u32x4 b) {
  if constexpr (Z) asm volatile("v_mfma_f32_32x32x16_bf16 %0, %1, %2, 0" : "=&v"(c) : "v"(a), "a"(b));
  else asm volatile("v_mfma_f32_32x32x16_bf16 %0, %1, %2, %0" : "+v"(c) : "v"(a), "a"(b));
}
template <bool Z> DI void mm_av(f32x16& c, u32x4 a, u32x4 b) {
  if constexpr (Z) asm volatile("v_mfma_f32_32x32x16_bf16 %0, %1, %2, 0" : "=&v"(c) : "a"(a), "v"(b));
  else asm volatile("v_mfma_f32_32x32x16_bf16 %0, %1, %2, %0" : "+v"(c) : "a"(a), "v"(b));
}
template <bool Z> DI void mm_vv(f32x16& c, u32x4 a, u32x4 b) {
  if constexpr (Z) asm volatile("v_mfma_f32_32x32x16_bf16 %0, %1, %2, 0" : "=&v"(c) : "v"(a), "v"(b));
  else asm volatile("v_mfma_f32_32x32x16_bf16 %0, %1, %2, %0" : "+v"(c) : "v"(a), "v"(b));
}
template <bool Z> DI void m16_va(f32x4& c, u32x4 a, u32x4 b) {
  if constexpr (Z) asm volatile("v_mfma_f32_16x16x32_bf16 %0, %1, %2, 0" : "=&v"(c) : "v"(a), "a"(b));
  else asm volatile("v_mfma_f32_16x16x32_bf16 %0, %1, %2, %0" : "+v"(c) : "v"(a), "a"(b));
}
DI void m16_fence(f32x4 (&y)[4]) { asm volatile("s_nop 11" : "+v"(y[0]), "+v"(y[1]), "+v"(y[2]), "+v"(y[3])); }
DI void m16_tie(f32x4 (&y)[4]) { asm volatile("" : "+v"(y[0]), "+v"(y[1]), "+v"(y[2]), "+v"(y[3])); }
DI void mm_fence(f32x16& a, f32x16& b) { asm volatile("s_nop 15" : "+v"(a), "+v"(b)); }
DI void mm_tie(f32x16& a, f32x16& b) { asm volatile("" : "+v"(a), "+v"(b)); }
DI float smul(float a, float b) { float r; asm("v_mul_f32_e32 %0, %1, %2" : "=v"(r) : "v"(a), "v"(b)); return r; }
DI float sfma(float a, float b, float c) { float r; asm("v_fma_f32 %0, %1, %2, %3" : "=v"(r) : "v"(a), "v"(b), "v"(c)); return r; }
DI u32x4 lds16(LAS char* sm, u32 off) { return *(LAS u32x4*)(sm + off); }
DI f32x4 ldsf4(LAS char* sm, u32 off) { return *(LAS f32x4*)(sm + off); }
DI u32x4 lds_tr(LAS char* sm, u32 off0, u32 off1) {
  s16x4 lo = __builtin_amdgcn_ds_read_tr16_b64_v4i16((LAS s16x4*)(sm + off0));
  s16x4 hi = __builtin_amdgcn_ds_read_tr16_b64_v4i16((LAS s16x4*)(sm + off1));
  s16x8 w = __builtin_shufflevector(lo, hi, 0, 1, 2, 3, 4, 5, 6, 7);
  return __builtin_bit_cast(u32x4, w);
}
template <int OFF> DI u32x4 tr_asm(u32 a) {
  u32x2 lo, hi;
  asm volatile("ds_read_b64_tr_b16 %0, %2 offset:%c3\n\tds_read_b64_tr_b16 %1, %2 offset:%c4" : "=&v"(lo), "=&v"(hi) : "v"(a), "i"(OFF), "i"(OFF + 512));
  return __builtin_shufflevector(lo, hi, 0, 1, 2, 3);
}
template <int OFF> DI u32x4 rd_asm(u32 a) { u32x4 r; asm volatile("ds_read_b128 %0, %1 offset:%c2" : "=v"(r) : "v"(a), "i"(OFF)); return r; }
template <int N> DI void wait_l() { asm volatile("s_waitcnt lgkmcnt(%c0)" :: "i"(N)); }
DI void lds_spin(LAS char* sm, u32 off, u32 want) {
  for (u32 sp = 0; sp < SPIN_CAP; ++sp)
    if (__builtin_amdgcn_readfirstlane(*(volatile LAS u32*)(sm + off)) == want) break;
  asm volatile("" ::: "memory");
}
template <int CTRL> DI float dpp(float v) { return __int_as_float(__builtin_amdgcn_update_dpp(0, __float_as_int(v), CTRL, 0xF, 0xF, true)); }
DI float add_x16(float v) { auto r = __builtin_amdgcn_permlane16_swap(__float_as_uint(v), __float_as_uint(v), false, false); u32 a = r[0], b = r[1]; return __uint_as_float(a) + __uint_as_float(b); }
DI float add_x32(float v) { auto r = __builtin_amdgcn_permlane32_swap(__float_as_uint(v), __float_as_uint(v), false, false); u32 a = r[0], b = r[1]; return __uint_as_float(a) + __uint_as_float(b); }
DI float red_x32(float a, float b) { auto r = __builtin_amdgcn_permlane32_swap(__float_as_uint(a), __float_as_uint(b), false, false); u32 p = r[0], q = r[1]; return __uint_as_float(p) + __uint_as_float(q); }
DI float red_x16(float a, float b) { auto r = __builtin_amdgcn_permlane16_swap(__float_as_uint(a), __float_as_uint(b), false, false); u32 p = r[0], q = r[1]; return __uint_as_float(p) + __uint_as_float(q); }
DI float red_x8(float a, float b, bool hi) { float keep = hi ? b : a, give = hi ? a : b; return keep + dpp<0x128>(give); }
DI float wave_sum(float v) {
  v += dpp<0xB1>(v); v += dpp<0x4E>(v); v += dpp<0x141>(v); v += dpp<0x140>(v);
  return add_x32(add_x16(v));
}
DI float wave_sum4(float v0, float v1, float v2, float v3) {
  float v = red_x16(red_x32(v0, v1), red_x32(v2, v3));
  v += dpp<0xB1>(v); v += dpp<0x4E>(v); v += dpp<0x141>(v); v += dpp<0x140>(v);
  return v;
}
DI float lane_of(float v, int row) { return __int_as_float(__builtin_amdgcn_readlane(__float_as_int(v), (row & 1) * 32 + (row >> 1) * 16)); }
template <bool NEG> DI u32 scale_pack(float a, float b, float sa, float sb) {
  u32 o; float t0, t1;
  if constexpr (NEG) asm volatile("v_mul_f32 %1, -%3, %5\n\tv_mul_f32 %2, -%4, %6\n\tv_cvt_pk_bf16_f32 %0, %1, %2" : "=v"(o), "=&v"(t0), "=&v"(t1) : "v"(a), "v"(b), "v"(sa), "v"(sb));
  else asm volatile("v_mul_f32 %1, %3, %5\n\tv_mul_f32 %2, %4, %6\n\tv_cvt_pk_bf16_f32 %0, %1, %2" : "=v"(o), "=&v"(t0), "=&v"(t1) : "v"(a), "v"(b), "v"(sa), "v"(sb));
  return o;
}
DI u32x4 chunk_swap(u32 a0, u32 a1, u32 b0, u32 b1) {
  auto r0 = __builtin_amdgcn_permlane32_swap(a0, b0, false, false);
  auto r1 = __builtin_amdgcn_permlane32_swap(a1, b1, false, false);
  u32x4 o; o[0] = r0[0]; o[1] = r1[0]; o[2] = r0[1]; o[3] = r1[1];
  return o;
}
DI u32x4 chunk_swap16(u32 a0, u32 a1, u32 b0, u32 b1) {
  auto r0 = __builtin_amdgcn_permlane16_swap(a0, b0, false, false);
  auto r1 = __builtin_amdgcn_permlane16_swap(a1, b1, false, false);
  u32x4 o; o[0] = r0[0]; o[1] = r1[0]; o[2] = r0[1]; o[3] = r1[1];
  return o;
}
DI void ld_v4s(u32x4& d, u32 voff, rsrc_t r, u32 soff) {
  asm volatile("buffer_load_dwordx4 %0, %1, %2, %3 offen sc1" : "+v"(d) : "v"(voff), "s"(r), "s"(soff));
}
template <class Bad, class Reload> DI void ensure(Bad bad, Reload reload) {
  if (__builtin_expect(__builtin_amdgcn_ballot_w64(bad() != 0) != 0, 0)) {
#pragma nounroll
    for (u32 sp = 0; sp < SPIN_CAP; ++sp) {
      __builtin_amdgcn_s_sleep(1);
      reload();
      if (__builtin_amdgcn_ballot_w64(bad() != 0) == 0) break;
    }
  }
  asm volatile("" ::: "memory");
}
typedef u64 u64x8 __attribute__((ext_vector_type(8)));
DI void s_sample(u64x8& v, u64x8& v2, const char* p) { asm volatile("s_load_dwordx16 %0, %2, 0x0 glc\n\ts_load_dwordx16 %1, %2, 0x40 glc" : "=&s"(v), "=&s"(v2) : "s"(p)); }
DI void s_land(u64x8& v, u64x8& v2) { asm volatile("" : "+s"(v), "+s"(v2) :: "memory"); }
template <int N> DI bool s_match(u64x8& v, u64x8& v2, u64 tag) {
  u64 x = 0;
  sfor<8>([&](auto i) { x |= v[+i] ^ tag; });
  sfor<N - 8>([&](auto i) { x |= v2[+i] ^ tag; });
  return x == 0;
}
template <int N> DI void s_wait(const char* p, u64 tag) {
#pragma nounroll
  for (u32 sp = 0; sp < 4096; ++sp) {
    u64x8 v, v2;
    u64 x = 0;
    if constexpr (N > 8) {
      asm volatile("s_load_dwordx16 %0, %2, 0x0 glc\n\ts_load_dwordx16 %1, %2, 0x40 glc\n\ts_waitcnt lgkmcnt(0)" : "=&s"(v), "=&s"(v2) : "s"(p) : "memory");
      sfor<N - 8>([&](auto i) { x |= v2[+i] ^ tag; });
    } else {
      asm volatile("s_load_dwordx16 %0, %1, 0x0 glc\n\ts_waitcnt lgkmcnt(0)" : "=&s"(v) : "s"(p) : "memory");
    }
    sfor<8>([&](auto i) { x |= v[+i] ^ tag; });
    if (x == 0) return;
    __builtin_amdgcn_s_sleep(POLL_SLEEP);
  }
  const GAS u64* q = (const GAS u64*)p + (__builtin_amdgcn_mbcnt_lo(~0u, 0u) % N);
#pragma nounroll
  for (u32 sp = 0; sp < SPIN_CAP; ++sp) if (__all(__hip_atomic_load(q, RLX_AGENT) == tag)) break;
  asm volatile("" ::: "memory");
}
DI void post_flag(char* p, u64 tag) { __hip_atomic_store((GAS u64*)p, tag, RLX_AGENT); }

#define STAMP(i) do {} while (0)
#define FS(i) do {} while (0)

#define PRO_LEFT 0
#define PRE_WAIT 1
#define FA_AT 0
#define NH1 2
#define GW_AT 1
#define WM_EARLY 1
#define NTHR1 0
#define NTHR0 4
#define UG_AT 24
#define UV_FIRST 1
#define SAMEX 0
#define EPI_FIRST 1
#define NU0 8
#define NU1 10
#define LATIN 0
#define ROT2 0
#define ROT4 0
#define ROT5 0
#define NINIT 3
#define NTHR 8
#define ND1 4
#define TPAUSE 6
#define RATE 21
constexpr int FLT = 7;
constexpr int P1_MODFL = 5, P1_MODLD = 6, P1_MODUSE = 10, P1_XR = 3, P1_RS = 6, XSETS = 6;
enum { O_NONE, O_X, O_A, O_D, O_FM, O_MG, O_M2, O_XR, O_ST };
struct Op { int kind, a, b; };
struct Sched1 {
  Op op[64][4][6]; int nop[64][4];
  int posx[16][4], posa[20], posfm, posmg, top[64], bar[16], gap2[16], end, last;
};
constexpr Sched1 mk_sched1() {
  Sched1 s{};
  auto add = [&](int t, int m, int kind, int a, int b) { s.op[t][m][s.nop[t][m]++] = Op{kind, a, b}; };
  {
    int e = 0;
    for (int i = 0; i < 4; ++i, ++e) add(e >> 1, 1 + 2 * (e & 1), O_X, 2, i);
    for (int st = NINIT + 1; st < 4; ++st) for (int i = 0; i < 4; ++i, ++e) add(e >> 1, 1 + 2 * (e & 1), O_A, st, i);
    for (int i = 0; i < 4; ++i, ++e) add(e >> 1, 1 + 2 * (e & 1), O_X, 3, i);
  }
  add(4 * P1_MODFL + 3, 0, O_FM, 0, 0);
  add(4 * P1_MODLD + 8, 0, O_MG, 0, 0);
  {
    int e = 0, acc = 0;
    for (int t = TPAUSE; t < 64 && e < 8 * (16 - 4); ++t) {
      acc += RATE;
      for (int m = 1; m < 4 && acc >= 8 && e < 8 * (16 - 4); ++m) {
        const int k = 4 + (e >> 3), i = e & 3;
        if ((e & 4) == 0) { if (t <= 4 * (k - 8) + 3) break; add(t, m, O_A, k, i); }
        else { if (t < 4 * (k - XSETS) + 4 || t <= FLT) break; add(t, m, O_X, k, i); }
        acc -= 8; ++e; s.last = t;
      }
      if (acc > 24) acc = 24;
    }
  }
  for (int t = 48; t < 64; ++t) {
    if (((t - 48) >> 2) < ND1) add(t, 2, O_D, (t - 48) >> 2, t & 3);
    if (t >= 52 && (t & 1)) add(t, 3, O_ST, 0, 0);
    if (t >= 48 && t < 52) add(t, 0, O_M2, t - 48, 0);
    if (t == 53 || t == 54) add(t, 0, O_XR, t - 53, 0);
  }
  int n = 8;
  for (int i = 0; i < NINIT; ++i) s.posa[i] = -1;
  for (int i = 0; i < 8; ++i) s.posx[i >> 2][i & 3] = i;
  if (NINIT < 4) { n += 4; s.posa[NINIT] = n - 1; }
  for (int t = 0; t < 64; ++t) {
    s.top[t] = n;
    for (int m = 0; m < 4; ++m) {
      if (m == 1 && (t & 3) == 3) s.bar[t >> 2] = n;
      if (m == 2 && (t & 3) == 3) s.gap2[t >> 2] = n;
      for (int q = 0; q < s.nop[t][m]; ++q, ++n) {
        const Op o = s.op[t][m][q];
        if (o.kind == O_X) s.posx[o.a][o.b] = n;
        if (o.kind == O_A) s.posa[o.a] = n;
        if (o.kind == O_FM) s.posfm = n;
        if (o.kind == O_MG) s.posmg = n;
      }
    }
  }
  s.end = n;
  return s;
}
constexpr Sched1 S1 = mk_sched1();
static_assert(S1.last < 46, "phase (i) requests do not fit");
constexpr int p1_slot(int t, int m) {
  for (int q = 0; q < S1.nop[t][m]; ++q) { if (S1.op[t][m][q].kind == O_A) return S1.op[t][m][q].a & 7; if (S1.op[t][m][q].kind == O_D) return S1.op[t][m][q].a; }
  return -1;
}
constexpr bool p1_newm0(int t, int m) {
  const int sl = p1_slot(t, m);
  if (sl < 0) return false;
  for (int g = 4 * t + m - 1; g >= 0; --g) if (p1_slot(g >> 2, g & 3) >= 0) return p1_slot(g >> 2, g & 3) != sl;
  return true;
}
#define NG0 6
constexpr int P4_PRE = 8;
constexpr int P4_FJ = 10;
constexpr int P4_ST = 6, P4_UG = UG_AT, P4_UR = 31;
struct Sched4 { int posx[32], posg[24], posst, posug, posfa, possto, wait[64], bar[16], end; };
constexpr Sched4 mk_sched4() {
  Sched4 s{}; int n = 0;
  for (int i = 0; i < P4_PRE; ++i) s.posx[i] = n++;
  n += 4; s.posst = n - 1;
  for (int t = 0; t < 64; ++t) {
    if (t == 33) { n += 4; s.possto = n - 1; }
    s.wait[t] = n;
    for (int m = 0; m < 4; ++m) {
      if (m == 0 && t < 4 * (8 - NG0)) s.posg[NG0 + (t >> 2)] = n++;
      if (m == 1 && (t & 3) == 3) s.bar[t >> 2] = n;
      if (m == 1) { if (t + P4_PRE < 32) s.posx[t + P4_PRE] = n++; }
      if (m == 2 && t >= P4_UG && t < P4_UG + 2) s.posug = n++;
      if (m == 3 && t >= 3) s.posg[8 + ((t - 3) >> 2)] = n++;
    }
  }
  s.end = n;
  return s;
}
constexpr Sched4 S4 = mk_sched4();
#define P5CHK 12
constexpr int P5_PRE = 5, P5_CHK = P5CHK;
struct Sched5 { int op[28][8], posa[28][2], posn[16], wait[28], bar[16]; };
constexpr Sched5 mk_sched5() {
  Sched5 s{}; int n = 2 * P5_PRE, e = 2 * P5_PRE, d = 0;
  for (int i = 0; i < 2 * P5_PRE; ++i) s.posa[i >> 1][i & 1] = i;
  for (int t = 0; t < 28; ++t) {
    s.wait[t] = n;
    for (int m = 0; m < 8; ++m) {
      if (m == 2 && (t & 1)) s.bar[t >> 1] = n;
      if (m != 0 && m != 3 && m != 4 && m != 7) continue;
      const bool dok = d < 24 && 8 * t + m > 8 * (2 * (d >> 2) + 1) + 2;
      const bool eok = e < 56 && (e < 28 || t >= P5_CHK) && (e < 28 + 12 || m == 0 || m == 4);
      if (dok && (!eok || m == 3 || m == 7)) { s.op[t][m] = 100 + d; s.posn[8 + (d >> 2)] = n++; ++d; }
      else if (eok) { s.op[t][m] = 1 + e; s.posa[e >> 1][e & 1] = n++; ++e; }
    }
  }
  return s;
}
constexpr Sched5 S5 = mk_sched5();

struct Ge { float g0, g1, a0, a1, m0, m1; };
template <int U> DI void ge_unit(Ge& e, float q0, float q1, float u0, float u1, float r, float sd, float c3, float c1, u32& o) {
  if constexpr (U == 0) asm volatile("v_mul_f32 %0, %2, %3\n\tv_mul_f32 %1, %2, %4" : "=&v"(e.g0), "=&v"(e.g1) : "v"(r), "v"(q0), "v"(q1));
  if constexpr (U == 1) asm volatile("v_mul_f32 %0, %2, %2\n\tv_mul_f32 %1, %3, %3" : "=&v"(e.a0), "=&v"(e.a1) : "v"(e.g0), "v"(e.g1));
  if constexpr (U == 2) asm volatile("v_fma_f32 %0, %0, %2, %3\n\tv_fma_f32 %1, %1, %2, %3" : "+v"(e.a0), "+v"(e.a1) : "v"(c3), "v"(c1));
  if constexpr (U == 3) asm volatile("v_mul_f32 %0, %0, %2\n\tv_mul_f32 %1, %1, %3" : "+v"(e.a0), "+v"(e.a1) : "v"(e.g0), "v"(e.g1));
  if constexpr (U == 4) asm volatile("v_exp_f32 %0, %0" : "+v"(e.a0));
  if constexpr (U == 5) asm volatile("v_exp_f32 %0, %0" : "+v"(e.a1));
  if constexpr (U == 6) asm volatile("v_mul_f32 %0, %2, %4\n\tv_mul_f32 %1, %3, %5" : "=&v"(e.m0), "=&v"(e.m1) : "v"(e.g0), "v"(e.g1), "v"(u0), "v"(u1));
  if constexpr (U == 7) asm volatile("v_fma_f32 %0, %0, %2, %2\n\tv_fma_f32 %1, %1, %2, %2" : "+v"(e.a0), "+v"(e.a1) : "v"(sd));
  if constexpr (U == 8) asm volatile("v_rcp_f32 %0, %0" : "+v"(e.a0));
  if constexpr (U == 9) asm volatile("v_rcp_f32 %0, %0" : "+v"(e.a1));
  if constexpr (U == 10) asm volatile("v_mul_f32 %0, %0, %2\n\tv_mul_f32 %1, %1, %3" : "+v"(e.m0), "+v"(e.m1) : "v"(e.a0), "v"(e.a1));
  if constexpr (U == 11) asm volatile("v_cvt_pk_bf16_f32 %0, %1, %2" : "=v"(o) : "v"(e.m0), "v"(e.m1));
}
DI u32x4 geglu8(f32x4 ga, f32x4 gb, f32x4 ua, f32x4 ub, f32x2 rsa, f32x2 rsb, f32x2 k) {
  f32x2 g0 = {ga[0], ga[1]}, g1 = {ga[2], ga[3]}, g2 = {gb[0], gb[1]}, g3 = {gb[2], gb[3]};
  f32x2 u0 = {ua[0], ua[1]}, u1 = {ua[2], ua[3]}, u2 = {ub[0], ub[1]}, u3 = {ub[2], ub[3]};
  f32x2 a0, a1, a2, a3;
#define LO "op_sel_hi:[0,1]\n\t"
  asm volatile("v_pk_mul_f32 %0, %8, %0 " LO "v_pk_mul_f32 %1, %8, %1 " LO "v_pk_mul_f32 %2, %9, %2 " LO "v_pk_mul_f32 %3, %9, %3 " LO
               "v_pk_mul_f32 %4, %0, %0\n\tv_pk_mul_f32 %5, %1, %1\n\tv_pk_mul_f32 %6, %2, %2\n\tv_pk_mul_f32 %7, %3, %3\n\t"
               "v_pk_fma_f32 %4, %4, %10, %10 op_sel:[0,0,1] op_sel_hi:[1,0,1]\n\tv_pk_fma_f32 %5, %5, %10, %10 op_sel:[0,0,1] op_sel_hi:[1,0,1]\n\t"
               "v_pk_fma_f32 %6, %6, %10, %10 op_sel:[0,0,1] op_sel_hi:[1,0,1]\n\tv_pk_fma_f32 %7, %7, %10, %10 op_sel:[0,0,1] op_sel_hi:[1,0,1]\n\t"
               "v_pk_mul_f32 %4, %4, %0\n\tv_pk_mul_f32 %5, %5, %1\n\tv_pk_mul_f32 %6, %6, %2\n\tv_pk_mul_f32 %7, %7, %3"
               : "+v"(g0), "+v"(g1), "+v"(g2), "+v"(g3), "=&v"(a0), "=&v"(a1), "=&v"(a2), "=&v"(a3) : "v"(rsa), "v"(rsb), "v"(k));
#undef LO
  f32x2* const a[4] = {&a0, &a1, &a2, &a3};
  sfor<4>([&](auto i) { (*a[i])[0] = __builtin_amdgcn_exp2f((*a[i])[0]); (*a[i])[1] = __builtin_amdgcn_exp2f((*a[i])[1]); });
#define HI " op_sel:[0,1,1] op_sel_hi:[1,1,1]\n\t"
  asm volatile("v_pk_mul_f32 %4, %4, %8\n\tv_pk_mul_f32 %5, %5, %9\n\tv_pk_mul_f32 %6, %6, %10\n\tv_pk_mul_f32 %7, %7, %11\n\t"
               "v_pk_fma_f32 %0, %0, %12, %12" HI "v_pk_fma_f32 %1, %1, %12, %12" HI "v_pk_fma_f32 %2, %2, %13, %13" HI "v_pk_fma_f32 %3, %3, %13, %13 op_sel:[0,1,1] op_sel_hi:[1,1,1]"
               : "+v"(a0), "+v"(a1), "+v"(a2), "+v"(a3), "+v"(u0), "+v"(u1), "+v"(u2), "+v"(u3) : "v"(g0), "v"(g1), "v"(g2), "v"(g3), "v"(rsa), "v"(rsb));
#undef HI
  sfor<4>([&](auto i) { (*a[i])[0] = __builtin_amdgcn_rcpf((*a[i])[0]); (*a[i])[1] = __builtin_amdgcn_rcpf((*a[i])[1]); });
  u0 *= a0; u1 *= a1; u2 *= a2; u3 *= a3;
  return chunk_swap(pack2(u0[0], u0[1]), pack2(u1[0], u1[1]), pack2(u2[0], u2[1]), pack2(u3[0], u3[1]));
}
DI void mac2(float& a0, float& a1, u32 d, float s) {
  float t0, t1;
  asm volatile("v_lshlrev_b32 %2, 16, %4\n\tv_and_b32 %3, 0xffff0000, %4\n\tv_fmac_f32 %0, %5, %2\n\tv_fmac_f32 %1, %5, %3"
               : "+v"(a0), "+v"(a1), "=&v"(t0), "=&v"(t1) : "v"(d), "v"(s));
}

__global__ void __launch_bounds__(256, 1)
mk_block(const u16* __restrict__ x, const u16* __restrict__ Sre, const u16* __restrict__ Sim, const u16* __restrict__ Dre,
         const u16* __restrict__ Dim, const u16* __restrict__ Wg, const u16* __restrict__ Wu, const u16* __restrict__ Wn,
         const u16* __restrict__ cvec, const u16* __restrict__ wmod, u16* __restrict__ out, char* __restrict__ ws) {
  extern __shared__ __attribute__((aligned(16))) char smem_[];
  LAS char* sm = (LAS char*)smem_;
  const int tid = threadIdx.x, lane = tid & 63, w = __builtin_amdgcn_readfirstlane(tid >> 6);
  const int l31 = lane & 31, hh = lane >> 5, l15 = lane & 15, qq = lane >> 4;
  const int bid = blockIdx.x, xcd = bid & 7, loc = bid >> 3;
  const int tb = xcd * 4 + (loc >> 3), nt = loc & 7;
  const int cb = nt & 3, kh = nt >> 2;
  const int rb = SAMEX ? tb >> 1 : (xcd >> 1) * 4 + loc / 7, ft = SAMEX ? (tb & 1) * 7 + nt : (xcd & 1) * 7 + loc % 7;
  const int w1 = w & 1, wk = w >> 1;
  const bool act4 = SAMEX ? nt < 7 : loc < 28;
  const u32 lane16 = lane * 16, wl = w * 4096;
  const rsrc_t rX = mk_rsrc(x, S * D * 2);
  u32x4 xr[4];
  sfor<4>([&](auto i) { ld_v4p<i * 1024>(xr[i], lane16, rX, w < 2 ? (bid * 8 + w * 4) * 1024 : 0x40000000); });
  STAMP(0); FS(90);
  const rsrc_t rW = mk_rsrc(ws, WS_END);
  u32 vA[4];
  sfor<4>([&](auto i) { vA[i] = (lane >> 3) * 4096 + (((lane & 7) ^ ((i & 1) * 4 + qq)) << 4) + i * (8 * 4096 - 1024); });
  const int ro = cb;
  auto kblk = [&](int st) { return kh * 16 + ((ro * 4 + st) & 15); };
  u64 tag = *(const __attribute__((address_space(4))) u64*)(ws + WS_GEN);

  const int mkq = bid < 192 ? bid / 48 : 3, mcb = bid % 48;
  u32x4 gvd = {0u, 0u, 0u, 0u}, gvd2 = gvd;
  u64 pc; u32 clen, cof2, cof4, cpf = 0;
  asm volatile("s_getpc_b64 %0\n.Lmk_a:\n\ts_mov_b32 %1, .Lmk_z-.Lmk_a\n\ts_mov_b32 %2, .Lmk_2-.Lmk_a\n\ts_mov_b32 %3, .Lmk_4-.Lmk_a"
               : "=s"(pc), "=s"(clen), "=s"(cof2), "=s"(cof4));
  const rsrc_t rP = mk_rsrc((const void*)pc, clen);
  *(LAS u32*)(sm + L_CNT + (lane & 3) * 4) = 0;
  bar();
  const u32 sF = WS_F1 + (xcd * 256 + kh * 128) * 8;
  if (w < 2) {
    u32x4 wm[8], cr;
    if constexpr (WM_EARLY) {
    sfor<8>([&](auto r) { ld_v4p<0>(wm[r], (lane >> 3) * 8 * 6144 + (lane & 7) * 16, mk_rsrc(wmod, D * 6 * D * 2), (mkq * 128 + w * 64 + r) * 6144 + mcb * 128); });
    ld_v4p<0>(cr, (lane >> 3) * 16, mk_rsrc(cvec, D * 2), (mkq * 128 + w * 64) * 2);
    }
    wait_vm<(WM_EARLY ? 9 : 0)>();
    sfor<4>([&](auto i) { tie_v(xr[i]); });
    FS(91);
    f32x2 v[4][4];
    float r4[4];
    sfor<4>([&](auto i) {
      sfor<4>([&](auto j) { u32 d = xr[i][+j]; v[i][j] = f32x2{b_lo(d), b_hi(d)}; });
      const f32x2 t = (v[i][0] + v[i][1]) + (v[i][2] + v[i][3]);
      r4[i] = t[0] + t[1];
    });
    const float mu = wave_sum4(r4[0], r4[1], r4[2], r4[3]) * (1.f / D);
    sfor<4>([&](auto i) {
      const float m = lane_of(mu, i);
      sfor<4>([&](auto j) { v[i][j] -= m; });
      const f32x2 t = (v[i][0] * v[i][0] + v[i][1] * v[i][1]) + (v[i][2] * v[i][2] + v[i][3] * v[i][3]);
      r4[i] = t[0] + t[1];
    });
    const float rs = __builtin_amdgcn_rsqf(wave_sum4(r4[0], r4[1], r4[2], r4[3]) * (1.f / D) + 1e-5f);
    sfor<4>([&](auto i) {
      const float r = lane_of(rs, i);
      u32x4 o;
      sfor<4>([&](auto j) { const f32x2 t = v[i][j] * r; o[+j] = pack2(t[0], t[1]); });
      *(LAS u32x4*)(sm + L_EX + (w * 4 + i) * 1088 + lane16) = o;
    });
    FS(110);
    asm volatile("s_waitcnt lgkmcnt(0)" : "+s"(tag) :: "memory");
    tag += 1;
    *(volatile LAS u32*)(sm + L_CNT + w * 4) = 1;
    lds_spin(sm, L_CNT + (w ^ 1) * 4, 1);
    FS(92);
    const u32 q = (lane & 15) >> 2, pp = lane & 3;
    const u32 ta = L_EX + q * 1088 + (w * 256 + qq * 16 + pp * 4) * 2;
    sfor<4>([&](auto m) {
      u32x4 o = lds_tr(sm, ta + m * 128, ta + m * 128 + 4 * 1088);
      pub16<1>(o, rW, hh * 1024 + l31 * 16, WS_XN + (bid >> 1) * 16384 + (w * 8 + m * 2) * 1024 + (bid & 1) * 512);
    });
    auto modp = [&] {
    sfor<8>([&](auto r) { tie_v(wm[r]); });
    tie_v(cr);
    float ma[8];
    sfor<8>([&](auto q) { ma[q] = 0.f; });
    sfor<8>([&](auto r) {
      const u32 cd = cr[r >> 1];
      const float cv = (r & 1) ? b_hi(cd) : b_lo(cd);
      const float sv = cv * __builtin_amdgcn_rcpf(1.f + __builtin_amdgcn_exp2f(-1.4426950408889634f * cv));
      sfor<4>([&](auto jj) { u32 d = wm[r][+jj]; ma[2 * jj] += sv * b_lo(d); ma[2 * jj + 1] += sv * b_hi(d); });
    });
    float b4[4], b2[2];
    sfor<4>([&](auto jj) { b4[jj] = red_x32(ma[jj], ma[4 + jj]); });
    sfor<2>([&](auto jj) { b2[jj] = red_x16(b4[jj], b4[2 + jj]); });
    *(LAS float*)(sm + L_MP + w * 256 + ((lane & 7) * 8 + (lane >> 3)) * 4) = red_x8(b2[0], b2[1], (lane >> 3) & 1);
    FS(113);
    };
    if constexpr (WM_EARLY) { wait_vm<4>(); modp(); wait_vm<0, 1>(); }
    else {
    sfor<8>([&](auto r) { ld_v4p<0>(wm[r], (lane >> 3) * 8 * 6144 + (lane & 7) * 16, mk_rsrc(wmod, D * 6 * D * 2), (mkq * 128 + w * 64 + r) * 6144 + mcb * 128); });
    ld_v4p<0>(cr, (lane >> 3) * 16, mk_rsrc(cvec, D * 2), (mkq * 128 + w * 64) * 2);
      wait_vm<9, 1>();
    }
    FS(93);
    *(volatile LAS u32*)(sm + L_CNT + 8 + w * 4) = 1;
    lds_spin(sm, L_CNT + 8 + (w ^ 1) * 4, 1);
    pub8<0>(u32x2{(u32)tag, (u32)(tag >> 32)}, rW, (w * 4 + (lane & 3)) * 2048, WS_F1 + bid * 8);
    STAMP(14);
    FS(111);
    wait_vm<1>();
    if constexpr (!WM_EARLY) modp();
  } else {
    ld_v1p<0>(cpf, lane * 128, rP, w1 == 0 ? 0 : (1 + loc % 5) * 8192);
    const rsrc_t r0 = mk_rsrc(w1 == 0 ? Sre : Sim, S * S * 2);
    sfor<NINIT * 8>([&](auto qc) {
      constexpr int q = qc, st = q >> 3, hf = (q >> 2) & 1, p = q & 3;
      if constexpr (p == 0) set_m0n<st * 16384 + hf * 4096>(w1 * 8192);
      if constexpr (q >= NTHR + NTHR1) wait_vm<NTHR - 1>(); else if constexpr (q == NTHR0) wait_vm<NTHR0 - 1>();
      dma<100 + st, p * 1024>(vA[p], r0, (tb * 64 + hf * 32) * 4096 + kblk(st) * 128);
    });
    ld_v4t(gvd, lane16, mk_rsrc(bid < 128 ? Dre : Dim, D * D * 2), ((bid & 127) * 4 + w1 * 2) * 1024);
    ld_v4t(gvd2, lane16, mk_rsrc(bid < 128 ? Dre : Dim, D * D * 2), ((bid & 127) * 4 + w1 * 2 + 1) * 1024);
    sfor<4>([&](auto i) { tie_v(xr[i]); });
    asm volatile("" : "+s"(tag));
    tag += 1;
  }
  const rsrc_t rS = mk_rsrc(w < 2 ? Sre : Sim, S * S * 2);
  const rsrc_t rD = mk_rsrc(w < 2 ? Dre : Dim, D * D * 2);
  u32 vD[4], vX[4];
  sfor<4>([&](auto i) { vD[i] = (lane >> 3) * 1024 + (((lane & 7) ^ ((i & 1) * 4 + qq)) << 4) + i * (8 * 1024 - 1024); vX[i] = lane16 + i * 16384; tie_v(vD[i]); tie_v(vX[i]); });
  const u32 sA = (tb * 64 + w1 * 32) * 4096, sD = (nt * 64 + w1 * 32) * 1024;
  const u32 sXN = WS_XN + (cb * 4 + w) * 1024;
  auto a_dma = [&](auto stc, auto pc) { constexpr int st = stc, p = pc; dma<100 + st, p * 1024>(vA[p], rS, sA + kblk(st) * 128); };
  auto d_dma = [&](auto sc_, auto pc) { constexpr int s = sc_, p = pc; dma<2, p * 1024>(vD[p], rD, sD + s * 128); };
  u32x4 xw[XSETS][4];
  auto xn_ld = [&](auto stc, auto ic_) { constexpr int st = stc, i = ic_; ld_a<0>(xw[st % XSETS][i], vX[i], rW, sXN + kblk(st) * 65536); };
  u32x4 mg[4], xres[2];
  u32x4 fm;
  u32x2 cam;
  u64 ap[4];

  u32 aB[4][2];
  sfor<4>([&](auto i) { sfor<2>([&](auto hf) { aB[i][hf] = l31 * 128 + (((2 * i + hh) ^ ((l31 >> 1) & 7)) << 4) + hf * 65536; tie_v(aB[i][hf]); }); });
  u32x4 af[2][4], rf[2];
  u32 tch = 0;
  auto a_rd = [&](auto tc, auto jc) {
    constexpr int t = tc, j = jc;
    if constexpr (t < 48) af[t & 1][j] = rd_asm<((t >> 2) & 3) * 16384 + j * 4096>(aB[t & 3][(t >> 4) & 1]);
    else af[t & 1][j] = rd_asm<(t & 3) * 16384 + ((t - 48) >> 2) * 4096>(aB[j][1]);
  };
  f32x16 acc[4], rsa;
  u32x4 ones = {0x3f803f80u, 0x3f803f80u, 0x3f803f80u, 0x3f803f80u};
  tie_v(ones);
  const bool h0 = (lane >> 3) & 1;
  u32 lM = L_MODV; tie_v(lM);
  u32 mI = w < 2 ? tid * 4 : w == 2 ? (lane < 32 ? 512 + cb * 128 + lane * 4 : 1024 + (qq & 1) * 1024 + nt * 64 + l15 * 4) : 2560 + nt * 64 + l15 * 4;
  u32 vFM = w * 384 + (lane < 24 ? lane : 0) * 16;
  tie_v(mI); tie_v(vFM);
  f32x4 sc[4];
  u32 pk[8];
  u32 vP = (l31 >> 4) * 65536 + hh * 256 + l15 * 16;
  tie_v(vP);
  const u32 sP = WS_PQ + (tb * 4 * 64 + kh * 32 + cb * 4 + w) * 1024;
  auto p_unit = [&](auto jc, auto uc) {
    constexpr int j = jc, u = uc;
    pk[u] = scale_pack<(j >= 2)>(acc[j][2 * u], acc[j][2 * u + 1], sc[u >> 1][2 * (u & 1)], sc[u >> 1][2 * (u & 1) + 1]);
    if constexpr ((u & 3) == 3) {
      asm volatile("s_nop 1" : "+v"(pk[u]));
      pub16<3, (u >> 2) * 512>(chunk_swap(pk[u - 3], pk[u - 2], pk[u - 1], pk[u]), rW, vP, sP + ((j & 1) * 128 + (j >> 1) * 16) * 1024);
    }
  };
  u32x4 fl4 = {0u, 0u, 0u, 0u};
  if (w == 2) s_wait<16>(ws + sF + ro * 256, tag);
  FS(112);
  bar();
  if (w == 1 && bid < 192) {
    const float s = *(LAS float*)(sm + L_MP + lane * 4) + *(LAS float*)(sm + L_MP + 256 + lane * 4);
    pub4<7>(__float_as_uint(s), rW, lane * 4, WS_MODP + (mkq * 3072 + mcb * 64) * 4);
  }
  const u32 tag32 = (u32)tag, tagh = (u32)(tag >> 32);
  STAMP(1); FS(94);

  sfor<2>([&](auto kc) { sfor<4>([&](auto i) { xn_ld(kc, i); }); });
  if constexpr (NINIT < 4) { set_m0n<NINIT * 16384>(wl); sfor<4>([&](auto p) { a_dma(ic<NINIT>{}, p); }); }
  if (w == 0) {
    s_wait<16>(ws + sF + ro * 256 + 128, tag);
    ld_v4s(fl4, lane16, rW, sF);
  }
  bar_vm<S1.top[0] - 1, 100>();

  FS(95);
  sfor<4>([&](auto j) { a_rd(ic<0>{}, j); });
  sfor<64>([&](auto tc) {
    constexpr int t = tc, k = t >> 2, i = t & 3;
    SCHED();
    if constexpr (t < 48) wait_a<S1.top[t] - 1 - S1.posx[k][i]>(xw[0][0]);
    else if constexpr (t < 52) wait_a<S1.top[t] - 1 - S1.posx[t - 36][3]>(xw[0][0]);
    if constexpr (t < 3) FS(97 + t);
    if constexpr (t >= 48 && i == 0) FS(k);
    sfor<4>([&](auto mc) {
      constexpr int m = mc;
      constexpr bool rs_next = t + 1 >= 4 * P1_RS && t + 1 < 4 * P1_RS + 16, rs_now = k >= P1_RS && k < P1_RS + 4;
      if constexpr (p1_newm0(t, m)) set_m0<p1_slot(t, m) * 16384>(wl);
      if constexpr (m == 0) wait_l<(rs_now ? 3 : 2)>();
      if constexpr (m == 2) wait_l<(t < 63 ? 2 : 0)>();
      if constexpr (t < 48) mm_av<t == 0>(acc[m], xw[k % XSETS][i], af[t & 1][m]);
      else mm_av<false>(acc[k - 12], xw[(12 + i) % XSETS][m], af[t & 1][m]);
      if constexpr (rs_now && m == 2) mm_vv<t == 4 * P1_RS>(rsa, ones, rf[t & 1]);
      SCHED();
      if constexpr (m == 0 && (i != 3 || k >= 12) && t < 63) { a_rd(ic<t + 1>{}, ic<0>{}); a_rd(ic<t + 1>{}, ic<1>{}); }
      if constexpr (m == 0 && t == FLT) {
        tie_v(cpf);
        wait_vm<S1.top[FLT] - S1.top[0]>();
        tie_v(fl4);
        if (w == 0) {
          ensure([&] { return (fl4[0] ^ tag32) | (fl4[1] ^ tagh) | (fl4[2] ^ tag32) | (fl4[3] ^ tagh); }, [&] { ld_v4s(fl4, lane16, rW, sF); wait_vm<0>(); tie_v(fl4); });
        } else if (w == 1 && bid < 192) {
          wait_vm<S1.top[FLT] - 1, 7>();
          if (lane == 0) post_flag(ws + WS_FM + bid * 8, tag);
        }
      }
      if constexpr (m == 1 && i == 3 && k < 12) {
        constexpr int na = S1.bar[k] - 1 - S1.posa[k == 11 ? 15 : k + 1];
        constexpr int ne = k == P1_MODLD ? S1.bar[k] - 1 - S1.posfm : k == P1_MODUSE ? S1.bar[k] - 1 - S1.posmg : 63;
        if constexpr (k == 11) argp4(ap, 0, w < 2 ? 5 + w : 7, w < 2 ? 5 : 6, 7);
        bar_vm<(na < ne ? na : ne), 101 + k, (k == 11 ? 115 : 0)>();
        if constexpr (k == 11) argp4_land(ap);
        FS(k);
        a_rd(ic<t + 1>{}, ic<0>{}); a_rd(ic<t + 1>{}, ic<1>{});
      }
      if constexpr (m == 2 && t < 63) {
        a_rd(ic<t + 1>{}, ic<2>{}); a_rd(ic<t + 1>{}, ic<3>{});
        if constexpr (rs_next) rf[(t + 1) & 1] = rd_asm<(((t + 1) >> 2) & 3) * 16384>(aB[(t + 1) & 3][((t + 1) >> 4) & 1] + wl);
      }
      if constexpr (t >= 48 && t < 52 && m == 0) sc[i] = ldsf4(sm, lM + (L_RS - L_MODV) + w * 512 + (i * 8 + hh * 4) * 4);
      if constexpr (t >= 52 && (m & 1)) {
        if constexpr (m == 1 && i == 0) asm volatile("s_nop 7" : "+v"(acc[k - 13]));
        p_unit(ic<k - 13>{}, ic<2 * i + (m >> 1)>{});
      }
      if constexpr (m == 2 && i == 3) {
        if constexpr (k == P1_MODLD) {
          tie_v(fm);
          ensure([&] { return (fm[0] ^ tag32) | (fm[1] ^ tagh) | (fm[2] ^ tag32) | (fm[3] ^ tagh); }, [&] { ld_v4s(fm, vFM, rW, WS_FM); wait_vm<0>(); tie_v(fm); });
        }
        if constexpr (k == P1_MODUSE) {
          tie_v(cam);
          const u32 c0 = cam[0], c1 = cam[1];
          *(LAS f32x2*)(sm + lM + (L_RS - L_MODV) + w * 512 + l15 * 8) = f32x2{add_x32(add_x16(__uint_as_float(c0))) + 1.f, add_x32(add_x16(__uint_as_float(c1))) + 1.f};
        }
      }
      sfor<S1.nop[t][m]>([&](auto qc) {
        constexpr Op o = S1.op[t][m][qc];
        if constexpr (o.kind == O_X) xn_ld(ic<o.a>{}, ic<o.b>{});
        if constexpr (o.kind == O_A) a_dma(ic<o.a>{}, ic<o.b>{});
        if constexpr (o.kind == O_D) d_dma(ic<o.a>{}, ic<o.b>{});
        if constexpr (o.kind == O_FM) ld_v4<0>(fm, vFM, rW, WS_FM);
        if constexpr (o.kind == O_M2) {
          ld_v4<0>(mg[o.a], mI * 4, rW, WS_MODP + o.a * 3072 * 4);
        }
        if constexpr (o.kind == O_XR) ld_v4p<o.a * 64>(xres[o.a], l15 * 1024 + (qq & 1) * 32 + (qq >> 1) * 16, mk_rsrc((const void*)ap[0], S * D * 2), (tb * 64 + w * 16) * 1024 + nt * 128);
        if constexpr (o.kind == O_MG) ld_v2<0>(cam, (qq * 3072 + 512 + cb * 128 + w * 32 + 2 * l15) * 4, rW, WS_MODP);
      });
      SCHED();
    });
  });
  STAMP(2); FS(32);
  {
    mm_fence(acc[3], rsa);
    sfor<8>([&](auto u) { p_unit(ic<3>{}, u); });
    pub4<3>(__float_as_uint(rsa[0]), rW, l31 * 4, WS_RS + ((tb * 8 + nt) * 128 + w * 32) * 4);
    bar();
    FS(39);
    sfor<NH1>([&](auto s) { set_m0n<(ND1 + s) * 16384>(wl); sfor<4>([&](auto p) { d_dma(ic<ND1 + s>{}, p); }); });
    wait_vm<4 * NH1 + 3>();
    {
      sfor<4>([&](auto q) { tie_v(mg[q]); });
      f32x4 p0 = __builtin_bit_cast(f32x4, mg[0]), p1 = __builtin_bit_cast(f32x4, mg[1]), p2 = __builtin_bit_cast(f32x4, mg[2]), p3 = __builtin_bit_cast(f32x4, mg[3]);
      *(LAS f32x4*)(sm + lM + tid * 16) = (p0 + p1) + (p2 + p3);
    }
    FS(108);
    bar_vm<4 * NH1, 3>();
    FS(109);
    if (tid < NREP) post_flag(ws + WS_F2 + tb * FG + tid * 128 + nt * 8, tag);
    sfor<8 - ND1 - NH1>([&](auto s) { set_m0n<(ND1 + NH1 + s) * 16384>(wl); sfor<4>([&](auto p) { d_dma(ic<ND1 + NH1 + s>{}, p); }); });
    if (w == 1) ld_v1p<0>(cpf, lane * 128, rP, cof2 + (loc & 3) * 8192);
    if (!act4) sfor<4>([&](auto j) { ld_v4s(mg[j], (1536 + tid * 4) * 4, rW, WS_MODP + j * 3072 * 4); });
    if (w >= 2) {
      tie_v(gvd); tie_v(gvd2);
      const f32x4 s0 = ldsf4(sm, lM + lane * 32), s1 = ldsf4(sm, lM + lane * 32 + 16);
      auto dot8 = [&](u32x4 g) { const u32 d0 = g[0], d1 = g[1], d2 = g[2], d3 = g[3];
        return s0[0] * b_lo(d0) + s0[1] * b_hi(d0) + s0[2] * b_lo(d1) + s0[3] * b_hi(d1) + s1[0] * b_lo(d2) + s1[1] * b_hi(d2) + s1[2] * b_lo(d3) + s1[3] * b_hi(d3); };
      float v = add_x16(red_x32(dot8(gvd), dot8(gvd2)));
      v += dpp<0xB1>(v); v += dpp<0x4E>(v); v += dpp<0x141>(v); v += dpp<0x140>(v);
      pub8<0>(u32x2{__float_as_uint(v), tag32}, rW, hh * 8, WS_VG + (bid * 4 + w1 * 2) * 8);
    }
  }
  const rsrc_t rU = mk_rsrc((const void*)ap[1], D * F * 2), rG = mk_rsrc((const void*)ap[2], D * F * 2), rN = mk_rsrc((const void*)ap[3], F * D * 2);
  f32x4 x1[4], ga[4], cbv[4];
  tie_v(xres[0]); tie_v(xres[1]);
  sfor<2>([&](auto p) {
    const u32x4 xt = chunk_swap16(xres[p][0], xres[p][1], xres[p][2], xres[p][3]);
    sfor<2>([&](auto h) { const u32 d0 = xt[2 * h], d1 = xt[2 * h + 1]; x1[2 * p + h] = f32x4{b_lo(d0), b_hi(d0), b_lo(d1), b_hi(d1)}; });
  });
  sfor<4>([&](auto c) {
    const u32 eo = (c * 16 + qq * 4) * 4;
    ga[c] = ldsf4(sm, lM + 640 * 4 + eo); cbv[c] = ldsf4(sm, lM + 704 * 4 + eo) + 1.f;
    tie_v(x1[c]); tie_v(ga[c]); tie_v(cbv[c]);
  });
  u32 vG[4], vN[4];
  sfor<4>([&](auto i) {
    vG[i] = (lane >> 3) * (F * 2) + (((lane & 7) ^ ((qq & 1) << 2)) << 4) + i * (8 * F * 2 - 1024);
    vN[i] = (lane >> 3) * 1024 + (((lane & 7) ^ (((qq & 1) + 2 * (i & 1)) << 1)) << 4) + i * (8 * 1024 - 1024);
    tie_v(vG[i]); tie_v(vN[i]);
  });
  u32 vS1 = (qq & 1) * 1024 + (qq >> 1) * 512 + l15 * 16; tie_v(vS1);
  u32 dB[2][2];
  sfor<2>([&](auto u) { sfor<2>([&](auto hf) { dB[u][hf] = l15 * 128 + (((4 * u + qq) ^ ((l15 >> 1) & 7)) << 4) + hf * 65536; tie_v(dB[u][hf]); }); });
  STAMP(3); FS(33);
  if (w == 0) s_wait<8>(ws + WS_F2 + tb * FG + (nt & (NREP - 1)) * 128, tag);
  bar();
  if (bid == 0 && tid == 0) post_flag(ws + WS_GEN, tag);
  asm volatile(".Lmk_2:");
  STAMP(4); FS(34);

  {
    constexpr int PRE = 6;
    constexpr int SM = 16;
    u32x4 pa[32][2];
    const u32 so = WS_PQ + (tb * 4 + w) * 65536;
    const u32 vP1 = lane16 + 32768;
    auto pa_ld = [&](auto sc_, auto hc) {
      constexpr int s = sc_, h = hc;
      ld_a<(s & 1) * 1024>(pa[s][h], h ? vP1 : lane16, rW, so + (s >> 2) * 2048 + ((s >> 1) & 1) * 16384);
    };
    sfor<PRE>([&](auto s) { pa_ld(s, ic<0>{}); pa_ld(s, ic<1>{}); });
    bar_vm<2 * PRE, 2>();
    u32 rp[4];
    u32x4 vg;
    float rre, rim;
    f32x4 tq[8];
    f32x4 y[4];
    u32x4 df[2][4];
    auto d_rd = [&](auto sc_, auto cc) { constexpr int s = sc_, c = cc; df[s & 1][c] = rd_asm<((s >> 2) & 3) * 16384 + ((s >> 1) & 1) * 8192 + c * 2048>(dB[s & 1][s >> 4]); };
    sfor<4>([&](auto c) { d_rd(ic<0>{}, c); });
    sfor<32>([&](auto sc_) {
      constexpr int s = sc_;
      constexpr int issued = 2 * (s + PRE) < 65 ? 2 * (s + PRE) : 65;
      constexpr int small = (s > SM && s <= SM + PRE) ? 5 : 0;
      SCHED();
      wait_l<0>();
      wait_a<issued - 1 - 2 * s + small>(pa[s][0]);
      if constexpr (s == 0) tie_v(cpf);
      if constexpr ((s & 3) == 0) FS(80 + (s >> 2));
      sfor<8>([&](auto mc) {
        constexpr int m = mc, c = m & 3, h = m >> 2, n = 2 * (s + PRE) + h;
        if constexpr (m == 4) wait_a<(issued < 65 ? issued + 1 : 65) - 1 - (2 * s + 1) + small>(pa[s][1]);
        m16_va<s == 0 && h == 0>(y[c], df[s & 1][c], pa[s][h]);
        SCHED();
        if constexpr (h == 0 && s < 31) d_rd(ic<s + 1>{}, ic<c>{});
        if constexpr (c == 1 && n < 64) pa_ld(ic<s + PRE>{}, ic<h>{});
        if constexpr (c == 1 && n == 64) ld_v1p<0>(tch, w < 2 ? (lane < 56 ? lane * 128 : 0x40000000) : (lane < 28 ? lane * 128 : 0x40000000), rU, w < 2 ? bid * 7168 : bid * 7168 + w1 * 3584);
        if constexpr (s == SM && m == 7) {
          sfor<4>([&](auto p) { ld_v1<p * 512>(rp[p], ((qq >> 1) * 512 + (qq & 1) * 64 + w * 16 + l15) * 4, rW, WS_RS + tb * 4096); });
          ld_v4<0>(vg, (hh * 512 + nt * 64 + 2 * l31) * 8, rW, WS_VG);
        }
        if constexpr (s == SM + PRE + 2 && m == 7) {
          sfor<4>([&](auto p) { tie_v(rp[p]); });
          tie_v(vg);
          ensure([&] { return (vg[1] ^ tag32) | (vg[3] ^ tag32); }, [&] { ld_v4s(vg, (hh * 512 + nt * 64 + 2 * l31) * 8, rW, WS_VG); wait_vm<0>(); tie_v(vg); });
          { u32 a = vg[0], b = vg[2]; u32x2 o = {a, b}; *(LAS u32x2*)(sm + lM + (L_RS - L_MODV) + w * 512 + lane * 8) = o; }
          const float r4 = add_x32((__uint_as_float(rp[0]) + __uint_as_float(rp[1])) + (__uint_as_float(rp[2]) + __uint_as_float(rp[3])));
          auto r = __builtin_amdgcn_permlane16_swap(__float_as_uint(r4), __float_as_uint(r4), false, false);
          u32 a = r[0], b = r[1]; rre = __uint_as_float(a); rim = __uint_as_float(b);
          tie_v(rre); tie_v(rim);
        }
        if constexpr (s == 26) tq[m] = ldsf4(sm, lM + (L_RS - L_MODV) + w * 512 + (m >> 2) * 256 + ((m & 3) * 16 + qq * 4) * 4);
        if constexpr (s >= 28 && (m == 2 || m == 4 || m == 6)) {
          constexpr int cc = s - 28;
          if constexpr (m == 2) { tq[cc] *= rre; tie_v(tq[cc]); }
          if constexpr (m == 4) { tq[cc] -= rim * tq[4 + cc]; tie_v(tq[cc]); }
          if constexpr (m == 6) { x1[cc] += ga[cc] * tq[cc]; tie_v(x1[cc]); }
        }
        SCHED();
      });
    });
    STAMP(5); FS(35);
    m16_fence(y);
    u32 pk[4][2];
    sfor<4>([&](auto c) {
      x1[c] += ga[c] * y[c];
      f32x4 s = x1[c] * cbv[c];
      pk[c][0] = pack2(s[0], s[1]); pk[c][1] = pack2(s[2], s[3]);
    });
    sfor<2>([&](auto p) {
      pub16<4, p * 2048>(chunk_swap16(pk[2 * p][0], pk[2 * p][1], pk[2 * p + 1][0], pk[2 * p + 1][1]), rW,
                         vS1, WS_X1 + ((tb * 2 + wk) * 32 + nt * 4) * 1024 + w1 * 256);
    });
    const f32x4 s4 = (x1[0] + x1[1]) + (x1[2] + x1[3]);
    const float mean = add_x32(add_x16((s4[0] + s4[1]) + (s4[2] + s4[3]))) * (1.f / 64);
    f32x4 q4 = {0.f, 0.f, 0.f, 0.f};
    sfor<4>([&](auto c) { f32x4 d = x1[c] - mean; q4 += d * d; });
    const float m2 = add_x32(add_x16((q4[0] + q4[1]) + (q4[2] + q4[3])));
    u32x2 o = {__float_as_uint(mean), __float_as_uint(m2)};
    pub8<4>(o, rW, l15 * 8, WS_ST + ((tb * 2 + wk) * 8 + nt) * 256 + w1 * 128);
    bar();
  }
  FS(36);
  const u32 sG = w1 * 32 * (F * 2) + ft * 256, sN = (wk * 128 + w1 * 32) * 1024 + nt * 128;
  auto g_dma = [&](auto jc, auto ic_) {
    constexpr int j = jc, i = ic_;
    dma<200 + j, i * 1024>(vG[i], rG, sG + ((j + ROT4) & 7) * (64 * F * 2) + (j >> 3) * 128);
  };
  auto n_dma = [&](auto jc, auto ic_) {
    constexpr int j = jc, i = ic_;
    dma<300 + j, i * 1024>(vN[i], rN, sN + (j % 7) * (256 * 1024) + (j / 7) * 65536);
  };
  u32x4 ug[2];
  const u32 vUV = ((tid >> 7) * F + ft * 128 + (tid & 127)) * 16;
  if (act4) {
    sfor<3>([&](auto j) { set_m0n<j * 16384>(wl); sfor<4>([&](auto i) { g_dma(j, i); }); });
    bar_vm<12, 4>();
    if (tid < NREP) post_flag(ws + WS_F3 + (tb >> 1) * FG + tid * 128 + ((tb & 1) * 8 + nt) * 8, tag);
  } else {
    const int uq = (xcd * 4 + (loc - 28)) * 4 + w, ublk = uq < 112 ? uq >> 1 : 0, uk0 = (uq & 1) * 256;
    u32x4 g[32];
    const rsrc_t rH = mk_rsrc(argp(ublk < 28 ? 5 : 6), D * F * 2);
    auto g_ld = [&](auto i) { ld_v4p<0>(g[i], (lane >> 3) * 32 * (F * 2) + (lane & 7) * 16, rH, (uk0 + i) * (F * 2) + (ublk % 28) * 128); };
    sfor<NU0>([&](auto i) { g_ld(i); });
    sfor<4>([&](auto j) { tie_v(mg[j]); });
    *(LAS f32x4*)(sm + lM + (L_UV - L_MODV) + tid * 16) = (__builtin_bit_cast(f32x4, mg[0]) + __builtin_bit_cast(f32x4, mg[1])) + (__builtin_bit_cast(f32x4, mg[2]) + __builtin_bit_cast(f32x4, mg[3]));
    bar_vm<NU0, 4>();
    if (tid < NREP) post_flag(ws + WS_F3 + (tb >> 1) * FG + tid * 128 + ((tb & 1) * 8 + nt) * 8, tag);
    sfor<NU1 - NU0>([&](auto i) { g_ld(ic<NU0 + i>{}); });
      {
        f32x4 sh[8], cs[8];
        sfor<8>([&](auto j) { const u32 o = lM + (L_UV - L_MODV) + (uk0 + (lane >> 3) * 32 + j * 4) * 4; sh[j] = ldsf4(sm, o); cs[j] = ldsf4(sm, o + 2048) + 1.f; });
        f32x2 au[4], av[4];
        sfor<4>([&](auto j) { au[j] = f32x2{0.f, 0.f}; av[j] = f32x2{0.f, 0.f}; });
        sfor<32>([&](auto i) {
          if constexpr (i + NU1 < 32) g_ld(ic<i + NU1>{});
          asm volatile("s_waitcnt vmcnt(%c1)" : "+v"(g[i]) : "i"(i + NU1 < 32 ? NU1 : 31 - i));
          sfor<4>([&](auto j) { const u32 d = g[i][+j]; const f32x2 e = {b_lo(d), b_hi(d)}; au[j] += cs[i >> 2][i & 3] * e; av[j] += sh[i >> 2][i & 3] * e; });
        });
        float a[16];
        sfor<4>([&](auto j) { a[2 * j] = au[j][0]; a[2 * j + 1] = au[j][1]; a[8 + 2 * j] = av[j][0]; a[8 + 2 * j + 1] = av[j][1]; });
        float b8[8], b4[4];
        sfor<8>([&](auto j) { b8[j] = red_x32(a[j], a[8 + j]); });
        sfor<4>([&](auto j) { b4[j] = red_x16(b8[j], b8[4 + j]); });
        auto r = __builtin_amdgcn_permlane32_swap(__float_as_uint(red_x8(b4[0], b4[2], h0)), __float_as_uint(red_x8(b4[1], b4[3], h0)), false, false);
        const u32 uu = r[0], vv = r[1];
        if (uq < 112) pub16<0>(u32x4{uu, tag32, vv, tag32}, rW, ((lane & 7) * 8 + ((lane >> 3) & 3) * 2 + hh) * 16, WS_UVG + ((uq & 1) * 3584 + (ublk / 28) * F + (ublk % 28) * 64) * 16);
      }
  }
  FS(100);
  tie_v(tch);
  if (w == 2) ld_v1p<0>(cpf, lane * 128, rP, cof4 + (loc & 3) * 8192);
  if (act4) {
    sfor<NG0 - 3>([&](auto j) { set_m0n<(j + 3) * 16384>(wl); sfor<4>([&](auto i) { g_dma(ic<j + 3>{}, i); }); });
  }
  STAMP(6); FS(37);

  u32x4 aa[28][2];
  const u32 sAa = WS_ACT + ((tb * 4 + wk * 2) * 56 + w1 * 4) * 1024;
  const u32 vA1 = lane16 + w1 * (56 * 1024), vA2 = lane16 + (w1 ^ 1) * (56 * 1024);
  auto aa_ld = [&](auto sc_, auto rc) {
    constexpr int s = sc_, r = rc;
    ld_a<(s & 1) * 1024 + (s / 14) * 2048>(aa[s][r], r ? vA2 : vA1, rW, sAa + ((s >> 1) % 7) * 8192);
  };
  auto fl_wait = [&](u32 so) { if (w == 0) s_wait<14>(ws + so + (tb >> 1) * FG + ((tb * 8 + nt) & (NREP - 1)) * 128, tag); bar(); };

  if (act4) {
    u32 tB[2][2];
    {
      const u32 q = l15 >> 2, pp = lane & 3;
      sfor<2>([&](auto c) { sfor<2>([&](auto hf) {
        tB[c][hf] = (8 * hh + q) * 128 + ((4 * (c ^ (q >> 1)) + 2 * (qq & 1) + (pp >> 1)) << 4) + 8 * (pp & 1) + hf * 65536; tie_v(tB[c][hf]); }); });
    }
    FS(101);
    if (w == 0) s_wait<16>(ws + WS_F3 + rb * FG + (ft & (NREP - 1)) * 128, tag);
    bar();
    asm volatile(".Lmk_4:");
    STAMP(7); FS(38);
    u32x4 xa[32];
    const u32 so = WS_X1 + (rb * 4 + w) * 32 * 1024;
    auto xa_ld = [&](auto sc_) { constexpr int s = sc_; ld_a<(s & 3) * 1024>(xa[s], lane16, rW, so + (((s >> 2) + ROT4) & 7) * 4096); };
    sfor<P4_PRE>([&](auto s) { xa_ld(s); });
    u32x2 stv[4];
    sfor<4>([&](auto i) { ld_v2<i * 256>(stv[i], hh * 1024 + l31 * 8, rW, WS_ST + (rb * 4 + w) * 2048); });
    f32x16 z[2][4];
    u32x4 wf[2][4], uf[4];
    auto w_rd = [&](auto tc, auto jc) {
      constexpr int t = tc, j = jc;
      wf[t & 1][j] = tr_asm<((t >> 2) & 3) * 16384 + (j >> 1) * 8192 + (t & 3) * 2048>(tB[j & 1][(t >> 4) & 1]);
    };
    bar_vm<P4_PRE + 4, 200, 200 + NG0 - 1>();
    FS(103);
    sfor<4>([&](auto j) { w_rd(ic<0>{}, j); });
    f32x2 rs2;
    u32x4 xst;
    auto u_rd = [&](int p, int j) { uf[j] = lds16(sm, lM + (L_UV - L_MODV) + ((j >> 1) * 128 + p * 64 + (j & 1) * 32 + l31) * 16); };
    constexpr float C1 = -2.f * 0.7978845608028654f * 1.4426950408889634f, C3 = C1 * 0.044715f;
    f32x2 kc = {C3, C1};
    tie_v(kc);
    const u32 sAct = WS_ACT + ((rb * 8 + 2 * w) * 56 + ft * 4) * 1024;
    const u32 vAct = (l31 >> 4) * (56 * 1024) + hh * 256 + l15 * 16;
    u64x8 fa, fa2;
    const char* pFA = ws + WS_F4A + (tb >> 1) * FG + ((tb * 8 + nt) & (NREP - 1)) * 128;
    auto epi = [&](auto pc, auto qc) {
      constexpr int p = pc, c = qc >> 1, g0 = (qc & 1) * 2;
      auto grp = [&](const f32x16& v, int g) { return f32x4{v[4 * g], v[4 * g + 1], v[4 * g + 2], v[4 * g + 3]}; };
      pub16<5 + p, p * 2048 + c * 1024 + (qc & 1) * 512>(geglu8(grp(z[p][c], g0), grp(z[p][c], g0 + 1), grp(z[p][2 + c], g0), grp(z[p][2 + c], g0 + 1), rs2, rs2, kc), rW, vAct, sAct);
    };
    sfor<64>([&](auto tc) {
      constexpr int t = tc, j = t >> 2, s4 = t & 3, p = j >> 3;
      constexpr bool many = t < 4 * (8 - NG0) + 4;
      SCHED();
      if constexpr (t == 33) {
        mm_fence(z[0][3], z[0][2]); mm_tie(z[0][1], z[0][0]);
        sfor<4>([&](auto qc) { epi(ic<0>{}, qc); });
        SCHED();
      }
      if constexpr (p == 0) wait_a<S4.wait[t] - 1 - S4.posx[t]>(xa[t]);
      if constexpr (t < 4) FS(104 + t);
      sfor<4>([&](auto mc) {
        constexpr int m = mc;
        if constexpr (m == 0 && t < 4 * (8 - NG0)) set_m0<(NG0 + (t >> 2)) * 16384>(wl);
        if constexpr (m == 3 && t >= 3 && (many || s4 == 3)) set_m0<(((t - 3) >> 2) & 7) * 16384>(wl);
        if constexpr (m == 0) wait_l<4>();
        if constexpr (m == 2) wait_l<(t < 63 ? 4 : 2)>();
        mm_va<(t & 31) == 0>(z[p][m], wf[t & 1][m], xa[t & 31]);
        if constexpr (t == 32) mm_vv<false>(z[0][m], uf[m], xst);
        SCHED();
        if constexpr (m == 0 && s4 != 3) { w_rd(ic<t + 1>{}, ic<0>{}); w_rd(ic<t + 1>{}, ic<1>{}); }
        if constexpr (m == 2) { if constexpr (t < 63) { w_rd(ic<t + 1>{}, ic<2>{}); w_rd(ic<t + 1>{}, ic<3>{}); } else { u_rd(1, 2); u_rd(1, 3); } }
        if constexpr (t == 31 && m == 3) sfor<4>([&](auto q) { u_rd(0, q); });
        if constexpr (m == 1 && t == P4_ST) {
          wait_vm<S4.wait[t] - 1 - S4.posst + (t < 4 * (8 - NG0) ? 1 : 0)>();
          sfor<4>([&](auto i) { tie_v(stv[i]); });
          float ms = 0.f;
          sfor<4>([&](auto i) { u32 a = stv[i][0]; ms += __uint_as_float(a); });
          const float mean = add_x32(ms) * (1.f / 8);
          float q = 0.f;
          sfor<4>([&](auto i) { u32 a = stv[i][0], b = stv[i][1]; float d = __uint_as_float(a) - mean; q += __uint_as_float(b) + 64.f * d * d; });
          const float var = add_x32(q) * (1.f / D) + 1e-5f;
          const float rr = __builtin_amdgcn_rsqf(var), sd = var * rr;
          rs2 = f32x2{rr, sd};
          const float mh = b_lo(pack2(mean, 0.f)), sh = b_lo(pack2(sd, 0.f));
          xst[0] = hh ? 0u : pack2(-mh, -mh); xst[1] = hh ? 0u : pack2(mh - mean, mh - mean);
          xst[2] = hh ? 0u : pack2(sh, sh); xst[3] = hh ? 0u : pack2(sd - sh, sd - sh);
          tie_v(xst); tie_v(rs2);
        }
        if constexpr (m == 0 && t == P4_UR) {
          wait_vm<S4.wait[t] - 1 - S4.posug>();
          tie_v(ug[0]); tie_v(ug[1]);
          ensure([&] { return (ug[0][1] ^ tag32) | (ug[0][3] ^ tag32) | (ug[1][1] ^ tag32) | (ug[1][3] ^ tag32); },
                 [&] { sfor<2>([&](auto q) { ld_v4s(ug[q], vUV, rW, WS_UVG + q * 3584 * 16); }); wait_vm<0>(); tie_v(ug[0]); tie_v(ug[1]); });
          u32 d[2];
          sfor<2>([&](auto e) { u32 a = ug[0][2 * e], b = ug[1][2 * e];
            const float s = __uint_as_float(a) + __uint_as_float(b);
            const float hi = b_lo(pack2(s, 0.f));
            d[e] = pack2(hi, s - hi); });
          u32x4 rec = {d[0], d[0], d[1], d[1]};
          *(LAS u32x4*)(sm + lM + (L_UV - L_MODV) + tid * 16) = rec;
        }
        if constexpr (m == 0 && t < 4 * (8 - NG0)) g_dma(ic<NG0 + (t >> 2)>{}, ic<(t & 3)>{});
        if constexpr (m == 1 && s4 == 3) {
          constexpr int need = j + 1 >= NG0 && j < 15 ? S4.bar[j] - 1 - S4.posg[j + 1] : 63;
          constexpr int nsto = j == P4_FJ ? S4.bar[j] - 1 - S4.possto : 63;
          bar_vm<(need < nsto ? need : nsto), (j < 15 ? 201 + j : 0), (j == P4_FJ ? 5 : 0)>();
          if constexpr (j == P4_FJ) { if (tid < NREP) post_flag(ws + WS_F4A + rb * FG + tid * 128 + ft * 8, tag); }
          FS(40 + j);
          if constexpr (t < 63) { w_rd(ic<t + 1>{}, ic<0>{}); w_rd(ic<t + 1>{}, ic<1>{}); } else { u_rd(1, 0); u_rd(1, 1); }
        }
        if constexpr (m == 1) {
          if constexpr (t + P4_PRE < 32) xa_ld(ic<t + P4_PRE>{});
        }
        if constexpr (m == 1 && t == 61) {
          s_land(fa, fa2);
          if (w == 0) { if (!s_match<14>(fa, fa2, tag)) s_wait<14>(pFA, tag); }
        }
        if constexpr (m == 2 && t == 55) s_sample(fa, fa2, w == 0 ? pFA : ws + (bid * 4 + w) * 128);
        if constexpr (m == 2 && t >= P4_UG && t < P4_UG + 2) ld_v4<0>(ug[t - P4_UG], vUV, rW, WS_UVG + (t - P4_UG) * 3584 * 16);
        if constexpr (m == 3 && t >= 3) {
          constexpr int gj = (t - 3) >> 2, gi = (t - 3) & 3;
          if constexpr (gj < 8) g_dma(ic<8 + gj>{}, ic<gi>{}); else n_dma(ic<gj - 8>{}, ic<gi>{});
        }
        SCHED();
      });
    });
    sfor<4>([&](auto m) { mm_vv<false>(z[1][m], uf[m], xst); if constexpr (m < 3) n_dma(ic<7>{}, ic<1 + m>{}); });
    STAMP(8); FS(56);
    if constexpr (!EPI_FIRST) sfor<P5_PRE>([&](auto s) { aa_ld(s, ic<0>{}); aa_ld(s, ic<1>{}); });
    FS(102);
    mm_fence(z[1][0], z[1][2]); mm_tie(z[1][1], z[1][3]);
    sfor<4>([&](auto qc) {
      epi(ic<1>{}, qc);
      SCHED();
    });
    if constexpr (EPI_FIRST) sfor<P5_PRE>([&](auto s) { aa_ld(s, ic<0>{}); aa_ld(s, ic<1>{}); });
  } else {
    sfor<8>([&](auto j) { set_m0n<j * 16384>(wl); sfor<4>([&](auto i) { n_dma(j, i); }); });
    wait_vm<0>();
    fl_wait(WS_F4A);
    sfor<P5_PRE>([&](auto s) { aa_ld(s, ic<0>{}); aa_ld(s, ic<1>{}); });
  }
  STAMP(9); FS(57);

  {
    bar_vm<(EPI_FIRST ? 2 * P5_PRE : 0), 6, 307>();
    if (act4 && tid < NREP) post_flag(ws + WS_F4B + rb * FG + tid * 128 + ft * 8, tag);
    STAMP(10); FS(58);
    u32 tN[4][2];
    {
      const u32 q = l15 >> 2, pp = lane & 3, f = (q >> 1) + 2 * (qq & 1);
      sfor<4>([&](auto c) { sfor<2>([&](auto hf) {
        tN[c][hf] = (8 * qq + q) * 128 + ((2 * (c ^ f) + (pp >> 1)) << 4) + 8 * (pp & 1) + w1 * 8192 + hf * 65536; tie_v(tN[c][hf]); }); });
    }
    const rsrc_t rO = mk_rsrc(argp(10), S * D * 2);
    f32x4 gb[4];
    sfor<4>([&](auto c) { gb[c] = ldsf4(sm, lM + 768 * 4 + (c * 16 + qq * 4) * 4); tie_v(gb[c]); });
    u32 ex = L_EX + wl + lane16; tie_v(ex);
    f32x4 y[2][4];
    u64x8 fb, fb2;
    const char* pFB = ws + WS_F4B + (tb >> 1) * FG + ((tb * 8 + nt) & (NREP - 1)) * 128;
    static_assert((P5_CHK & 1) == 0, "");
    u32x4 nf[2][4];
    auto n_rd = [&](auto tc, auto cc) { constexpr int t = tc, c = cc; nf[t & 1][c] = tr_asm<((t >> 1) & 3) * 16384 + (t & 1) * 4096>(tN[c][(t >> 3) & 1]); };
    sfor<4>([&](auto c) { n_rd(ic<0>{}, c); });
    tie_v(cpf);
    sfor<28>([&](auto tc) {
      constexpr int t = tc, i = t >> 1, u = t & 1;
      SCHED();
      if constexpr (t == P5_CHK) {
        STAMP(11); FS(59);
      }
      sfor<8>([&](auto mc) {
        constexpr int m = mc, c = m >> 1, r = m & 1;
        if constexpr (m == 0) wait_a<S5.wait[t] - 1 - S5.posa[t][1]>(aa[t][0]);
        if constexpr (S5.op[t][m] >= 100 && (S5.op[t][m] & 3) == 0) set_m0<((S5.op[t][m] - 100) >> 2) * 16384>(wl);
        if constexpr (m == 0) wait_l<4>();
        if constexpr (m == 4) wait_l<(t < 27 ? 4 : 0)>();
        m16_va<t == 0>(y[r][c], nf[t & 1][c], aa[t][r]);
        SCHED();
        if constexpr (u == 0 && (m == 1 || m == 2)) n_rd(ic<t + 1>{}, ic<m - 1>{});
        if constexpr (m == 3 && t == P5_CHK - 3) s_sample(fb, fb2, w == 0 ? pFB : ws + (bid * 4 + w) * 128);
        if constexpr (m == 1 && t == P5_CHK - 1) {
          wait_l<0>();
          s_land(fb, fb2);
          if (w == 0) { if (!s_match<14>(fb, fb2, tag)) s_wait<14>(pFB, tag); }
        }
        if constexpr (m == 2 && u == 1 && i < 13) { bar_vm<(i >= 7 ? S5.bar[i] - 1 - S5.posn[i + 1] : 63), (i >= 7 ? 301 + i : 0)>(); FS(60 + i); n_rd(ic<t + 1>{}, ic<0>{}); n_rd(ic<t + 1>{}, ic<1>{}); }
        if constexpr ((m == 5 || m == 6) && t < 27) n_rd(ic<t + 1>{}, ic<m - 3>{});
        if constexpr (S5.op[t][m] >= 100) n_dma(ic<8 + ((S5.op[t][m] - 100) >> 2)>{}, ic<(S5.op[t][m] & 3)>{});
        else if constexpr (S5.op[t][m] > 0) aa_ld(ic<((S5.op[t][m] - 1) >> 1)>{}, ic<((S5.op[t][m] - 1) & 1)>{});
        SCHED();
      });
    });
    STAMP(12); FS(73);
    m16_fence(y[1]); m16_tie(y[0]);
    sfor<4>([&](auto c) { *(LAS f32x4*)(sm + ex + c * 1024) = y[1][c]; });
    sfor<4>([&](auto c) { x1[c] += gb[c] * y[0][c]; tie_v(x1[c]); });
    bar();
    u32x4 o[4];
    sfor<4>([&](auto c) { o[c] = rd_asm<c * 1024>(ex ^ 4096); });
    asm volatile("s_waitcnt lgkmcnt(0)" : "+v"(o[0]), "+v"(o[1]), "+v"(o[2]), "+v"(o[3]));
    u32 pk[4][2];
    sfor<4>([&](auto c) {
      f32x4 m = x1[c] + gb[c] * __builtin_bit_cast(f32x4, o[c]);
      pk[c][0] = pack2(m[0], m[1]); pk[c][1] = pack2(m[2], m[3]);
    });
    sfor<2>([&](auto p) {
      __builtin_amdgcn_raw_buffer_store_b128(chunk_swap16(pk[2 * p][0], pk[2 * p][1], pk[2 * p + 1][0], pk[2 * p + 1][1]), rO,
                                             l15 * 1024 + (qq & 1) * 32 + (qq >> 1) * 16 + p * 64, (tb * 64 + w * 16) * 1024 + nt * 128, 0);
    });
  }
  STAMP(13); FS(74);
  asm volatile(".Lmk_z:");
}

extern "C" void kernel_launch(void* const* d_in, const int* in_sizes, int n_in, void* d_out, int out_size, void* d_ws,
                              size_t ws_size, hipStream_t stream) {
  (void)in_sizes; (void)n_in; (void)out_size; (void)ws_size;
  (void)hipFuncSetAttribute((const void*)mk_block, hipFuncAttributeMaxDynamicSharedMemorySize, LDS_BYTES);
  mk_block<<<256, 256, LDS_BYTES, stream>>>((const u16*)d_in[0], (const u16*)d_in[1], (const u16*)d_in[2], (const u16*)d_in[3],
                                            (const u16*)d_in[4], (const u16*)d_in[6], (const u16*)d_in[7], (const u16*)d_in[8],
                                            (const u16*)d_in[9], (const u16*)d_in[10], (u16*)d_out, (char*)d_ws);
}
```
